```python
import torch

def _softmax(x, axis=-1):
    return torch.softmax(x, dim=axis)
def _gelu(x):
    return torch.nn.functional.gelu(x, approximate="tanh")
def _silu(x):
    return torch.nn.functional.silu(x)
def _sigmoid(x):
    return torch.sigmoid(x)
def _layernorm(x, axis=-1, eps=1e-5):
    m = x.mean(dim=axis, keepdim=True)
    v = x.var(dim=axis, keepdim=True, unbiased=False)
    return (x - m) / torch.sqrt(v + eps)
def _scatter_add(tgt, idx, src, axis=0):
    return tgt.clone().index_add_(axis, idx, src)
def _segment_sum(src, seg, n):
    out = torch.zeros((n,) + src.shape[1:], dtype=src.dtype)
    return out.index_add_(0, seg, src)
def _pad(x, pw):
    flat = tuple(v for pair in reversed(pw) for v in pair)
    return torch.nn.functional.pad(x, flat)

kernel_name = 'sequential_preLN__softmax_full_mha__dense_activation_relu__prompt'

def setup_inputs():

    _w = (lambda a: (lambda t: t.to(torch.bfloat16) if t.is_floating_point() else t)(torch.as_tensor(a)))
    def _A_setup():
        B, S, D = 1, 2048, 512
        return {
            "x": _w((lambda *s: torch.randn(*s, dtype=torch.bfloat16) * 0.05)(B, S, D)),
            "wqkv": _w((lambda *s: torch.randn(*s, dtype=torch.bfloat16) * 0.05)(D, 3 * D)),
            "wo": _w((lambda *s: torch.randn(*s, dtype=torch.bfloat16) * 0.05)(D, D)),
        }
    _A_in = _A_setup()

    def _B_setup():
        B, S, D, Dff = 1, 2048, 512, 1792
        return {
            "x": _w((lambda *s: torch.randn(*s, dtype=torch.bfloat16) * 0.05)(B, S, D)),
            "w1": _w((lambda *s: torch.randn(*s, dtype=torch.bfloat16) * 0.05)(D, Dff)),
            "w2": _w((lambda *s: torch.randn(*s, dtype=torch.bfloat16) * 0.05)(Dff, D)),
        }
    _B_in = _B_setup()

    return {
        "A_x": _A_in["x"],
        "A_wqkv": _A_in["wqkv"],
        "A_wo": _A_in["wo"],
        "B_x": _B_in["x"],
        "B_w1": _B_in["w1"],
        "B_w2": _B_in["w2"],
    }

def reference(A_x, A_wqkv, A_wo, B_x, B_w1, B_w2):
    def _A_ref(x, wqkv, wo):
        B, S, D = x.shape
        H = 32; Dh = D // H
        qkv = (lambda x, *d: x.permute(*d))((x @ wqkv).reshape(B, S, 3, H, Dh), 2, 0, 3, 1, 4)
        q, k, v = qkv[0], qkv[1], qkv[2]
        scale = Dh ** -0.5
        scores = torch.einsum("bhqd,bhkd->bhqk", q, k) * scale
        p = _softmax(scores, axis=-1)
        o = torch.einsum("bhqk,bhkd->bhqd", p, v)
        return (lambda x, *d: x.permute(*d))(o, 0, 2, 1, 3).reshape(B, S, D) @ wo

    def _B_ref(x, w1, w2):
        h = torch.einsum("bsd,df->bsf", x, w1)
        zero = torch.zeros_like(h)
        h = torch.maximum(h, zero)
        return torch.einsum("bsf,fd->bsd", h, w2)

    x = A_x
    h = x + _A_ref(_layernorm(x, axis=-1), A_wqkv, A_wo)
    h2 = h + _B_ref(_layernorm(h, axis=-1), B_w1, B_w2)
    return h2
```

```cpp
#include <hip/hip_runtime.h>
#include <stdint.h>

typedef __bf16 bf16x8 __attribute__((ext_vector_type(8)));
typedef __bf16 bf16x2 __attribute__((ext_vector_type(2)));
typedef short s16x4 __attribute__((ext_vector_type(4)));
typedef float f32x16 __attribute__((ext_vector_type(16)));
typedef float f32x4 __attribute__((ext_vector_type(4)));
typedef unsigned u32x4 __attribute__((ext_vector_type(4)));
typedef unsigned u32x2 __attribute__((ext_vector_type(2)));
#define LAS __attribute__((address_space(3)))
#define DEV __device__ __forceinline__

constexpr int S_ = 2048, D_ = 512, DFF = 1792;
constexpr size_t WS_Q = 0, WS_K = 2u << 20, WS_V = 4u << 20, WS_O = 6u << 20, WS_H = 8u << 20, WS_A = 10u << 20;
constexpr size_t WS_WO = 17u << 20, WS_W1 = WS_WO + (512u << 10), WS_W2 = WS_W1 + (1792u << 10), WS_CS = WS_W2 + (1792u << 10);
constexpr float QSCALE = 0.25f * 1.4426950408889634f;
constexpr float LN_EPS = 1e-5f;

DEV f32x16 mfma(u32x4 a, u32x4 b, f32x16 c) {
    return __builtin_amdgcn_mfma_f32_32x32x16_bf16(__builtin_bit_cast(bf16x8, a), __builtin_bit_cast(bf16x8, b), c, 0, 0, 0);
}
DEV unsigned cvtpk(float lo, float hi) {
    bf16x2 r; r[0] = (__bf16)lo; r[1] = (__bf16)hi; return __builtin_bit_cast(unsigned, r);
}
DEV float dot2(unsigned a, unsigned b, float c) {
    return __builtin_amdgcn_fdot2_f32_bf16(__builtin_bit_cast(bf16x2, a), __builtin_bit_cast(bf16x2, b), c, false);
}
DEV float bflo(unsigned v) { return __uint_as_float(v << 16); }
DEV float bfhi(unsigned v) { return __uint_as_float(v & 0xffff0000u); }
DEV float xhalf_add(float v) {
    auto r = __builtin_amdgcn_permlane32_swap(__float_as_uint(v), __float_as_uint(v), false, false);
    return __uint_as_float(r[0]) + __uint_as_float(r[1]);
}
DEV float xhalf_max(float v) {
    auto r = __builtin_amdgcn_permlane32_swap(__float_as_uint(v), __float_as_uint(v), false, false);
    return fmaxf(__uint_as_float(r[0]), __uint_as_float(r[1]));
}
DEV void glds16(const void* sbase, unsigned voff, unsigned ldsaddr) {
    asm volatile("s_mov_b32 m0, %2\n\ts_nop 0\n\tglobal_load_lds_dwordx4 %0, %1" ::"v"(voff), "s"(sbase), "s"(ldsaddr) : "memory");
}
DEV u32x4 lds128(LAS const char* p) { return *(LAS const u32x4*)p; }
DEV u32x2 ldstr(LAS const char* p) {
    return __builtin_bit_cast(u32x2, __builtin_amdgcn_ds_read_tr16_b64_v4i16((LAS s16x4*)p));
}
DEV u32x4 cat(u32x2 a, u32x2 b) { return (u32x4){a[0], a[1], b[0], b[1]}; }
#define WAIT_BAR(N) asm volatile("s_waitcnt vmcnt(" #N ") lgkmcnt(0)\n\ts_barrier" ::: "memory")
constexpr unsigned ONES2 = 0x3f803f80u;

constexpr int K1_SLOT = 28672, K1_CS = 5 * K1_SLOT;
__global__ __launch_bounds__(256, 1) void k_qkv(const char* __restrict__ x, const char* __restrict__ w, char* __restrict__ ws) {
    __shared__ __attribute__((aligned(16))) char smem[K1_CS + 512];
    LAS char* lds = (LAS char*)smem;
    const unsigned lds0 = (unsigned)(uintptr_t)lds;
    const int lane = threadIdx.x & 63, wv = __builtin_amdgcn_readfirstlane(threadIdx.x >> 6);
    const int r32 = lane & 31, hh = lane >> 5;
    const int b = blockIdx.x, xcd = b & 7, r = b >> 3;
    const int rt = (xcd >> 1) * 4 + (r >> 3), jt = (xcd & 1) * 8 + (r & 7);
    const int c0 = ((r & 7) + 2 * (r >> 3)) & 7;
    unsigned xo[4], wo[3];
#pragma unroll
    for (int q = 0; q < 4; ++q) {
        const int row = 8 * q + (lane >> 3);
        xo[q] = (rt * 128 + wv * 32 + row) * 1024 + (((lane & 7) ^ ((row >> 1) & 7)) << 4);
    }
#pragma unroll
    for (int q = 0; q < 3; ++q) {
        const int pos = (3 * wv + q) * 64 + lane;
        const int row = pos / 12, cb = pos % 12;
        wo[q] = row * 3072 + jt * 192 + cb * 16;
    }
    auto stage = [&](int c, int slot) {
        const int cc = (c + c0) & 7;
        const char* xs = x + cc * 128;
        const char* wsrc = w + cc * (64 * 3072);
        const unsigned base = lds0 + slot * K1_SLOT;
#pragma unroll
        for (int q = 0; q < 4; ++q) glds16(xs, xo[q], base + wv * 4096 + q * 1024);
#pragma unroll
        for (int q = 0; q < 3; ++q) glds16(wsrc, wo[q], base + 16384 + (3 * wv + q) * 1024);
    };
#pragma unroll
    for (int c = 0; c < 5; ++c) stage(c, c);

    const int xa = wv * 4096 + r32 * 128 + ((hh ^ ((r32 >> 1) & 7)) << 4);
    int tt[3];
    tt[0] = wv % 3; tt[1] = (wv + 1) % 3; tt[2] = (wv + 2) % 3;
    const int wa = 16384 + (8 * hh + ((lane & 15) >> 2)) * 192 + ((lane >> 4) & 1) * 32 + (lane & 3) * 8;
    f32x16 acc[3];
#pragma unroll
    for (int t = 0; t < 3; ++t) acc[t] = f32x16{};
    float sum = 0.f, sq = 0.f, cs = 0.f;
#pragma unroll
    for (int c = 0; c < 8; ++c) {
        if (c == 0) WAIT_BAR(28);
        else if (c <= 4) WAIT_BAR(21);
        else if (c == 5) WAIT_BAR(14);
        else if (c == 6) WAIT_BAR(7);
        else WAIT_BAR(0);
        if (c >= 1 && c <= 3) stage(c + 4, c - 1);
        LAS const char* sl = lds + (c % 5) * K1_SLOT;
#pragma unroll
        for (int s = 0; s < 4; ++s) {
            const u32x4 xf = lds128(sl + (xa ^ (s << 5)));
#pragma unroll
            for (int t = 0; t < 3; ++t) {
                LAS const char* p = sl + wa + tt[t] * 64 + s * (16 * 192);
                const u32x4 wf = cat(ldstr(p), ldstr(p + 4 * 192));
                acc[t] = mfma(wf, xf, acc[t]);
                if (t == 0) {
#pragma unroll
                    for (int i = 0; i < 4; ++i) cs = dot2(wf[i], ONES2, cs);
                }
            }
#pragma unroll
            for (int i = 0; i < 4; ++i) { sum = dot2(xf[i], ONES2, sum); sq = dot2(xf[i], xf[i], sq); }
        }
    }
    cs = xhalf_add(cs); sum = xhalf_add(sum); sq = xhalf_add(sq);
    *(LAS float*)(lds + K1_CS + (tt[0] * 32 + r32) * 4) = cs;
    const float mean = sum * (1.f / 512.f);
    const float var = fmaxf(sq * (1.f / 512.f) - mean * mean, 0.f);
    const float rstd = __builtin_amdgcn_rsqf(var + LN_EPS);
    WAIT_BAR(0);
    const int tile = rt * 4 + wv;
#pragma unroll
    for (int t = 0; t < 3; ++t) {
        const int col0 = jt * 96 + tt[t] * 32;
        const int part = col0 >> 9;
        const float sc = part == 0 ? rstd * QSCALE : rstd;
        const float ms = -mean * sc;
        char* dst = ws + (size_t)(col0 >> 4) * 65536 + tile * 1024 + (part == 2 ? r32 * 32 + hh * 16 : lane * 16);
#pragma unroll
        for (int e = 0; e < 2; ++e) {
            u32x4 o;
#pragma unroll
            for (int g = 0; g < 2; ++g) {
                const f32x4 c4 = *(LAS const f32x4*)(lds + K1_CS + (tt[t] * 32 + 16 * e + 8 * g + 4 * hh) * 4);
                float v[4];
#pragma unroll
                for (int i = 0; i < 4; ++i) v[i] = acc[t][8 * e + 4 * g + i] * sc + ms * c4[i];
                o[2 * g] = cvtpk(v[0], v[1]); o[2 * g + 1] = cvtpk(v[2], v[3]);
            }
            *(u32x4*)(dst + e * 65536) = o;
        }
    }
}

constexpr int A_K = 0, A_V = 65536, A_ONE = 131072, A_W = A_ONE + 4096 + 256, A_END = A_W + 16384;
__global__ __launch_bounds__(512, 2) void k_attn(const char* __restrict__ wo, const char* __restrict__ w1, const char* __restrict__ w2,
                                                 char* __restrict__ ws) {
    __shared__ __attribute__((aligned(16))) char smem[A_END];
    LAS char* lds = (LAS char*)smem;
    const unsigned lds0 = (unsigned)(uintptr_t)lds;
    const int tid = threadIdx.x, lane = tid & 63, wv = __builtin_amdgcn_readfirstlane(tid >> 6);
    const int r32 = lane & 31, hh = lane >> 5;
    const int b = blockIdx.x, xcd = b & 7, r = b >> 3;
    const int head = xcd * 4 + (r & 3), g = r >> 2;
    const char* kg = ws + WS_K + (size_t)head * 65536;
    const char* vg = ws + WS_V + (size_t)head * 65536;
    const u32x4 qf = *(const u32x4*)(ws + WS_Q + (size_t)head * 65536 + (g * 8 + wv) * 1024 + lane * 16);
    asm volatile("" ::: "memory");
    const unsigned lo = wv * 1024 + lane * 16;
#pragma unroll
    for (int c = 0; c < 8; ++c) {
        const int cc = (c + g) & 7;
        glds16(kg + cc * 8192, lo, lds0 + A_K + c * 8192 + wv * 1024);
        glds16(vg + cc * 8192, lo, lds0 + A_V + c * 8192 + wv * 1024);
    }
    const char* wsrc; int ldw, kb, nb, nks; char* wdst; bool permk, docs;
    if (b < 32) { wsrc = wo; ldw = 1024; kb = b >> 2; nb = b & 3; nks = 32; wdst = ws + WS_WO; permk = false; docs = false; }
    else if (b < 144) { const int u = b - 32; wsrc = w1; ldw = 3584; kb = u / 14; nb = u % 14; nks = 32; wdst = ws + WS_W1; permk = true; docs = true; }
    else { const int u = b - 144; wsrc = w2; ldw = 1024; kb = u >> 2; nb = u & 3; nks = 112; wdst = ws + WS_W2; permk = true; docs = false; }
#pragma unroll
    for (int q = 0; q < 2; ++q) {
        const int row = (wv * 2 + q) * 4 + (lane >> 4);
        glds16(wsrc + (size_t)kb * 64 * ldw + nb * 256, row * ldw + (lane & 15) * 16, lds0 + A_W + (wv * 2 + q) * 1024);
    }
    if (tid < 272) *(LAS u32x4*)(lds + A_ONE + tid * 16) = (u32x4){ONES2, ONES2, ONES2, ONES2};

    LAS const char* kp = lds + A_K + lane * 16;
    const bool isd = (lane & 16) == 0;
    LAS const char* vp = lds + (isd ? A_V + hh * 128 : A_ONE + (hh ^ 1) * 128) + ((lane & 15) >> 2) * 32 + (lane & 3) * 8;
    const int vinc = isd ? 4096 : 0;

    WAIT_BAR(16);
    f32x16 negm;
    {
        const f32x16 s0 = mfma(lds128(kp), qf, f32x16{});
        float m = s0[0];
#pragma unroll
        for (int i = 1; i < 16; ++i) m = fmaxf(m, s0[i]);
        m = xhalf_max(m);
#pragma unroll
        for (int i = 0; i < 16; ++i) negm[i] = -m;
        asm volatile("" : "+v"(negm));
    }
    f32x16 o = f32x16{};
    f32x16 s = mfma(lds128(kp), qf, negm);
#pragma unroll 1
    for (int it = 0; it < 16; ++it) {
        if (it == 1) {
            WAIT_BAR(0);
            float cs = 0.f;
#pragma unroll
            for (int q = 0; q < 2; ++q) {
                const int ks = 2 * (wv & 1) + q;
                const int row0 = 16 * ks + (permk ? 4 * hh : 8 * hh) + ((lane & 15) >> 2);
                LAS const char* p = lds + A_W + row0 * 256 + (wv >> 1) * 64 + ((lane >> 4) & 1) * 32 + (lane & 3) * 8;
                const u32x4 f = cat(ldstr(p), ldstr(p + (permk ? 8 : 4) * 256));
                *(u32x4*)(wdst + (size_t)((nb * 4 + (wv >> 1)) * nks + kb * 4 + ks) * 1024 + lane * 16) = f;
#pragma unroll
                for (int i = 0; i < 4; ++i) cs = dot2(f[i], ONES2, cs);
            }
            cs = xhalf_add(cs);
            if (docs && lane < 32) *(float*)(ws + WS_CS + ((kb * 2 + (wv & 1)) * DFF + nb * 128 + (wv >> 1) * 32 + r32) * 4) = cs;
        }
#pragma unroll
        for (int t = 0; t < 4; ++t) {
            const f32x16 sn = mfma(lds128(kp + (t + 1) * 1024), qf, negm);
            const u32x4 v0 = cat(ldstr(vp + t * 1024), ldstr(vp + t * 1024 + 256));
            const u32x4 v1 = cat(ldstr(vp + t * 1024 + 512), ldstr(vp + t * 1024 + 768));
            u32x4 p0, p1;
#pragma unroll
            for (int i = 0; i < 4; ++i) p0[i] = cvtpk(__builtin_amdgcn_exp2f(s[2 * i]), __builtin_amdgcn_exp2f(s[2 * i + 1]));
            o = mfma(v0, p0, o);
#pragma unroll
            for (int i = 0; i < 4; ++i) p1[i] = cvtpk(__builtin_amdgcn_exp2f(s[8 + 2 * i]), __builtin_amdgcn_exp2f(s[8 + 2 * i + 1]));
            o = mfma(v1, p1, o);
            s = sn;
        }
        kp += 4096; vp += vinc;
    }
    bool bad = false;
#pragma unroll
    for (int i = 0; i < 9; ++i) bad |= !(fabsf(o[i]) < 1e37f);
    if (__any(bad)) {
        kp -= 65536; vp -= 16 * vinc;
        float m = -INFINITY;
        o = f32x16{};
#pragma unroll 1
        for (int t = 0; t < 64; ++t) {
            const f32x16 sc = mfma(lds128(kp), qf, f32x16{});
            float tm = sc[0];
#pragma unroll
            for (int i = 1; i < 16; ++i) tm = fmaxf(tm, sc[i]);
            const float mn = fmaxf(m, xhalf_max(tm));
            const float al = __builtin_amdgcn_exp2f(m - mn);
            m = mn;
            const u32x4 v0 = cat(ldstr(vp), ldstr(vp + 256));
            const u32x4 v1 = cat(ldstr(vp + 512), ldstr(vp + 768));
            u32x4 p0, p1;
#pragma unroll
            for (int i = 0; i < 4; ++i) {
                p0[i] = cvtpk(__builtin_amdgcn_exp2f(sc[2 * i] - mn), __builtin_amdgcn_exp2f(sc[2 * i + 1] - mn));
                p1[i] = cvtpk(__builtin_amdgcn_exp2f(sc[8 + 2 * i] - mn), __builtin_amdgcn_exp2f(sc[8 + 2 * i + 1] - mn));
            }
#pragma unroll
            for (int i = 0; i < 16; ++i) o[i] *= al;
            o = mfma(v0, p0, o);
            o = mfma(v1, p1, o);
            kp += 1024; vp += vinc >> 2;
        }
    }
    const float rl = 1.0f / o[8];
    u32x4 of;
#pragma unroll
    for (int i = 0; i < 4; ++i) of[i] = cvtpk(o[2 * i] * rl, o[2 * i + 1] * rl);
    *(u32x4*)(ws + WS_O + (size_t)((g * 8 + wv) * 32 + head) * 1024 + lane * 16) = of;
}

template <int MODE>
__global__ __launch_bounds__(256, 1) void k_proj(const char* __restrict__ act, const char* __restrict__ wt, const char* __restrict__ res,
                                                 char* __restrict__ out) {
    constexpr int NKS = MODE == 0 ? 32 : 112, PER = NKS / 4, DEPTH = MODE == 0 ? 8 : 7, NIT = PER / DEPTH;
    __shared__ __attribute__((aligned(16))) char smem[65536];
    LAS char* lds = (LAS char*)smem;
    const int lane = threadIdx.x & 63, wv = __builtin_amdgcn_readfirstlane(threadIdx.x >> 6);
    const int r32 = lane & 31, hh = lane >> 5;
    const int b = blockIdx.x, xcd = b & 7, r = b >> 3;
    const int rt = xcd * 4 + (r >> 3), ct = r & 7;
    const u32x4* pa = (const u32x4*)(act + ((size_t)rt * 2 * NKS + wv * PER) * 1024) + lane;
    const u32x4* pw = (const u32x4*)(wt + ((size_t)ct * 2 * NKS + wv * PER) * 1024) + lane;
    u32x4 fa[DEPTH][2], fw[DEPTH][2];
#pragma unroll
    for (int d = 0; d < DEPTH; ++d) {
        fa[d][0] = pa[d * 64]; fa[d][1] = pa[(NKS + d) * 64];
        fw[d][0] = pw[d * 64]; fw[d][1] = pw[(NKS + d) * 64];
    }
    const int tt = rt * 2 + (wv >> 1), nt = ct * 2 + (wv & 1);
    u32x2 rx[4];
    if (MODE == 0) {
#pragma unroll
        for (int g = 0; g < 4; ++g) rx[g] = *(const u32x2*)(res + (size_t)(tt * 32 + r32) * 1024 + (nt * 32 + 8 * g + 4 * hh) * 2);
    } else {
#pragma unroll
        for (int e = 0; e < 2; ++e) {
            const u32x4 v = *(const u32x4*)(res + (size_t)(tt * 32 + nt * 2 + e) * 1024 + lane * 16);
            rx[2 * e] = (u32x2){v[0], v[1]}; rx[2 * e + 1] = (u32x2){v[2], v[3]};
        }
    }
    f32x16 acc[2][2];
    acc[0][0] = f32x16{}; acc[0][1] = f32x16{}; acc[1][0] = f32x16{}; acc[1][1] = f32x16{};
#pragma unroll 1
    for (int it = 0; it < NIT - 1; ++it) {
        pa += DEPTH * 64; pw += DEPTH * 64;
#pragma unroll
        for (int d = 0; d < DEPTH; ++d) {
            acc[0][0] = mfma(fw[d][0], fa[d][0], acc[0][0]); acc[0][1] = mfma(fw[d][1], fa[d][0], acc[0][1]);
            acc[1][0] = mfma(fw[d][0], fa[d][1], acc[1][0]); acc[1][1] = mfma(fw[d][1], fa[d][1], acc[1][1]);
            fa[d][0] = pa[d * 64]; fa[d][1] = pa[(NKS + d) * 64];
            fw[d][0] = pw[d * 64]; fw[d][1] = pw[(NKS + d) * 64];
            __builtin_amdgcn_sched_barrier(0);
        }
    }
#pragma unroll
    for (int d = 0; d < DEPTH; ++d) {
        acc[0][0] = mfma(fw[d][0], fa[d][0], acc[0][0]); acc[0][1] = mfma(fw[d][1], fa[d][0], acc[0][1]);
        acc[1][0] = mfma(fw[d][0], fa[d][1], acc[1][0]); acc[1][1] = mfma(fw[d][1], fa[d][1], acc[1][1]);
    }
#pragma unroll
    for (int i = 0; i < 4; ++i) {
        const f32x16& a = acc[i >> 1][i & 1];
#pragma unroll
        for (int g = 0; g < 4; ++g)
            *(LAS f32x4*)(lds + (i * 4 + wv) * 4096 + g * 1024 + lane * 16) = (f32x4){a[4 * g], a[4 * g + 1], a[4 * g + 2], a[4 * g + 3]};
    }
    __syncthreads();
    unsigned pk[8];
#pragma unroll
    for (int g = 0; g < 4; ++g) {
        f32x4 v = *(LAS const f32x4*)(lds + (wv * 4) * 4096 + g * 1024 + lane * 16);
#pragma unroll
        for (int sw = 1; sw < 4; ++sw) v += *(LAS const f32x4*)(lds + (wv * 4 + sw) * 4096 + g * 1024 + lane * 16);
        pk[2 * g] = cvtpk(v[0] + bflo(rx[g][0]), v[1] + bfhi(rx[g][0]));
        pk[2 * g + 1] = cvtpk(v[2] + bflo(rx[g][1]), v[3] + bfhi(rx[g][1]));
    }
    if (MODE == 0) {
#pragma unroll
        for (int e = 0; e < 2; ++e)
            *(u32x4*)(out + (size_t)(tt * 32 + nt * 2 + e) * 1024 + lane * 16) = (u32x4){pk[4 * e], pk[4 * e + 1], pk[4 * e + 2], pk[4 * e + 3]};
    } else {
#pragma unroll
        for (int e = 0; e < 2; ++e) {
            auto s0 = __builtin_amdgcn_permlane32_swap(pk[4 * e], pk[4 * e + 2], false, false);
            auto s1 = __builtin_amdgcn_permlane32_swap(pk[4 * e + 1], pk[4 * e + 3], false, false);
            *(u32x4*)(out + (size_t)(tt * 32 + r32) * 1024 + (nt * 32 + 16 * e + 8 * hh) * 2) = (u32x4){s0[0], s1[0], s0[1], s1[1]};
        }
    }
}

#define GLOAD16(dst, voff, sbase, imm) asm volatile("global_load_dwordx4 %0, %1, %2 offset:%3" : "=v"(dst) : "v"(voff), "s"(sbase), "i"(imm))
__global__ __launch_bounds__(256, 1) void k_ffn1(char* __restrict__ ws) {
    __shared__ __attribute__((aligned(16))) char smem[131072 + 1024];
    LAS char* lds = (LAS char*)smem;
    const unsigned lds0 = (unsigned)(uintptr_t)lds;
    const int tid = threadIdx.x, lane = tid & 63, wv = __builtin_amdgcn_readfirstlane(tid >> 6);
    const int hh = lane >> 5;
    const int b = blockIdx.x, xcd = b & 7, r = b >> 3;
    const int rt = xcd * 2 + (r / 14), ct = r % 14;
    const int tile = rt * 4 + wv;
    float csp[8];
    {
        const char* cp = ws + WS_CS + ((tid >> 7) * 8 * DFF + ct * 128 + (tid & 127)) * 4;
#pragma unroll
        for (int i = 0; i < 8; ++i) asm volatile("global_load_dword %0, %1, off" : "=v"(csp[i]) : "v"(cp + i * DFF * 4));
    }
    const char* hb = ws + WS_H + (size_t)tile * 32768;
    const char* wb = ws + WS_W1 + (size_t)(ct * 4 + wv) * 32768;
    const unsigned lo = lane * 16;
    u32x4 hf[32];
#pragma unroll
    for (int c = 0; c < 8; ++c) {
#pragma unroll
        for (int s = 0; s < 4; ++s) GLOAD16(hf[4 * c + s], lo, hb + c * 4096, s * 1024);
#pragma unroll
        for (int s = 0; s < 4; ++s) glds16(wb + (4 * c + s) * 1024, lo, lds0 + ((4 * c + s) * 4 + wv) * 1024);
        if (c == 5) {
            asm volatile("s_waitcnt vmcnt(48)" : "+v"(csp[0]), "+v"(csp[1]), "+v"(csp[2]), "+v"(csp[3]), "+v"(csp[4]), "+v"(csp[5]), "+v"(csp[6]), "+v"(csp[7]));
            const float t = ((csp[0] + csp[1]) + (csp[2] + csp[3])) + ((csp[4] + csp[5]) + (csp[6] + csp[7]));
            *(LAS float*)(lds + 131072 + tid * 4) = t;
        }
        if (c == 6) asm volatile("s_waitcnt vmcnt(55)" ::: "memory");
    }
    f32x16 acc[4];
#pragma unroll
    for (int t = 0; t < 4; ++t) acc[t] = f32x16{};
    float sum = 0.f, sq = 0.f;
#define FFN1_WAIT(N, c) asm volatile("s_waitcnt vmcnt(" #N ") lgkmcnt(0)\n\ts_barrier" : "+v"(hf[4 * c]), "+v"(hf[4 * c + 1]), "+v"(hf[4 * c + 2]), "+v"(hf[4 * c + 3])::"memory")
#pragma unroll
    for (int c = 0; c < 8; ++c) {
        if (c == 0) FFN1_WAIT(56, 0);
        if (c == 1) FFN1_WAIT(48, 1);
        if (c == 2) FFN1_WAIT(40, 2);
        if (c == 3) FFN1_WAIT(32, 3);
        if (c == 4) FFN1_WAIT(24, 4);
        if (c == 5) FFN1_WAIT(16, 5);
        if (c == 6) FFN1_WAIT(8, 6);
        if (c == 7) FFN1_WAIT(0, 7);
#pragma unroll
        for (int s = 0; s < 4; ++s) {
            const u32x4 xf = hf[4 * c + s];
#pragma unroll
            for (int t = 0; t < 4; ++t) acc[t] = mfma(lds128(lds + ((4 * c + s) * 4 + t) * 1024 + lo), xf, acc[t]);
#pragma unroll
            for (int i = 0; i < 4; ++i) { sum = dot2(xf[i], ONES2, sum); sq = dot2(xf[i], xf[i], sq); }
        }
    }
    sum = xhalf_add(sum); sq = xhalf_add(sq);
    const float mean = sum * (1.f / 512.f);
    const float var = fmaxf(sq * (1.f / 512.f) - mean * mean, 0.f);
    const float rstd = __builtin_amdgcn_rsqf(var + LN_EPS);
    const float ms = -mean * rstd;
    char* dst = ws + WS_A + ((size_t)tile * 112 + ct * 8) * 1024 + lo;
#pragma unroll
    for (int t = 0; t < 4; ++t) {
#pragma unroll
        for (int e = 0; e < 2; ++e) {
            u32x4 o;
#pragma unroll
            for (int g = 0; g < 2; ++g) {
                LAS const char* cp = lds + 131072 + (t * 32 + 16 * e + 8 * g + 4 * hh) * 4;
                const f32x4 c4 = *(LAS const f32x4*)cp + *(LAS const f32x4*)(cp + 512);
                float v[4];
#pragma unroll
                for (int i = 0; i < 4; ++i) v[i] = fmaxf(acc[t][8 * e + 4 * g + i] * rstd + ms * c4[i], 0.f);
                o[2 * g] = cvtpk(v[0], v[1]); o[2 * g + 1] = cvtpk(v[2], v[3]);
            }
            *(u32x4*)(dst + (t * 2 + e) * 1024) = o;
        }
    }
}

extern "C" void kernel_launch(void* const* d_in, const int* in_sizes, int n_in, void* d_out, int out_size, void* d_ws, size_t ws_size,
                              hipStream_t stream) {
    const char* x = (const char*)d_in[0];
    const char* wqkv = (const char*)d_in[1];
    const char* wo = (const char*)d_in[2];
    const char* w1 = (const char*)d_in[4];
    const char* w2 = (const char*)d_in[5];
    char* ws = (char*)d_ws;
    k_qkv<<<256, 256, 0, stream>>>(x, wqkv, ws);
    k_attn<<<256, 512, 0, stream>>>(wo, w1, w2, ws);
    k_proj<0><<<256, 256, 0, stream>>>(ws + WS_O, ws + WS_WO, x, ws + WS_H);
    k_ffn1<<<224, 256, 0, stream>>>(ws);
    k_proj<1><<<256, 256, 0, stream>>>(ws + WS_A, ws + WS_W2, ws + WS_H, (char*)d_out);
}
```

```cpp
#include <hip/hip_runtime.h>
#include <stdint.h>

typedef __bf16 bf16x8 __attribute__((ext_vector_type(8)));
typedef __bf16 bf16x16 __attribute__((ext_vector_type(16)));
typedef __bf16 bf16x2 __attribute__((ext_vector_type(2)));
typedef short s16x4 __attribute__((ext_vector_type(4)));
typedef float f32x32 __attribute__((ext_vector_type(32)));
typedef float f32x16 __attribute__((ext_vector_type(16)));
typedef float f32x4 __attribute__((ext_vector_type(4)));
typedef float f32x2 __attribute__((ext_vector_type(2)));
typedef unsigned u32x32 __attribute__((ext_vector_type(32)));
typedef unsigned u32x16 __attribute__((ext_vector_type(16)));
typedef unsigned u32x8 __attribute__((ext_vector_type(8)));
typedef unsigned u32x4 __attribute__((ext_vector_type(4)));
typedef unsigned u32x2 __attribute__((ext_vector_type(2)));
typedef unsigned long long u64;
#define LAS __attribute__((address_space(3)))
#define GAS __attribute__((address_space(1)))
#define DEV __device__ __forceinline__

#define ROT ((((r >> 2) + r) & 3) * 2)
constexpr unsigned WS_Q = 0, WS_K = 2u << 20, WS_V = 4u << 20, WS_O = 6u << 20, WS_H = 8u << 20, WS_A = 10u << 20, WS_ST = 17u << 20;
constexpr unsigned WS_FL = WS_ST + (256u << 10), FL_KV = WS_FL, FL_O = WS_FL + 256 * 128, FL_H = WS_FL + 384 * 128, FL_EPOCH = WS_FL + 640 * 128, FL_A = WS_FL + 1024 * 128;
constexpr unsigned WS_BYTES = 18u << 20;
constexpr float QSCALE = 0.25f * 1.4426950408889634f;
constexpr float LN_EPS = 1e-5f;
constexpr unsigned ONES2 = 0x3f803f80u;
constexpr int LDS_SCR = 114688;

#define STAMP_INIT()
#define STAMP(i)
#define ASTAMP(i)
#define ASTAMP_OPS
#define ASTAMP_CLOB
#define STAMP_END()

DEV unsigned cvtpk(float lo, float hi) { bf16x2 r; r[0] = (__bf16)lo; r[1] = (__bf16)hi; return __builtin_bit_cast(unsigned, r); }
DEV float bflo(unsigned v) { return __uint_as_float(v << 16); }
DEV float bfhi(unsigned v) { return __uint_as_float(v & 0xffff0000u); }
DEV float xhalf_add(float v) {
    auto r = __builtin_amdgcn_permlane32_swap(__float_as_uint(v), __float_as_uint(v), false, false);
    return __uint_as_float(r[0]) + __uint_as_float(r[1]);
}
DEV float xhalf_max(float v) {
    auto r = __builtin_amdgcn_permlane32_swap(__float_as_uint(v), __float_as_uint(v), false, false);
    return fmaxf(__uint_as_float(r[0]), __uint_as_float(r[1]));
}
DEV u32x4 mksrd(const void* p, unsigned bytes) { const u64 a = (u64)p; return (u32x4){(unsigned)a, (unsigned)(a >> 32), bytes, 0x20000u}; }
#define LDS_BAR() asm volatile("s_waitcnt lgkmcnt(0)\n\ts_barrier" ::: "memory")
typedef __amdgpu_buffer_rsrc_t rsrc_t;
DEV void st16(rsrc_t r, unsigned off, u32x4 v) { __builtin_amdgcn_raw_buffer_store_b128(v, r, off, 0, 16); }
DEV void publish(char* ws, unsigned flag, u64 tag, int lane) {
    asm volatile("s_waitcnt vmcnt(0)" ::: "memory");
    if (lane == 0) __hip_atomic_store((GAS u64*)(ws + flag), tag, __ATOMIC_RELAXED, __HIP_MEMORY_SCOPE_AGENT);
}
DEV int lane_id() {
    int l; asm volatile("v_mbcnt_lo_u32_b32 %0, -1, 0\n\tv_mbcnt_hi_u32_b32 %0, -1, %0" : "=v"(l)); return l;
}

__global__ __launch_bounds__(256, 1) void fused_block(const char* __restrict__ x, const char* __restrict__ wqkv, const char* __restrict__ wo,
                                                      const char* __restrict__ w1, const char* __restrict__ w2, char* ws, char* out) {
    __shared__ __attribute__((aligned(16))) char smem[163840];
    LAS char* lds = (LAS char*)smem;
    const unsigned lds0 = (unsigned)(uintptr_t)lds;
    const int wv = __builtin_amdgcn_readfirstlane(threadIdx.x >> 6);
    const int b = blockIdx.x, xcd = b & 7, r = b >> 3;
    STAMP_INIT();
    STAMP(0);
    const rsrc_t rws = __builtin_amdgcn_make_buffer_rsrc(ws, 0, WS_BYTES, 0x00020000);
    const u32x4 sws = mksrd(ws, WS_BYTES);
    u32x4 scode;
    asm volatile("s_getpc_b64 s[92:93]\n"
                 "fused_block_pc:\n\t"
                 "s_mov_b32 s94, fused_block_end - fused_block_pc\n\t"
                 "s_mov_b32 s95, 0x20000\n\t"
                 "buffer_load_dword a255, %1, s[92:95], 0 offen"
                 : "={s[92:95]}"(scode) : "v"((r * 256 + threadIdx.x) * 128) : "a255");
    u64 epoch;
    u64 tag;
    unsigned kvflag;
    u32x4 la; u32x2 ix;

    const int rt64 = xcd * 4 + (r >> 3), ct = r & 7;
    const int rt128 = xcd * 2 + (r >> 4), ct4 = r & 15;
    const int tt5 = rt64 * 2 + (wv >> 1), nt5 = ct * 2 + (wv & 1);
    const int head = xcd * 4 + (r & 3), g = r >> 2, qh = wv >> 1, kh = wv & 1, qt = g * 8 + qh * 4;
    const int opos = (head & 3) * 8 + (head >> 2);
    const int tile4 = rt128 * 4 + wv;
    const unsigned mA = lds0 + wv * 28672, mB = lds0 + LDS_SCR + wv * 12288;
    unsigned kv, q0, tv, fl, xv, ov, xw, xr;
    u32x8 w1v; u32x4 tr2, sq; u32x16 sp;
    {
        const int lane = lane_id(), r32 = lane & 31, hh = lane >> 5;
        const int rt = (xcd >> 1) * 8 + (r >> 2), jg = (xcd & 1) * 4 + (r & 3), c0 = ROT;
        const int tt = wv & 1, ch = wv >> 1;
        const u32x4 sx = mksrd(x, 2048 * 1024), sw = mksrd(wqkv, 512 * 3072);
        unsigned xo[2];
#pragma unroll
        for (int q = 0; q < 2; ++q) {
            const int row = 16 * wv + 8 * q + (lane >> 3);
            xo[q] = (rt * 64 + row) * 1024 + (((lane & 7) ^ ((row >> 1) & 7)) << 4);
        }
        const unsigned wof = (16 * wv + (lane >> 3)) * 3072 + jg * 128 + (((lane & 7) ^ (((lane >> 4) & 1) << 2)) << 4);
        asm volatile(
    "s_add_u32 s60, %[c0], 0\n\t"
    "s_and_b32 s60, s60, 7\n\t"
    "s_mul_i32 s61, s60, 0x30000\n\t"
    "s_lshl_b32 s60, s60, 7\n\t"
    "s_add_u32 m0, %[mb], 0x8000\n\t"
    "s_nop 0\n\t"
    "buffer_load_dwordx4 %[xo0], %[sx], s60 offen lds\n\t"
    "s_add_u32 m0, %[mb], 0x8400\n\t"
    "s_nop 0\n\t"
    "buffer_load_dwordx4 %[xo1], %[sx], s60 offen lds\n\t"
    "s_add_u32 m0, %[mb], 0xa000\n\t"
    "s_add_u32 s62, s61, 0x0\n\t"
    "buffer_load_dwordx4 %[wof], %[sw], s62 offen lds\n\t"
    "s_add_u32 m0, %[mb], 0xa400\n\t"
    "s_add_u32 s62, s61, 0x6000\n\t"
    "buffer_load_dwordx4 %[wof], %[sw], s62 offen lds\n\t"
    "s_add_u32 m0, %[mb], 0xc000\n\t"
    "s_add_u32 s62, s61, 0x400\n\t"
    "buffer_load_dwordx4 %[wof], %[sw], s62 offen lds\n\t"
    "s_add_u32 m0, %[mb], 0xc400\n\t"
    "s_add_u32 s62, s61, 0x6400\n\t"
    "buffer_load_dwordx4 %[wof], %[sw], s62 offen lds\n\t"
    "s_add_u32 m0, %[mb], 0xe000\n\t"
    "s_add_u32 s62, s61, 0x800\n\t"
    "buffer_load_dwordx4 %[wof], %[sw], s62 offen lds\n\t"
    "s_add_u32 m0, %[mb], 0xe400\n\t"
    "s_add_u32 s62, s61, 0x6800\n\t"
    "buffer_load_dwordx4 %[wof], %[sw], s62 offen lds\n\t"
    "s_add_u32 s60, %[c0], 1\n\t"
    "s_and_b32 s60, s60, 7\n\t"
    "s_mul_i32 s61, s60, 0x30000\n\t"
    "s_lshl_b32 s60, s60, 7\n\t"
    "s_add_u32 m0, %[mb], 0x10000\n\t"
    "s_nop 0\n\t"
    "buffer_load_dwordx4 %[xo0], %[sx], s60 offen lds\n\t"
    "s_add_u32 m0, %[mb], 0x10400\n\t"
    "s_nop 0\n\t"
    "buffer_load_dwordx4 %[xo1], %[sx], s60 offen lds\n\t"
    "s_add_u32 m0, %[mb], 0x12000\n\t"
    "s_add_u32 s62, s61, 0x0\n\t"
    "buffer_load_dwordx4 %[wof], %[sw], s62 offen lds\n\t"
    "s_add_u32 m0, %[mb], 0x12400\n\t"
    "s_add_u32 s62, s61, 0x6000\n\t"
    "buffer_load_dwordx4 %[wof], %[sw], s62 offen lds\n\t"
    "s_add_u32 m0, %[mb], 0x14000\n\t"
    "s_add_u32 s62, s61, 0x400\n\t"
    "buffer_load_dwordx4 %[wof], %[sw], s62 offen lds\n\t"
    "s_add_u32 m0, %[mb], 0x14400\n\t"
    "s_add_u32 s62, s61, 0x6400\n\t"
    "buffer_load_dwordx4 %[wof], %[sw], s62 offen lds\n\t"
    "s_add_u32 m0, %[mb], 0x16000\n\t"
    "s_add_u32 s62, s61, 0x800\n\t"
    "buffer_load_dwordx4 %[wof], %[sw], s62 offen lds\n\t"
    "s_add_u32 m0, %[mb], 0x16400\n\t"
    "s_add_u32 s62, s61, 0x6800\n\t"
    "buffer_load_dwordx4 %[wof], %[sw], s62 offen lds\n\t"
            :: [xo0] "v"(xo[0]), [xo1] "v"(xo[1]), [wof] "v"(wof), [sx] "s"(sx), [sw] "s"(sw), [c0] "s"(c0), [mb] "s"(lds0 + wv * 2048)
            : "s60", "s61", "s62", "scc", "memory");
        __builtin_amdgcn_sched_barrier(0);
        asm volatile("global_load_dword a253, %0, %1 sc1" :: "v"(WS_FL + ((b * 4 + wv) * 3 + lane % 3) * 128), "s"(ws) : "a253");
        asm volatile("global_load_dwordx2 %0, %1, %2 sc1" : "=v"(epoch) : "v"(FL_EPOCH), "s"(ws));
        const int q4 = (lane & 15) >> 2;
        const unsigned xa = lds0 + (32 * tt + r32) * 128 + ((hh ^ ((r32 >> 1) & 7)) << 4);
        const unsigned wa = lds0 + 8192 + (8 * hh + q4) * 128 + ((ch ^ (q4 >> 1)) << 6) + ((lane >> 4) & 1) * 32 + (lane & 3) * 8;
        const int par = tt ^ ch;
        u32x8 xad; u32x4 wad;
#pragma unroll
        for (int s = 0; s < 4; ++s) { xad[s] = xa ^ ((s ^ (2 * par)) << 5); xad[4 + s] = xad[s] + 65536; }
#pragma unroll
        for (int p = 0; p < 2; ++p) { wad[p] = wa + ((p ^ par) << 12); wad[2 + p] = wad[p] + 65536; }
        const unsigned scr = lds0 + LDS_SCR;
        const int tile = rt * 2 + tt, pair = jg * 2 + ch;
        const u32x8 ea = {scr + (wv ^ 2) * 12288 + lane * 8, scr + (wv ^ 1) * 12288 + lane * 4, scr + wv * 12288 + lane * 4, scr + wv * 12288 + lane * 8,
                          scr + wv * 12288 + hh * 16, lane * 16u, (unsigned)((r32 >> 4) * 65536 + (hh * 32 + (r32 & 15)) * 16), 0u};
        const unsigned on = (lane & 2) ? 0u : ONES2;
        la = (lane & 4) ? (u32x4){0u, 0u, on, on} : (u32x4){on, on, 0u, 0u};
        ix = (u32x2){(lane & 16) ? 0xeeeeu : 0x4444u, (lane & 1) ? 0xeeeeu : 0x4444u};
        asm volatile(
    "v_mov_b32 v160, 0\n\t"
    "v_mov_b32 v161, 0\n\t"
    "v_mov_b32 v162, 0\n\t"
    "v_mov_b32 v163, 0\n\t"
    "v_mov_b32 v164, 0\n\t"
    "v_mov_b32 v165, 0\n\t"
    "v_mov_b32 v166, 0\n\t"
    "v_mov_b32 v167, 0\n\t"
    "v_mov_b32 v168, 0\n\t"
    "v_mov_b32 v169, 0\n\t"
    "v_mov_b32 v170, 0\n\t"
    "v_mov_b32 v171, 0\n\t"
    "v_mov_b32 v172, 0\n\t"
    "v_mov_b32 v173, 0\n\t"
    "v_mov_b32 v174, 0\n\t"
    "v_mov_b32 v175, 0\n\t"
    "v_mov_b32 v176, 0\n\t"
    "v_mov_b32 v177, 0\n\t"
    ASTAMP(1)
    "s_waitcnt vmcnt(10)\n\t"
    "s_barrier\n\t"
    "ds_read_b128 v[24:27], v88 offset:32768\n\t"
    "ds_read_b64_tr_b16 v[32:33], v96 offset:32768\n\t"
    "ds_read_b64_tr_b16 v[34:35], v96 offset:33280\n\t"
    "ds_read_b64_tr_b16 v[40:41], v96 offset:40960\n\t"
    "ds_read_b64_tr_b16 v[42:43], v96 offset:41472\n\t"
    "ds_read_b64_tr_b16 v[48:49], v96 offset:49152\n\t"
    "ds_read_b64_tr_b16 v[50:51], v96 offset:49664\n\t"
    "ds_read_b128 v[28:31], v89 offset:32768\n\t"
    "ds_read_b64_tr_b16 v[36:37], v96 offset:34816\n\t"
    "ds_read_b64_tr_b16 v[38:39], v96 offset:35328\n\t"
    "ds_read_b64_tr_b16 v[44:45], v96 offset:43008\n\t"
    "ds_read_b64_tr_b16 v[46:47], v96 offset:43520\n\t"
    "ds_read_b64_tr_b16 v[52:53], v96 offset:51200\n\t"
    "ds_read_b64_tr_b16 v[54:55], v96 offset:51712\n\t"
    "s_waitcnt lgkmcnt(11)\n\t"
    "v_mfma_f32_32x32x16_bf16 v[112:127], v[32:35], v[24:27], 0\n\t"
    "ds_read_b128 v[56:59], v90 offset:32768\n\t"
    "ds_read_b64_tr_b16 v[64:65], v97 offset:32768\n\t"
    "s_add_u32 s60, %[c0], 2\n\t"
    "s_and_b32 s60, s60, 7\n\t"
    "s_mul_i32 s61, s60, 0x30000\n\t"
    "s_lshl_b32 s60, s60, 7\n\t"
    "s_add_u32 m0, %[mb], 0x18000\n\t"
    "s_nop 0\n\t"
    "buffer_load_dwordx4 %[xo0], %[sx], s60 offen lds\n\t"
    "s_waitcnt lgkmcnt(11)\n\t"
    "v_mfma_f32_32x32x16_bf16 v[128:143], v[40:43], v[24:27], 0\n\t"
    "ds_read_b64_tr_b16 v[66:67], v97 offset:33280\n\t"
    "ds_read_b64_tr_b16 v[72:73], v97 offset:40960\n\t"
    "s_add_u32 m0, %[mb], 0x18400\n\t"
    "s_nop 0\n\t"
    "buffer_load_dwordx4 %[xo1], %[sx], s60 offen lds\n\t"
    "s_waitcnt lgkmcnt(11)\n\t"
    "v_mfma_f32_32x32x16_bf16 v[144:159], v[24:27], v[48:51], 0\n\t"
    "ds_read_b64_tr_b16 v[74:75], v97 offset:41472\n\t"
    "ds_read_b64_tr_b16 v[80:81], v97 offset:49152\n\t"
    "s_add_u32 m0, %[mb], 0x1a000\n\t"
    "s_add_u32 s62, s61, 0x0\n\t"
    "buffer_load_dwordx4 %[wof], %[sw], s62 offen lds\n\t"
    "s_waitcnt lgkmcnt(10)\n\t"
    "v_mfma_f32_32x32x16_bf16 v[112:127], v[36:39], v[28:31], v[112:127]\n\t"
    "ds_read_b64_tr_b16 v[82:83], v97 offset:49664\n\t"
    "ds_read_b128 v[60:63], v91 offset:32768\n\t"
    "s_add_u32 m0, %[mb], 0x1a400\n\t"
    "s_add_u32 s62, s61, 0x6000\n\t"
    "buffer_load_dwordx4 %[wof], %[sw], s62 offen lds\n\t"
    "s_waitcnt lgkmcnt(10)\n\t"
    "v_mfma_f32_32x32x16_bf16 v[128:143], v[44:47], v[28:31], v[128:143]\n\t"
    "ds_read_b64_tr_b16 v[68:69], v97 offset:34816\n\t"
    "ds_read_b64_tr_b16 v[70:71], v97 offset:35328\n\t"
    "s_add_u32 m0, %[mb], 0x1c000\n\t"
    "s_add_u32 s62, s61, 0x400\n\t"
    "buffer_load_dwordx4 %[wof], %[sw], s62 offen lds\n\t"
    "s_waitcnt lgkmcnt(10)\n\t"
    "v_mfma_f32_32x32x16_bf16 v[144:159], v[28:31], v[52:55], v[144:159]\n\t"
    "ds_read_b64_tr_b16 v[76:77], v97 offset:43008\n\t"
    "ds_read_b64_tr_b16 v[78:79], v97 offset:43520\n\t"
    "s_add_u32 m0, %[mb], 0x1c400\n\t"
    "s_add_u32 s62, s61, 0x6400\n\t"
    "buffer_load_dwordx4 %[wof], %[sw], s62 offen lds\n\t"
    "v_smfmac_f32_16x16x64_bf16 v[160:163], v[104:107], v[32:39], v109\n\t"
    "ds_read_b64_tr_b16 v[84:85], v97 offset:51200\n\t"
    "ds_read_b64_tr_b16 v[86:87], v97 offset:51712\n\t"
    "s_add_u32 m0, %[mb], 0x1e000\n\t"
    "s_add_u32 s62, s61, 0x800\n\t"
    "buffer_load_dwordx4 %[wof], %[sw], s62 offen lds\n\t"
    "v_smfmac_f32_16x16x64_bf16 v[164:167], v[104:107], v[40:47], v109\n\t"
    "s_add_u32 m0, %[mb], 0x1e400\n\t"
    "s_add_u32 s62, s61, 0x6800\n\t"
    "buffer_load_dwordx4 %[wof], %[sw], s62 offen lds\n\t"
    "v_smfmac_f32_16x16x64_bf16 v[168:171], v[104:107], v[48:55], v109\n\t"
    "v_smfmac_f32_16x16x64_bf16 v[172:175], v[104:107], v[24:31], v109\n\t"
    "s_waitcnt lgkmcnt(11)\n\t"
    "v_mfma_f32_32x32x16_bf16 v[112:127], v[64:67], v[56:59], v[112:127]\n\t"
    "s_waitcnt lgkmcnt(0)\n\t"
    "s_waitcnt vmcnt(10)\n\t"
    "s_barrier\n\t"
    "ds_read_b128 v[24:27], v92 offset:0\n\t"
    "ds_read_b64_tr_b16 v[32:33], v98 offset:0\n\t"
    "ds_read_b64_tr_b16 v[34:35], v98 offset:512\n\t"
    "s_add_u32 s60, %[c0], 3\n\t"
    "s_and_b32 s60, s60, 7\n\t"
    "s_mul_i32 s61, s60, 0x30000\n\t"
    "s_lshl_b32 s60, s60, 7\n\t"
    "s_add_u32 m0, %[mb], 0x0\n\t"
    "s_nop 0\n\t"
    "buffer_load_dwordx4 %[xo0], %[sx], s60 offen lds\n\t"
    "v_lshlrev_b32 v178, 16, v56\n\t"
    "v_and_b32 v179, 0xffff0000, v56\n\t"
    "v_fmac_f32 v176, v178, v178\n\t"
    "v_fmac_f32 v177, v179, v179\n\t"
    "v_mfma_f32_32x32x16_bf16 v[128:143], v[72:75], v[56:59], v[128:143]\n\t"
    "ds_read_b64_tr_b16 v[40:41], v98 offset:8192\n\t"
    "ds_read_b64_tr_b16 v[42:43], v98 offset:8704\n\t"
    "ds_read_b64_tr_b16 v[48:49], v98 offset:16384\n\t"
    "s_add_u32 m0, %[mb], 0x400\n\t"
    "s_nop 0\n\t"
    "buffer_load_dwordx4 %[xo1], %[sx], s60 offen lds\n\t"
    "v_lshlrev_b32 v178, 16, v57\n\t"
    "v_and_b32 v179, 0xffff0000, v57\n\t"
    "v_fmac_f32 v176, v178, v178\n\t"
    "v_fmac_f32 v177, v179, v179\n\t"
    "v_mfma_f32_32x32x16_bf16 v[144:159], v[56:59], v[80:83], v[144:159]\n\t"
    "ds_read_b64_tr_b16 v[50:51], v98 offset:16896\n\t"
    "ds_read_b128 v[28:31], v93 offset:0\n\t"
    "ds_read_b64_tr_b16 v[36:37], v98 offset:2048\n\t"
    "s_add_u32 m0, %[mb], 0x2000\n\t"
    "s_add_u32 s62, s61, 0x0\n\t"
    "buffer_load_dwordx4 %[wof], %[sw], s62 offen lds\n\t"
    "v_lshlrev_b32 v178, 16, v58\n\t"
    "v_and_b32 v179, 0xffff0000, v58\n\t"
    "v_fmac_f32 v176, v178, v178\n\t"
    "v_fmac_f32 v177, v179, v179\n\t"
    "v_mfma_f32_32x32x16_bf16 v[112:127], v[68:71], v[60:63], v[112:127]\n\t"
    "ds_read_b64_tr_b16 v[38:39], v98 offset:2560\n\t"
    "ds_read_b64_tr_b16 v[44:45], v98 offset:10240\n\t"
    "ds_read_b64_tr_b16 v[46:47], v98 offset:10752\n\t"
    "s_add_u32 m0, %[mb], 0x2400\n\t"
    "s_add_u32 s62, s61, 0x6000\n\t"
    "buffer_load_dwordx4 %[wof], %[sw], s62 offen lds\n\t"
    "v_lshlrev_b32 v178, 16, v59\n\t"
    "v_and_b32 v179, 0xffff0000, v59\n\t"
    "v_fmac_f32 v176, v178, v178\n\t"
    "v_fmac_f32 v177, v179, v179\n\t"
    "v_mfma_f32_32x32x16_bf16 v[128:143], v[76:79], v[60:63], v[128:143]\n\t"
    "ds_read_b64_tr_b16 v[52:53], v98 offset:18432\n\t"
    "ds_read_b64_tr_b16 v[54:55], v98 offset:18944\n\t"
    "s_add_u32 m0, %[mb], 0x4000\n\t"
    "s_add_u32 s62, s61, 0x400\n\t"
    "buffer_load_dwordx4 %[wof], %[sw], s62 offen lds\n\t"
    "v_lshlrev_b32 v178, 16, v60\n\t"
    "v_and_b32 v179, 0xffff0000, v60\n\t"
    "v_fmac_f32 v176, v178, v178\n\t"
    "v_fmac_f32 v177, v179, v179\n\t"
    "v_lshlrev_b32 v178, 16, v61\n\t"
    "v_and_b32 v179, 0xffff0000, v61\n\t"
    "v_fmac_f32 v176, v178, v178\n\t"
    "v_fmac_f32 v177, v179, v179\n\t"
    "v_mfma_f32_32x32x16_bf16 v[144:159], v[60:63], v[84:87], v[144:159]\n\t"
    "s_add_u32 m0, %[mb], 0x4400\n\t"
    "s_add_u32 s62, s61, 0x6400\n\t"
    "buffer_load_dwordx4 %[wof], %[sw], s62 offen lds\n\t"
    "v_lshlrev_b32 v178, 16, v62\n\t"
    "v_and_b32 v179, 0xffff0000, v62\n\t"
    "v_fmac_f32 v176, v178, v178\n\t"
    "v_fmac_f32 v177, v179, v179\n\t"
    "v_lshlrev_b32 v178, 16, v63\n\t"
    "v_and_b32 v179, 0xffff0000, v63\n\t"
    "v_fmac_f32 v176, v178, v178\n\t"
    "v_fmac_f32 v177, v179, v179\n\t"
    "s_waitcnt lgkmcnt(11)\n\t"
    "v_mfma_f32_32x32x16_bf16 v[112:127], v[32:35], v[24:27], v[112:127]\n\t"
    "ds_read_b128 v[56:59], v94 offset:0\n\t"
    "ds_read_b64_tr_b16 v[64:65], v99 offset:0\n\t"
    "s_add_u32 m0, %[mb], 0x6000\n\t"
    "s_add_u32 s62, s61, 0x800\n\t"
    "buffer_load_dwordx4 %[wof], %[sw], s62 offen lds\n\t"
    "s_waitcnt lgkmcnt(11)\n\t"
    "v_mfma_f32_32x32x16_bf16 v[128:143], v[40:43], v[24:27], v[128:143]\n\t"
    "ds_read_b64_tr_b16 v[66:67], v99 offset:512\n\t"
    "ds_read_b64_tr_b16 v[72:73], v99 offset:8192\n\t"
    "s_add_u32 m0, %[mb], 0x6400\n\t"
    "s_add_u32 s62, s61, 0x6800\n\t"
    "buffer_load_dwordx4 %[wof], %[sw], s62 offen lds\n\t"
    "s_waitcnt lgkmcnt(11)\n\t"
    "v_mfma_f32_32x32x16_bf16 v[144:159], v[24:27], v[48:51], v[144:159]\n\t"
    "ds_read_b64_tr_b16 v[74:75], v99 offset:8704\n\t"
    "ds_read_b64_tr_b16 v[80:81], v99 offset:16384\n\t"
    "s_add_u32 s60, %[c0], 4\n\t"
    "s_and_b32 s60, s60, 7\n\t"
    "s_mul_i32 s61, s60, 0x30000\n\t"
    "s_lshl_b32 s60, s60, 7\n\t"
    "s_add_u32 m0, %[mb], 0x8000\n\t"
    "s_nop 0\n\t"
    "buffer_load_dwordx4 %[xo0], %[sx], s60 offen lds\n\t"
    "s_waitcnt lgkmcnt(10)\n\t"
    "v_mfma_f32_32x32x16_bf16 v[112:127], v[36:39], v[28:31], v[112:127]\n\t"
    "ds_read_b64_tr_b16 v[82:83], v99 offset:16896\n\t"
    "ds_read_b128 v[60:63], v95 offset:0\n\t"
    "s_add_u32 m0, %[mb], 0x8400\n\t"
    "s_nop 0\n\t"
    "buffer_load_dwordx4 %[xo1], %[sx], s60 offen lds\n\t"
    "s_waitcnt lgkmcnt(10)\n\t"
    "v_mfma_f32_32x32x16_bf16 v[128:143], v[44:47], v[28:31], v[128:143]\n\t"
    "ds_read_b64_tr_b16 v[68:69], v99 offset:2048\n\t"
    "ds_read_b64_tr_b16 v[70:71], v99 offset:2560\n\t"
    "s_add_u32 m0, %[mb], 0xa000\n\t"
    "s_add_u32 s62, s61, 0x0\n\t"
    "buffer_load_dwordx4 %[wof], %[sw], s62 offen lds\n\t"
    "s_waitcnt lgkmcnt(10)\n\t"
    "v_mfma_f32_32x32x16_bf16 v[144:159], v[28:31], v[52:55], v[144:159]\n\t"
    "ds_read_b64_tr_b16 v[76:77], v99 offset:10240\n\t"
    "ds_read_b64_tr_b16 v[78:79], v99 offset:10752\n\t"
    "s_add_u32 m0, %[mb], 0xa400\n\t"
    "s_add_u32 s62, s61, 0x6000\n\t"
    "buffer_load_dwordx4 %[wof], %[sw], s62 offen lds\n\t"
    "v_smfmac_f32_16x16x64_bf16 v[160:163], v[104:107], v[32:39], v109\n\t"
    "ds_read_b64_tr_b16 v[84:85], v99 offset:18432\n\t"
    "ds_read_b64_tr_b16 v[86:87], v99 offset:18944\n\t"
    "v_smfmac_f32_16x16x64_bf16 v[164:167], v[104:107], v[40:47], v109\n\t"
    "v_smfmac_f32_16x16x64_bf16 v[168:171], v[104:107], v[48:55], v109\n\t"
    "v_smfmac_f32_16x16x64_bf16 v[172:175], v[104:107], v[24:31], v109\n\t"
    "s_waitcnt lgkmcnt(11)\n\t"
    "v_mfma_f32_32x32x16_bf16 v[112:127], v[64:67], v[56:59], v[112:127]\n\t"
    "s_waitcnt lgkmcnt(0)\n\t"
    "s_waitcnt vmcnt(12)\n\t"
    "s_barrier\n\t"
    "ds_read_b128 v[24:27], v92 offset:32768\n\t"
    "ds_read_b64_tr_b16 v[32:33], v98 offset:32768\n\t"
    "ds_read_b64_tr_b16 v[34:35], v98 offset:33280\n\t"
    "v_lshlrev_b32 v178, 16, v56\n\t"
    "v_and_b32 v179, 0xffff0000, v56\n\t"
    "v_fmac_f32 v176, v178, v178\n\t"
    "v_fmac_f32 v177, v179, v179\n\t"
    "v_mfma_f32_32x32x16_bf16 v[128:143], v[72:75], v[56:59], v[128:143]\n\t"
    "ds_read_b64_tr_b16 v[40:41], v98 offset:40960\n\t"
    "ds_read_b64_tr_b16 v[42:43], v98 offset:41472\n\t"
    "ds_read_b64_tr_b16 v[48:49], v98 offset:49152\n\t"
    "v_lshlrev_b32 v178, 16, v57\n\t"
    "v_and_b32 v179, 0xffff0000, v57\n\t"
    "v_fmac_f32 v176, v178, v178\n\t"
    "v_fmac_f32 v177, v179, v179\n\t"
    "v_mfma_f32_32x32x16_bf16 v[144:159], v[56:59], v[80:83], v[144:159]\n\t"
    "ds_read_b64_tr_b16 v[50:51], v98 offset:49664\n\t"
    "ds_read_b128 v[28:31], v93 offset:32768\n\t"
    "ds_read_b64_tr_b16 v[36:37], v98 offset:34816\n\t"
    "v_lshlrev_b32 v178, 16, v58\n\t"
    "v_and_b32 v179, 0xffff0000, v58\n\t"
    "v_fmac_f32 v176, v178, v178\n\t"
    "v_fmac_f32 v177, v179, v179\n\t"
    "v_mfma_f32_32x32x16_bf16 v[112:127], v[68:71], v[60:63], v[112:127]\n\t"
    "ds_read_b64_tr_b16 v[38:39], v98 offset:35328\n\t"
    "ds_read_b64_tr_b16 v[44:45], v98 offset:43008\n\t"
    "ds_read_b64_tr_b16 v[46:47], v98 offset:43520\n\t"
    "v_lshlrev_b32 v178, 16, v59\n\t"
    "v_and_b32 v179, 0xffff0000, v59\n\t"
    "v_fmac_f32 v176, v178, v178\n\t"
    "v_fmac_f32 v177, v179, v179\n\t"
    "v_mfma_f32_32x32x16_bf16 v[128:143], v[76:79], v[60:63], v[128:143]\n\t"
    "ds_read_b64_tr_b16 v[52:53], v98 offset:51200\n\t"
    "ds_read_b64_tr_b16 v[54:55], v98 offset:51712\n\t"
    "s_add_u32 m0, %[mb], 0xc000\n\t"
    "s_add_u32 s62, s61, 0x400\n\t"
    "buffer_load_dwordx4 %[wof], %[sw], s62 offen lds\n\t"
    "v_lshlrev_b32 v178, 16, v60\n\t"
    "v_and_b32 v179, 0xffff0000, v60\n\t"
    "v_fmac_f32 v176, v178, v178\n\t"
    "v_fmac_f32 v177, v179, v179\n\t"
    "v_lshlrev_b32 v178, 16, v61\n\t"
    "v_and_b32 v179, 0xffff0000, v61\n\t"
    "v_fmac_f32 v176, v178, v178\n\t"
    "v_fmac_f32 v177, v179, v179\n\t"
    "v_mfma_f32_32x32x16_bf16 v[144:159], v[60:63], v[84:87], v[144:159]\n\t"
    "s_add_u32 m0, %[mb], 0xc400\n\t"
    "s_add_u32 s62, s61, 0x6400\n\t"
    "buffer_load_dwordx4 %[wof], %[sw], s62 offen lds\n\t"
    "v_lshlrev_b32 v178, 16, v62\n\t"
    "v_and_b32 v179, 0xffff0000, v62\n\t"
    "v_fmac_f32 v176, v178, v178\n\t"
    "v_fmac_f32 v177, v179, v179\n\t"
    "v_lshlrev_b32 v178, 16, v63\n\t"
    "v_and_b32 v179, 0xffff0000, v63\n\t"
    "v_fmac_f32 v176, v178, v178\n\t"
    "v_fmac_f32 v177, v179, v179\n\t"
    "s_waitcnt lgkmcnt(11)\n\t"
    "v_mfma_f32_32x32x16_bf16 v[112:127], v[32:35], v[24:27], v[112:127]\n\t"
    "ds_read_b128 v[56:59], v94 offset:32768\n\t"
    "ds_read_b64_tr_b16 v[64:65], v99 offset:32768\n\t"
    "s_add_u32 m0, %[mb], 0xe000\n\t"
    "s_add_u32 s62, s61, 0x800\n\t"
    "buffer_load_dwordx4 %[wof], %[sw], s62 offen lds\n\t"
    "s_waitcnt lgkmcnt(11)\n\t"
    "v_mfma_f32_32x32x16_bf16 v[128:143], v[40:43], v[24:27], v[128:143]\n\t"
    "ds_read_b64_tr_b16 v[66:67], v99 offset:33280\n\t"
    "ds_read_b64_tr_b16 v[72:73], v99 offset:40960\n\t"
    "s_add_u32 m0, %[mb], 0xe400\n\t"
    "s_add_u32 s62, s61, 0x6800\n\t"
    "buffer_load_dwordx4 %[wof], %[sw], s62 offen lds\n\t"
    "s_waitcnt lgkmcnt(11)\n\t"
    "v_mfma_f32_32x32x16_bf16 v[144:159], v[24:27], v[48:51], v[144:159]\n\t"
    "ds_read_b64_tr_b16 v[74:75], v99 offset:41472\n\t"
    "ds_read_b64_tr_b16 v[80:81], v99 offset:49152\n\t"
    "s_add_u32 s60, %[c0], 5\n\t"
    "s_and_b32 s60, s60, 7\n\t"
    "s_mul_i32 s61, s60, 0x30000\n\t"
    "s_lshl_b32 s60, s60, 7\n\t"
    "s_add_u32 m0, %[mb], 0x10000\n\t"
    "s_nop 0\n\t"
    "buffer_load_dwordx4 %[xo0], %[sx], s60 offen lds\n\t"
    "s_waitcnt lgkmcnt(10)\n\t"
    "v_mfma_f32_32x32x16_bf16 v[112:127], v[36:39], v[28:31], v[112:127]\n\t"
    "ds_read_b64_tr_b16 v[82:83], v99 offset:49664\n\t"
    "ds_read_b128 v[60:63], v95 offset:32768\n\t"
    "s_add_u32 m0, %[mb], 0x10400\n\t"
    "s_nop 0\n\t"
    "buffer_load_dwordx4 %[xo1], %[sx], s60 offen lds\n\t"
    "s_waitcnt lgkmcnt(10)\n\t"
    "v_mfma_f32_32x32x16_bf16 v[128:143], v[44:47], v[28:31], v[128:143]\n\t"
    "ds_read_b64_tr_b16 v[68:69], v99 offset:34816\n\t"
    "ds_read_b64_tr_b16 v[70:71], v99 offset:35328\n\t"
    "s_add_u32 m0, %[mb], 0x12000\n\t"
    "s_add_u32 s62, s61, 0x0\n\t"
    "buffer_load_dwordx4 %[wof], %[sw], s62 offen lds\n\t"
    "s_waitcnt lgkmcnt(10)\n\t"
    "v_mfma_f32_32x32x16_bf16 v[144:159], v[28:31], v[52:55], v[144:159]\n\t"
    "ds_read_b64_tr_b16 v[76:77], v99 offset:43008\n\t"
    "ds_read_b64_tr_b16 v[78:79], v99 offset:43520\n\t"
    "s_add_u32 m0, %[mb], 0x12400\n\t"
    "s_add_u32 s62, s61, 0x6000\n\t"
    "buffer_load_dwordx4 %[wof], %[sw], s62 offen lds\n\t"
    "v_smfmac_f32_16x16x64_bf16 v[160:163], v[104:107], v[32:39], v109\n\t"
    "ds_read_b64_tr_b16 v[84:85], v99 offset:51200\n\t"
    "ds_read_b64_tr_b16 v[86:87], v99 offset:51712\n\t"
    "v_smfmac_f32_16x16x64_bf16 v[164:167], v[104:107], v[40:47], v109\n\t"
    "v_smfmac_f32_16x16x64_bf16 v[168:171], v[104:107], v[48:55], v109\n\t"
    "v_smfmac_f32_16x16x64_bf16 v[172:175], v[104:107], v[24:31], v109\n\t"
    "s_waitcnt lgkmcnt(11)\n\t"
    "v_mfma_f32_32x32x16_bf16 v[112:127], v[64:67], v[56:59], v[112:127]\n\t"
    "s_waitcnt lgkmcnt(0)\n\t"
    "s_waitcnt vmcnt(12)\n\t"
    "s_barrier\n\t"
    "ds_read_b128 v[24:27], v88 offset:0\n\t"
    "ds_read_b64_tr_b16 v[32:33], v96 offset:0\n\t"
    "ds_read_b64_tr_b16 v[34:35], v96 offset:512\n\t"
    "v_lshlrev_b32 v178, 16, v56\n\t"
    "v_and_b32 v179, 0xffff0000, v56\n\t"
    "v_fmac_f32 v176, v178, v178\n\t"
    "v_fmac_f32 v177, v179, v179\n\t"
    "v_mfma_f32_32x32x16_bf16 v[128:143], v[72:75], v[56:59], v[128:143]\n\t"
    "ds_read_b64_tr_b16 v[40:41], v96 offset:8192\n\t"
    "ds_read_b64_tr_b16 v[42:43], v96 offset:8704\n\t"
    "ds_read_b64_tr_b16 v[48:49], v96 offset:16384\n\t"
    "v_lshlrev_b32 v178, 16, v57\n\t"
    "v_and_b32 v179, 0xffff0000, v57\n\t"
    "v_fmac_f32 v176, v178, v178\n\t"
    "v_fmac_f32 v177, v179, v179\n\t"
    "v_mfma_f32_32x32x16_bf16 v[144:159], v[56:59], v[80:83], v[144:159]\n\t"
    "ds_read_b64_tr_b16 v[50:51], v96 offset:16896\n\t"
    "ds_read_b128 v[28:31], v89 offset:0\n\t"
    "ds_read_b64_tr_b16 v[36:37], v96 offset:2048\n\t"
    "v_lshlrev_b32 v178, 16, v58\n\t"
    "v_and_b32 v179, 0xffff0000, v58\n\t"
    "v_fmac_f32 v176, v178, v178\n\t"
    "v_fmac_f32 v177, v179, v179\n\t"
    "v_mfma_f32_32x32x16_bf16 v[112:127], v[68:71], v[60:63], v[112:127]\n\t"
    "ds_read_b64_tr_b16 v[38:39], v96 offset:2560\n\t"
    "ds_read_b64_tr_b16 v[44:45], v96 offset:10240\n\t"
    "ds_read_b64_tr_b16 v[46:47], v96 offset:10752\n\t"
    "v_lshlrev_b32 v178, 16, v59\n\t"
    "v_and_b32 v179, 0xffff0000, v59\n\t"
    "v_fmac_f32 v176, v178, v178\n\t"
    "v_fmac_f32 v177, v179, v179\n\t"
    "v_mfma_f32_32x32x16_bf16 v[128:143], v[76:79], v[60:63], v[128:143]\n\t"
    "ds_read_b64_tr_b16 v[52:53], v96 offset:18432\n\t"
    "ds_read_b64_tr_b16 v[54:55], v96 offset:18944\n\t"
    "s_add_u32 m0, %[mb], 0x14000\n\t"
    "s_add_u32 s62, s61, 0x400\n\t"
    "buffer_load_dwordx4 %[wof], %[sw], s62 offen lds\n\t"
    "v_lshlrev_b32 v178, 16, v60\n\t"
    "v_and_b32 v179, 0xffff0000, v60\n\t"
    "v_fmac_f32 v176, v178, v178\n\t"
    "v_fmac_f32 v177, v179, v179\n\t"
    "v_lshlrev_b32 v178, 16, v61\n\t"
    "v_and_b32 v179, 0xffff0000, v61\n\t"
    "v_fmac_f32 v176, v178, v178\n\t"
    "v_fmac_f32 v177, v179, v179\n\t"
    "v_mfma_f32_32x32x16_bf16 v[144:159], v[60:63], v[84:87], v[144:159]\n\t"
    "s_add_u32 m0, %[mb], 0x14400\n\t"
    "s_add_u32 s62, s61, 0x6400\n\t"
    "buffer_load_dwordx4 %[wof], %[sw], s62 offen lds\n\t"
    "v_lshlrev_b32 v178, 16, v62\n\t"
    "v_and_b32 v179, 0xffff0000, v62\n\t"
    "v_fmac_f32 v176, v178, v178\n\t"
    "v_fmac_f32 v177, v179, v179\n\t"
    "v_lshlrev_b32 v178, 16, v63\n\t"
    "v_and_b32 v179, 0xffff0000, v63\n\t"
    "v_fmac_f32 v176, v178, v178\n\t"
    "v_fmac_f32 v177, v179, v179\n\t"
    "s_waitcnt lgkmcnt(11)\n\t"
    "v_mfma_f32_32x32x16_bf16 v[112:127], v[32:35], v[24:27], v[112:127]\n\t"
    "ds_read_b128 v[56:59], v90 offset:0\n\t"
    "ds_read_b64_tr_b16 v[64:65], v97 offset:0\n\t"
    "s_add_u32 m0, %[mb], 0x16000\n\t"
    "s_add_u32 s62, s61, 0x800\n\t"
    "buffer_load_dwordx4 %[wof], %[sw], s62 offen lds\n\t"
    "s_waitcnt lgkmcnt(11)\n\t"
    "v_mfma_f32_32x32x16_bf16 v[128:143], v[40:43], v[24:27], v[128:143]\n\t"
    "ds_read_b64_tr_b16 v[66:67], v97 offset:512\n\t"
    "ds_read_b64_tr_b16 v[72:73], v97 offset:8192\n\t"
    "s_add_u32 m0, %[mb], 0x16400\n\t"
    "s_add_u32 s62, s61, 0x6800\n\t"
    "buffer_load_dwordx4 %[wof], %[sw], s62 offen lds\n\t"
    "s_waitcnt lgkmcnt(11)\n\t"
    "v_mfma_f32_32x32x16_bf16 v[144:159], v[24:27], v[48:51], v[144:159]\n\t"
    "ds_read_b64_tr_b16 v[74:75], v97 offset:8704\n\t"
    "ds_read_b64_tr_b16 v[80:81], v97 offset:16384\n\t"
    "s_add_u32 s60, %[c0], 6\n\t"
    "s_and_b32 s60, s60, 7\n\t"
    "s_mul_i32 s61, s60, 0x30000\n\t"
    "s_lshl_b32 s60, s60, 7\n\t"
    "s_add_u32 m0, %[mb], 0x18000\n\t"
    "s_nop 0\n\t"
    "buffer_load_dwordx4 %[xo0], %[sx], s60 offen lds\n\t"
    "s_waitcnt lgkmcnt(10)\n\t"
    "v_mfma_f32_32x32x16_bf16 v[112:127], v[36:39], v[28:31], v[112:127]\n\t"
    "ds_read_b64_tr_b16 v[82:83], v97 offset:16896\n\t"
    "ds_read_b128 v[60:63], v91 offset:0\n\t"
    "s_add_u32 m0, %[mb], 0x18400\n\t"
    "s_nop 0\n\t"
    "buffer_load_dwordx4 %[xo1], %[sx], s60 offen lds\n\t"
    "s_waitcnt lgkmcnt(10)\n\t"
    "v_mfma_f32_32x32x16_bf16 v[128:143], v[44:47], v[28:31], v[128:143]\n\t"
    "ds_read_b64_tr_b16 v[68:69], v97 offset:2048\n\t"
    "ds_read_b64_tr_b16 v[70:71], v97 offset:2560\n\t"
    "s_add_u32 m0, %[mb], 0x1a000\n\t"
    "s_add_u32 s62, s61, 0x0\n\t"
    "buffer_load_dwordx4 %[wof], %[sw], s62 offen lds\n\t"
    "s_waitcnt lgkmcnt(10)\n\t"
    "v_mfma_f32_32x32x16_bf16 v[144:159], v[28:31], v[52:55], v[144:159]\n\t"
    "ds_read_b64_tr_b16 v[76:77], v97 offset:10240\n\t"
    "ds_read_b64_tr_b16 v[78:79], v97 offset:10752\n\t"
    "s_add_u32 m0, %[mb], 0x1a400\n\t"
    "s_add_u32 s62, s61, 0x6000\n\t"
    "buffer_load_dwordx4 %[wof], %[sw], s62 offen lds\n\t"
    "v_smfmac_f32_16x16x64_bf16 v[160:163], v[104:107], v[32:39], v109\n\t"
    "ds_read_b64_tr_b16 v[84:85], v97 offset:18432\n\t"
    "ds_read_b64_tr_b16 v[86:87], v97 offset:18944\n\t"
    "v_smfmac_f32_16x16x64_bf16 v[164:167], v[104:107], v[40:47], v109\n\t"
    "v_smfmac_f32_16x16x64_bf16 v[168:171], v[104:107], v[48:55], v109\n\t"
    "v_smfmac_f32_16x16x64_bf16 v[172:175], v[104:107], v[24:31], v109\n\t"
    "s_waitcnt lgkmcnt(11)\n\t"
    "v_mfma_f32_32x32x16_bf16 v[112:127], v[64:67], v[56:59], v[112:127]\n\t"
    "s_waitcnt lgkmcnt(0)\n\t"
    "s_waitcnt vmcnt(12)\n\t"
    "s_barrier\n\t"
    "ds_read_b128 v[24:27], v88 offset:32768\n\t"
    "ds_read_b64_tr_b16 v[32:33], v96 offset:32768\n\t"
    "ds_read_b64_tr_b16 v[34:35], v96 offset:33280\n\t"
    "v_lshlrev_b32 v178, 16, v56\n\t"
    "v_and_b32 v179, 0xffff0000, v56\n\t"
    "v_fmac_f32 v176, v178, v178\n\t"
    "v_fmac_f32 v177, v179, v179\n\t"
    "v_mfma_f32_32x32x16_bf16 v[128:143], v[72:75], v[56:59], v[128:143]\n\t"
    "ds_read_b64_tr_b16 v[40:41], v96 offset:40960\n\t"
    "ds_read_b64_tr_b16 v[42:43], v96 offset:41472\n\t"
    "ds_read_b64_tr_b16 v[48:49], v96 offset:49152\n\t"
    "v_lshlrev_b32 v178, 16, v57\n\t"
    "v_and_b32 v179, 0xffff0000, v57\n\t"
    "v_fmac_f32 v176, v178, v178\n\t"
    "v_fmac_f32 v177, v179, v179\n\t"
    "v_mfma_f32_32x32x16_bf16 v[144:159], v[56:59], v[80:83], v[144:159]\n\t"
    "ds_read_b64_tr_b16 v[50:51], v96 offset:49664\n\t"
    "ds_read_b128 v[28:31], v89 offset:32768\n\t"
    "ds_read_b64_tr_b16 v[36:37], v96 offset:34816\n\t"
    "v_lshlrev_b32 v178, 16, v58\n\t"
    "v_and_b32 v179, 0xffff0000, v58\n\t"
    "v_fmac_f32 v176, v178, v178\n\t"
    "v_fmac_f32 v177, v179, v179\n\t"
    "v_mfma_f32_32x32x16_bf16 v[112:127], v[68:71], v[60:63], v[112:127]\n\t"
    "ds_read_b64_tr_b16 v[38:39], v96 offset:35328\n\t"
    "ds_read_b64_tr_b16 v[44:45], v96 offset:43008\n\t"
    "ds_read_b64_tr_b16 v[46:47], v96 offset:43520\n\t"
    "v_lshlrev_b32 v178, 16, v59\n\t"
    "v_and_b32 v179, 0xffff0000, v59\n\t"
    "v_fmac_f32 v176, v178, v178\n\t"
    "v_fmac_f32 v177, v179, v179\n\t"
    "v_mfma_f32_32x32x16_bf16 v[128:143], v[76:79], v[60:63], v[128:143]\n\t"
    "ds_read_b64_tr_b16 v[52:53], v96 offset:51200\n\t"
    "ds_read_b64_tr_b16 v[54:55], v96 offset:51712\n\t"
    "s_add_u32 m0, %[mb], 0x1c000\n\t"
    "s_add_u32 s62, s61, 0x400\n\t"
    "buffer_load_dwordx4 %[wof], %[sw], s62 offen lds\n\t"
    "v_lshlrev_b32 v178, 16, v60\n\t"
    "v_and_b32 v179, 0xffff0000, v60\n\t"
    "v_fmac_f32 v176, v178, v178\n\t"
    "v_fmac_f32 v177, v179, v179\n\t"
    "v_lshlrev_b32 v178, 16, v61\n\t"
    "v_and_b32 v179, 0xffff0000, v61\n\t"
    "v_fmac_f32 v176, v178, v178\n\t"
    "v_fmac_f32 v177, v179, v179\n\t"
    "v_mfma_f32_32x32x16_bf16 v[144:159], v[60:63], v[84:87], v[144:159]\n\t"
    "s_add_u32 m0, %[mb], 0x1c400\n\t"
    "s_add_u32 s62, s61, 0x6400\n\t"
    "buffer_load_dwordx4 %[wof], %[sw], s62 offen lds\n\t"
    "v_lshlrev_b32 v178, 16, v62\n\t"
    "v_and_b32 v179, 0xffff0000, v62\n\t"
    "v_fmac_f32 v176, v178, v178\n\t"
    "v_fmac_f32 v177, v179, v179\n\t"
    "v_lshlrev_b32 v178, 16, v63\n\t"
    "v_and_b32 v179, 0xffff0000, v63\n\t"
    "v_fmac_f32 v176, v178, v178\n\t"
    "v_fmac_f32 v177, v179, v179\n\t"
    "s_waitcnt lgkmcnt(11)\n\t"
    "v_mfma_f32_32x32x16_bf16 v[112:127], v[32:35], v[24:27], v[112:127]\n\t"
    "ds_read_b128 v[56:59], v90 offset:32768\n\t"
    "ds_read_b64_tr_b16 v[64:65], v97 offset:32768\n\t"
    "s_add_u32 m0, %[mb], 0x1e000\n\t"
    "s_add_u32 s62, s61, 0x800\n\t"
    "buffer_load_dwordx4 %[wof], %[sw], s62 offen lds\n\t"
    "s_waitcnt lgkmcnt(11)\n\t"
    "v_mfma_f32_32x32x16_bf16 v[128:143], v[40:43], v[24:27], v[128:143]\n\t"
    "ds_read_b64_tr_b16 v[66:67], v97 offset:33280\n\t"
    "ds_read_b64_tr_b16 v[72:73], v97 offset:40960\n\t"
    "s_add_u32 m0, %[mb], 0x1e400\n\t"
    "s_add_u32 s62, s61, 0x6800\n\t"
    "buffer_load_dwordx4 %[wof], %[sw], s62 offen lds\n\t"
    "s_waitcnt lgkmcnt(11)\n\t"
    "v_mfma_f32_32x32x16_bf16 v[144:159], v[24:27], v[48:51], v[144:159]\n\t"
    "ds_read_b64_tr_b16 v[74:75], v97 offset:41472\n\t"
    "ds_read_b64_tr_b16 v[80:81], v97 offset:49152\n\t"
    "s_add_u32 s60, %[c0], 7\n\t"
    "s_and_b32 s60, s60, 7\n\t"
    "s_mul_i32 s61, s60, 0x30000\n\t"
    "s_lshl_b32 s60, s60, 7\n\t"
    "s_add_u32 m0, %[mb], 0x0\n\t"
    "s_nop 0\n\t"
    "buffer_load_dwordx4 %[xo0], %[sx], s60 offen lds\n\t"
    "s_waitcnt lgkmcnt(10)\n\t"
    "v_mfma_f32_32x32x16_bf16 v[112:127], v[36:39], v[28:31], v[112:127]\n\t"
    "ds_read_b64_tr_b16 v[82:83], v97 offset:49664\n\t"
    "ds_read_b128 v[60:63], v91 offset:32768\n\t"
    "s_add_u32 m0, %[mb], 0x400\n\t"
    "s_nop 0\n\t"
    "buffer_load_dwordx4 %[xo1], %[sx], s60 offen lds\n\t"
    "s_waitcnt lgkmcnt(10)\n\t"
    "v_mfma_f32_32x32x16_bf16 v[128:143], v[44:47], v[28:31], v[128:143]\n\t"
    "ds_read_b64_tr_b16 v[68:69], v97 offset:34816\n\t"
    "ds_read_b64_tr_b16 v[70:71], v97 offset:35328\n\t"
    "s_add_u32 m0, %[mb], 0x2000\n\t"
    "s_add_u32 s62, s61, 0x0\n\t"
    "buffer_load_dwordx4 %[wof], %[sw], s62 offen lds\n\t"
    "s_waitcnt lgkmcnt(10)\n\t"
    "v_mfma_f32_32x32x16_bf16 v[144:159], v[28:31], v[52:55], v[144:159]\n\t"
    "ds_read_b64_tr_b16 v[76:77], v97 offset:43008\n\t"
    "ds_read_b64_tr_b16 v[78:79], v97 offset:43520\n\t"
    "s_add_u32 m0, %[mb], 0x2400\n\t"
    "s_add_u32 s62, s61, 0x6000\n\t"
    "buffer_load_dwordx4 %[wof], %[sw], s62 offen lds\n\t"
    "v_smfmac_f32_16x16x64_bf16 v[160:163], v[104:107], v[32:39], v109\n\t"
    "ds_read_b64_tr_b16 v[84:85], v97 offset:51200\n\t"
    "ds_read_b64_tr_b16 v[86:87], v97 offset:51712\n\t"
    "v_smfmac_f32_16x16x64_bf16 v[164:167], v[104:107], v[40:47], v109\n\t"
    "v_smfmac_f32_16x16x64_bf16 v[168:171], v[104:107], v[48:55], v109\n\t"
    "v_smfmac_f32_16x16x64_bf16 v[172:175], v[104:107], v[24:31], v109\n\t"
    "s_waitcnt lgkmcnt(11)\n\t"
    "v_mfma_f32_32x32x16_bf16 v[112:127], v[64:67], v[56:59], v[112:127]\n\t"
    "s_waitcnt lgkmcnt(0)\n\t"
    "s_waitcnt vmcnt(12)\n\t"
    "s_barrier\n\t"
    "ds_read_b128 v[24:27], v92 offset:0\n\t"
    "ds_read_b64_tr_b16 v[32:33], v98 offset:0\n\t"
    "ds_read_b64_tr_b16 v[34:35], v98 offset:512\n\t"
    "v_lshlrev_b32 v178, 16, v56\n\t"
    "v_and_b32 v179, 0xffff0000, v56\n\t"
    "v_fmac_f32 v176, v178, v178\n\t"
    "v_fmac_f32 v177, v179, v179\n\t"
    "v_mfma_f32_32x32x16_bf16 v[128:143], v[72:75], v[56:59], v[128:143]\n\t"
    "ds_read_b64_tr_b16 v[40:41], v98 offset:8192\n\t"
    "ds_read_b64_tr_b16 v[42:43], v98 offset:8704\n\t"
    "ds_read_b64_tr_b16 v[48:49], v98 offset:16384\n\t"
    "v_lshlrev_b32 v178, 16, v57\n\t"
    "v_and_b32 v179, 0xffff0000, v57\n\t"
    "v_fmac_f32 v176, v178, v178\n\t"
    "v_fmac_f32 v177, v179, v179\n\t"
    "v_mfma_f32_32x32x16_bf16 v[144:159], v[56:59], v[80:83], v[144:159]\n\t"
    "ds_read_b64_tr_b16 v[50:51], v98 offset:16896\n\t"
    "ds_read_b128 v[28:31], v93 offset:0\n\t"
    "ds_read_b64_tr_b16 v[36:37], v98 offset:2048\n\t"
    "v_lshlrev_b32 v178, 16, v58\n\t"
    "v_and_b32 v179, 0xffff0000, v58\n\t"
    "v_fmac_f32 v176, v178, v178\n\t"
    "v_fmac_f32 v177, v179, v179\n\t"
    "v_mfma_f32_32x32x16_bf16 v[112:127], v[68:71], v[60:63], v[112:127]\n\t"
    "ds_read_b64_tr_b16 v[38:39], v98 offset:2560\n\t"
    "ds_read_b64_tr_b16 v[44:45], v98 offset:10240\n\t"
    "ds_read_b64_tr_b16 v[46:47], v98 offset:10752\n\t"
    "v_lshlrev_b32 v178, 16, v59\n\t"
    "v_and_b32 v179, 0xffff0000, v59\n\t"
    "v_fmac_f32 v176, v178, v178\n\t"
    "v_fmac_f32 v177, v179, v179\n\t"
    "v_mfma_f32_32x32x16_bf16 v[128:143], v[76:79], v[60:63], v[128:143]\n\t"
    "ds_read_b64_tr_b16 v[52:53], v98 offset:18432\n\t"
    "ds_read_b64_tr_b16 v[54:55], v98 offset:18944\n\t"
    "s_add_u32 m0, %[mb], 0x4000\n\t"
    "s_add_u32 s62, s61, 0x400\n\t"
    "buffer_load_dwordx4 %[wof], %[sw], s62 offen lds\n\t"
    "v_lshlrev_b32 v178, 16, v60\n\t"
    "v_and_b32 v179, 0xffff0000, v60\n\t"
    "v_fmac_f32 v176, v178, v178\n\t"
    "v_fmac_f32 v177, v179, v179\n\t"
    "v_lshlrev_b32 v178, 16, v61\n\t"
    "v_and_b32 v179, 0xffff0000, v61\n\t"
    "v_fmac_f32 v176, v178, v178\n\t"
    "v_fmac_f32 v177, v179, v179\n\t"
    "v_mfma_f32_32x32x16_bf16 v[144:159], v[60:63], v[84:87], v[144:159]\n\t"
    "s_add_u32 m0, %[mb], 0x4400\n\t"
    "s_add_u32 s62, s61, 0x6400\n\t"
    "buffer_load_dwordx4 %[wof], %[sw], s62 offen lds\n\t"
    "v_lshlrev_b32 v178, 16, v62\n\t"
    "v_and_b32 v179, 0xffff0000, v62\n\t"
    "v_fmac_f32 v176, v178, v178\n\t"
    "v_fmac_f32 v177, v179, v179\n\t"
    "v_lshlrev_b32 v178, 16, v63\n\t"
    "v_and_b32 v179, 0xffff0000, v63\n\t"
    "v_fmac_f32 v176, v178, v178\n\t"
    "v_fmac_f32 v177, v179, v179\n\t"
    "s_waitcnt lgkmcnt(11)\n\t"
    "v_mfma_f32_32x32x16_bf16 v[112:127], v[32:35], v[24:27], v[112:127]\n\t"
    "ds_read_b128 v[56:59], v94 offset:0\n\t"
    "ds_read_b64_tr_b16 v[64:65], v99 offset:0\n\t"
    "s_add_u32 m0, %[mb], 0x6000\n\t"
    "s_add_u32 s62, s61, 0x800\n\t"
    "buffer_load_dwordx4 %[wof], %[sw], s62 offen lds\n\t"
    "s_waitcnt lgkmcnt(11)\n\t"
    "v_mfma_f32_32x32x16_bf16 v[128:143], v[40:43], v[24:27], v[128:143]\n\t"
    "ds_read_b64_tr_b16 v[66:67], v99 offset:512\n\t"
    "ds_read_b64_tr_b16 v[72:73], v99 offset:8192\n\t"
    "s_add_u32 m0, %[mb], 0x6400\n\t"
    "s_add_u32 s62, s61, 0x6800\n\t"
    "buffer_load_dwordx4 %[wof], %[sw], s62 offen lds\n\t"
    "s_waitcnt lgkmcnt(11)\n\t"
    "v_mfma_f32_32x32x16_bf16 v[144:159], v[24:27], v[48:51], v[144:159]\n\t"
    "ds_read_b64_tr_b16 v[74:75], v99 offset:8704\n\t"
    "ds_read_b64_tr_b16 v[80:81], v99 offset:16384\n\t"
    "s_waitcnt lgkmcnt(10)\n\t"
    "v_mfma_f32_32x32x16_bf16 v[112:127], v[36:39], v[28:31], v[112:127]\n\t"
    "ds_read_b64_tr_b16 v[82:83], v99 offset:16896\n\t"
    "ds_read_b128 v[60:63], v95 offset:0\n\t"
    "s_waitcnt lgkmcnt(10)\n\t"
    "v_mfma_f32_32x32x16_bf16 v[128:143], v[44:47], v[28:31], v[128:143]\n\t"
    "ds_read_b64_tr_b16 v[68:69], v99 offset:2048\n\t"
    "ds_read_b64_tr_b16 v[70:71], v99 offset:2560\n\t"
    "s_waitcnt lgkmcnt(10)\n\t"
    "v_mfma_f32_32x32x16_bf16 v[144:159], v[28:31], v[52:55], v[144:159]\n\t"
    "ds_read_b64_tr_b16 v[76:77], v99 offset:10240\n\t"
    "ds_read_b64_tr_b16 v[78:79], v99 offset:10752\n\t"
    "v_smfmac_f32_16x16x64_bf16 v[160:163], v[104:107], v[32:39], v109\n\t"
    "ds_read_b64_tr_b16 v[84:85], v99 offset:18432\n\t"
    "ds_read_b64_tr_b16 v[86:87], v99 offset:18944\n\t"
    "v_smfmac_f32_16x16x64_bf16 v[164:167], v[104:107], v[40:47], v109\n\t"
    "v_smfmac_f32_16x16x64_bf16 v[168:171], v[104:107], v[48:55], v109\n\t"
    "v_smfmac_f32_16x16x64_bf16 v[172:175], v[104:107], v[24:31], v109\n\t"
    "s_waitcnt lgkmcnt(11)\n\t"
    "v_mfma_f32_32x32x16_bf16 v[112:127], v[64:67], v[56:59], v[112:127]\n\t"
    "s_waitcnt lgkmcnt(0)\n\t"
    "s_waitcnt vmcnt(8)\n\t"
    "s_barrier\n\t"
    "ds_read_b128 v[24:27], v92 offset:32768\n\t"
    "ds_read_b64_tr_b16 v[32:33], v98 offset:32768\n\t"
    "ds_read_b64_tr_b16 v[34:35], v98 offset:33280\n\t"
    "v_lshlrev_b32 v178, 16, v56\n\t"
    "v_and_b32 v179, 0xffff0000, v56\n\t"
    "v_fmac_f32 v176, v178, v178\n\t"
    "v_fmac_f32 v177, v179, v179\n\t"
    "v_mfma_f32_32x32x16_bf16 v[128:143], v[72:75], v[56:59], v[128:143]\n\t"
    "ds_read_b64_tr_b16 v[40:41], v98 offset:40960\n\t"
    "ds_read_b64_tr_b16 v[42:43], v98 offset:41472\n\t"
    "ds_read_b64_tr_b16 v[48:49], v98 offset:49152\n\t"
    "v_lshlrev_b32 v178, 16, v57\n\t"
    "v_and_b32 v179, 0xffff0000, v57\n\t"
    "v_fmac_f32 v176, v178, v178\n\t"
    "v_fmac_f32 v177, v179, v179\n\t"
    "v_mfma_f32_32x32x16_bf16 v[144:159], v[56:59], v[80:83], v[144:159]\n\t"
    "ds_read_b64_tr_b16 v[50:51], v98 offset:49664\n\t"
    "ds_read_b128 v[28:31], v93 offset:32768\n\t"
    "ds_read_b64_tr_b16 v[36:37], v98 offset:34816\n\t"
    "v_lshlrev_b32 v178, 16, v58\n\t"
    "v_and_b32 v179, 0xffff0000, v58\n\t"
    "v_fmac_f32 v176, v178, v178\n\t"
    "v_fmac_f32 v177, v179, v179\n\t"
    "v_mfma_f32_32x32x16_bf16 v[112:127], v[68:71], v[60:63], v[112:127]\n\t"
    "ds_read_b64_tr_b16 v[38:39], v98 offset:35328\n\t"
    "ds_read_b64_tr_b16 v[44:45], v98 offset:43008\n\t"
    "ds_read_b64_tr_b16 v[46:47], v98 offset:43520\n\t"
    "v_lshlrev_b32 v178, 16, v59\n\t"
    "v_and_b32 v179, 0xffff0000, v59\n\t"
    "v_fmac_f32 v176, v178, v178\n\t"
    "v_fmac_f32 v177, v179, v179\n\t"
    "v_mfma_f32_32x32x16_bf16 v[128:143], v[76:79], v[60:63], v[128:143]\n\t"
    "ds_read_b64_tr_b16 v[52:53], v98 offset:51200\n\t"
    "ds_read_b64_tr_b16 v[54:55], v98 offset:51712\n\t"
    "v_lshlrev_b32 v178, 16, v60\n\t"
    "v_and_b32 v179, 0xffff0000, v60\n\t"
    "v_fmac_f32 v176, v178, v178\n\t"
    "v_fmac_f32 v177, v179, v179\n\t"
    "v_lshlrev_b32 v178, 16, v61\n\t"
    "v_and_b32 v179, 0xffff0000, v61\n\t"
    "v_fmac_f32 v176, v178, v178\n\t"
    "v_fmac_f32 v177, v179, v179\n\t"
    "v_mfma_f32_32x32x16_bf16 v[144:159], v[60:63], v[84:87], v[144:159]\n\t"
    "v_lshlrev_b32 v178, 16, v62\n\t"
    "v_and_b32 v179, 0xffff0000, v62\n\t"
    "v_fmac_f32 v176, v178, v178\n\t"
    "v_fmac_f32 v177, v179, v179\n\t"
    "v_lshlrev_b32 v178, 16, v63\n\t"
    "v_and_b32 v179, 0xffff0000, v63\n\t"
    "v_fmac_f32 v176, v178, v178\n\t"
    "v_fmac_f32 v177, v179, v179\n\t"
    "s_waitcnt lgkmcnt(11)\n\t"
    "v_mfma_f32_32x32x16_bf16 v[112:127], v[32:35], v[24:27], v[112:127]\n\t"
    "ds_read_b128 v[56:59], v94 offset:32768\n\t"
    "ds_read_b64_tr_b16 v[64:65], v99 offset:32768\n\t"
    "s_waitcnt lgkmcnt(11)\n\t"
    "v_mfma_f32_32x32x16_bf16 v[128:143], v[40:43], v[24:27], v[128:143]\n\t"
    "ds_read_b64_tr_b16 v[66:67], v99 offset:33280\n\t"
    "ds_read_b64_tr_b16 v[72:73], v99 offset:40960\n\t"
    "s_waitcnt lgkmcnt(11)\n\t"
    "v_mfma_f32_32x32x16_bf16 v[144:159], v[24:27], v[48:51], v[144:159]\n\t"
    "ds_read_b64_tr_b16 v[74:75], v99 offset:41472\n\t"
    "ds_read_b64_tr_b16 v[80:81], v99 offset:49152\n\t"
    "s_waitcnt lgkmcnt(10)\n\t"
    "v_mfma_f32_32x32x16_bf16 v[112:127], v[36:39], v[28:31], v[112:127]\n\t"
    "ds_read_b64_tr_b16 v[82:83], v99 offset:49664\n\t"
    "ds_read_b128 v[60:63], v95 offset:32768\n\t"
    "s_waitcnt lgkmcnt(10)\n\t"
    "v_mfma_f32_32x32x16_bf16 v[128:143], v[44:47], v[28:31], v[128:143]\n\t"
    "ds_read_b64_tr_b16 v[68:69], v99 offset:34816\n\t"
    "ds_read_b64_tr_b16 v[70:71], v99 offset:35328\n\t"
    "s_waitcnt lgkmcnt(10)\n\t"
    "v_mfma_f32_32x32x16_bf16 v[144:159], v[28:31], v[52:55], v[144:159]\n\t"
    "ds_read_b64_tr_b16 v[76:77], v99 offset:43008\n\t"
    "ds_read_b64_tr_b16 v[78:79], v99 offset:43520\n\t"
    "v_smfmac_f32_16x16x64_bf16 v[160:163], v[104:107], v[32:39], v109\n\t"
    "ds_read_b64_tr_b16 v[84:85], v99 offset:51200\n\t"
    "ds_read_b64_tr_b16 v[86:87], v99 offset:51712\n\t"
    "v_smfmac_f32_16x16x64_bf16 v[164:167], v[104:107], v[40:47], v109\n\t"
    "v_smfmac_f32_16x16x64_bf16 v[168:171], v[104:107], v[48:55], v109\n\t"
    "v_smfmac_f32_16x16x64_bf16 v[172:175], v[104:107], v[24:31], v109\n\t"
    "s_waitcnt lgkmcnt(11)\n\t"
    "v_mfma_f32_32x32x16_bf16 v[112:127], v[64:67], v[56:59], v[112:127]\n\t"
    "s_waitcnt lgkmcnt(0)\n\t"
    "s_waitcnt vmcnt(0)\n\t"
    "s_barrier\n\t"
    "ds_read_b128 v[24:27], v88 offset:0\n\t"
    "ds_read_b64_tr_b16 v[32:33], v96 offset:0\n\t"
    "ds_read_b64_tr_b16 v[34:35], v96 offset:512\n\t"
    "v_lshlrev_b32 v178, 16, v56\n\t"
    "v_and_b32 v179, 0xffff0000, v56\n\t"
    "v_fmac_f32 v176, v178, v178\n\t"
    "v_fmac_f32 v177, v179, v179\n\t"
    "v_mfma_f32_32x32x16_bf16 v[128:143], v[72:75], v[56:59], v[128:143]\n\t"
    "ds_read_b64_tr_b16 v[40:41], v96 offset:8192\n\t"
    "ds_read_b64_tr_b16 v[42:43], v96 offset:8704\n\t"
    "ds_read_b64_tr_b16 v[48:49], v96 offset:16384\n\t"
    "v_lshlrev_b32 v178, 16, v57\n\t"
    "v_and_b32 v179, 0xffff0000, v57\n\t"
    "v_fmac_f32 v176, v178, v178\n\t"
    "v_fmac_f32 v177, v179, v179\n\t"
    "v_mfma_f32_32x32x16_bf16 v[144:159], v[56:59], v[80:83], v[144:159]\n\t"
    "ds_read_b64_tr_b16 v[50:51], v96 offset:16896\n\t"
    "ds_read_b128 v[28:31], v89 offset:0\n\t"
    "ds_read_b64_tr_b16 v[36:37], v96 offset:2048\n\t"
    "v_lshlrev_b32 v178, 16, v58\n\t"
    "v_and_b32 v179, 0xffff0000, v58\n\t"
    "v_fmac_f32 v176, v178, v178\n\t"
    "v_fmac_f32 v177, v179, v179\n\t"
    "v_mfma_f32_32x32x16_bf16 v[112:127], v[68:71], v[60:63], v[112:127]\n\t"
    "ds_read_b64_tr_b16 v[38:39], v96 offset:2560\n\t"
    "ds_read_b64_tr_b16 v[44:45], v96 offset:10240\n\t"
    "ds_read_b64_tr_b16 v[46:47], v96 offset:10752\n\t"
    "v_lshlrev_b32 v178, 16, v59\n\t"
    "v_and_b32 v179, 0xffff0000, v59\n\t"
    "v_fmac_f32 v176, v178, v178\n\t"
    "v_fmac_f32 v177, v179, v179\n\t"
    "v_mfma_f32_32x32x16_bf16 v[128:143], v[76:79], v[60:63], v[128:143]\n\t"
    "ds_read_b64_tr_b16 v[52:53], v96 offset:18432\n\t"
    "ds_read_b64_tr_b16 v[54:55], v96 offset:18944\n\t"
    "v_lshlrev_b32 v178, 16, v60\n\t"
    "v_and_b32 v179, 0xffff0000, v60\n\t"
    "v_fmac_f32 v176, v178, v178\n\t"
    "v_fmac_f32 v177, v179, v179\n\t"
    "v_lshlrev_b32 v178, 16, v61\n\t"
    "v_and_b32 v179, 0xffff0000, v61\n\t"
    "v_fmac_f32 v176, v178, v178\n\t"
    "v_fmac_f32 v177, v179, v179\n\t"
    "v_mfma_f32_32x32x16_bf16 v[144:159], v[60:63], v[84:87], v[144:159]\n\t"
    "v_lshlrev_b32 v178, 16, v62\n\t"
    "v_and_b32 v179, 0xffff0000, v62\n\t"
    "v_fmac_f32 v176, v178, v178\n\t"
    "v_fmac_f32 v177, v179, v179\n\t"
    "v_lshlrev_b32 v178, 16, v63\n\t"
    "v_and_b32 v179, 0xffff0000, v63\n\t"
    "v_fmac_f32 v176, v178, v178\n\t"
    "v_fmac_f32 v177, v179, v179\n\t"
    "s_waitcnt lgkmcnt(11)\n\t"
    "v_mfma_f32_32x32x16_bf16 v[112:127], v[32:35], v[24:27], v[112:127]\n\t"
    "ds_read_b128 v[56:59], v90 offset:0\n\t"
    "ds_read_b64_tr_b16 v[64:65], v97 offset:0\n\t"
    "v_lshlrev_b32 v178, 16, v24\n\t"
    "v_and_b32 v179, 0xffff0000, v24\n\t"
    "v_fmac_f32 v176, v178, v178\n\t"
    "v_fmac_f32 v177, v179, v179\n\t"
    "s_waitcnt lgkmcnt(11)\n\t"
    "v_mfma_f32_32x32x16_bf16 v[128:143], v[40:43], v[24:27], v[128:143]\n\t"
    "ds_read_b64_tr_b16 v[66:67], v97 offset:512\n\t"
    "ds_read_b64_tr_b16 v[72:73], v97 offset:8192\n\t"
    "v_lshlrev_b32 v178, 16, v25\n\t"
    "v_and_b32 v179, 0xffff0000, v25\n\t"
    "v_fmac_f32 v176, v178, v178\n\t"
    "v_fmac_f32 v177, v179, v179\n\t"
    "s_waitcnt lgkmcnt(11)\n\t"
    "v_mfma_f32_32x32x16_bf16 v[144:159], v[24:27], v[48:51], v[144:159]\n\t"
    "ds_read_b64_tr_b16 v[74:75], v97 offset:8704\n\t"
    "ds_read_b64_tr_b16 v[80:81], v97 offset:16384\n\t"
    "v_lshlrev_b32 v178, 16, v26\n\t"
    "v_and_b32 v179, 0xffff0000, v26\n\t"
    "v_fmac_f32 v176, v178, v178\n\t"
    "v_fmac_f32 v177, v179, v179\n\t"
    "s_waitcnt lgkmcnt(10)\n\t"
    "v_mfma_f32_32x32x16_bf16 v[112:127], v[36:39], v[28:31], v[112:127]\n\t"
    "ds_read_b64_tr_b16 v[82:83], v97 offset:16896\n\t"
    "ds_read_b128 v[60:63], v91 offset:0\n\t"
    "v_lshlrev_b32 v178, 16, v27\n\t"
    "v_and_b32 v179, 0xffff0000, v27\n\t"
    "v_fmac_f32 v176, v178, v178\n\t"
    "v_fmac_f32 v177, v179, v179\n\t"
    "s_waitcnt lgkmcnt(10)\n\t"
    "v_mfma_f32_32x32x16_bf16 v[128:143], v[44:47], v[28:31], v[128:143]\n\t"
    "ds_read_b64_tr_b16 v[68:69], v97 offset:2048\n\t"
    "ds_read_b64_tr_b16 v[70:71], v97 offset:2560\n\t"
    "v_lshlrev_b32 v178, 16, v28\n\t"
    "v_and_b32 v179, 0xffff0000, v28\n\t"
    "v_fmac_f32 v176, v178, v178\n\t"
    "v_fmac_f32 v177, v179, v179\n\t"
    "s_waitcnt lgkmcnt(10)\n\t"
    "v_mfma_f32_32x32x16_bf16 v[144:159], v[28:31], v[52:55], v[144:159]\n\t"
    "ds_read_b64_tr_b16 v[76:77], v97 offset:10240\n\t"
    "ds_read_b64_tr_b16 v[78:79], v97 offset:10752\n\t"
    "v_lshlrev_b32 v178, 16, v29\n\t"
    "v_and_b32 v179, 0xffff0000, v29\n\t"
    "v_fmac_f32 v176, v178, v178\n\t"
    "v_fmac_f32 v177, v179, v179\n\t"
    "v_smfmac_f32_16x16x64_bf16 v[160:163], v[104:107], v[32:39], v109\n\t"
    "ds_read_b64_tr_b16 v[84:85], v97 offset:18432\n\t"
    "ds_read_b64_tr_b16 v[86:87], v97 offset:18944\n\t"
    "v_lshlrev_b32 v178, 16, v30\n\t"
    "v_and_b32 v179, 0xffff0000, v30\n\t"
    "v_fmac_f32 v176, v178, v178\n\t"
    "v_fmac_f32 v177, v179, v179\n\t"
    "v_smfmac_f32_16x16x64_bf16 v[164:167], v[104:107], v[40:47], v109\n\t"
    "v_lshlrev_b32 v178, 16, v31\n\t"
    "v_and_b32 v179, 0xffff0000, v31\n\t"
    "v_fmac_f32 v176, v178, v178\n\t"
    "v_fmac_f32 v177, v179, v179\n\t"
    "v_smfmac_f32_16x16x64_bf16 v[168:171], v[104:107], v[48:55], v109\n\t"
    "v_smfmac_f32_16x16x64_bf16 v[172:175], v[104:107], v[24:31], v109\n\t"
    "s_waitcnt lgkmcnt(11)\n\t"
    "v_mfma_f32_32x32x16_bf16 v[112:127], v[64:67], v[56:59], v[112:127]\n\t"
    "v_add_f32 v176, v176, v177\n\t"
    "s_nop 3\n\t"
    "v_add_f32 v160, v160, v161\n\t"
    "v_mov_b32 v177, v176\n\t"
    "v_add_f32 v164, v164, v165\n\t"
    "s_waitcnt lgkmcnt(9)\n\t"
    "v_mfma_f32_32x32x16_bf16 v[128:143], v[72:75], v[56:59], v[128:143]\n\t"
    "v_permlane32_swap_b32 v176, v177\n\t"
    "v_add_f32 v168, v168, v169\n\t"
    "v_add_f32 v172, v172, v173\n\t"
    "v_permlane32_swap_b32 v160, v164\n\t"
    "v_add_f32 v173, v176, v177\n\t"
    "s_waitcnt lgkmcnt(7)\n\t"
    "v_mfma_f32_32x32x16_bf16 v[144:159], v[56:59], v[80:83], v[144:159]\n\t"
    "ds_write_b64 v180, v[172:173]\n\t"
    "ds_write_b32 v181, v168 offset:1024\n\t"
    "ds_write_b32 v181, v160 offset:512\n\t"
    "ds_write_b32 v182, v160 offset:768\n\t"
    "s_waitcnt lgkmcnt(8)\n\t"
    "v_mfma_f32_32x32x16_bf16 v[112:127], v[68:71], v[60:63], v[112:127]\n\t"
    "s_waitcnt lgkmcnt(6)\n\t"
    "v_mfma_f32_32x32x16_bf16 v[128:143], v[76:79], v[60:63], v[128:143]\n\t"
    "s_waitcnt lgkmcnt(0)\n\t"
    "s_barrier\n\t"
    "ds_read_b64 v[176:177], v183\n\t"
    "ds_read_b32 v169, v182 offset:1024\n\t"
    "v_mfma_f32_32x32x16_bf16 v[144:159], v[60:63], v[84:87], v[144:159]\n\t"
    ASTAMP(10)
    "ds_read_b128 v[24:27], v184 offset:640\n\t"
    "ds_read_b128 v[40:43], v184 offset:896\n\t"
    "ds_read_b128 v[28:31], v184 offset:672\n\t"
    "ds_read_b128 v[44:47], v184 offset:928\n\t"
    "ds_read_b128 v[32:35], v184 offset:704\n\t"
    "ds_read_b128 v[48:51], v184 offset:960\n\t"
    "ds_read_b128 v[36:39], v184 offset:736\n\t"
    "ds_read_b128 v[52:55], v184 offset:992\n\t"
    "s_waitcnt lgkmcnt(8)\n\t"
    "v_pk_add_f32 v[172:173], v[172:173], v[176:177]\n\t"
    "v_add_f32 v168, v168, v169\n\t"
    "v_mul_f32 v172, 0xbb000000, v172\n\t"
    "v_mul_f32 v173, 0x3b000000, v173\n\t"
    "v_fma_f32 v160, -v172, v172, v173\n\t"
    "v_max_f32 v160, 0, v160\n\t"
    "v_add_f32 v160, 0x3727c5ac, v160\n\t"
    "v_rsq_f32 v160, v160\n\t"
    "v_mov_b32 v176, v172\n\t"
    "v_mov_b32 v177, v160\n\t"
    "v_mul_f32 v164, 0x3eb8aa3b, v160\n\t"
    "s_nop 0\n\t"
    "v_permlane32_swap_b32 v176, v177\n\t"
    "ds_write_b32 v182, v176 offset:1280\n\t"
    "s_waitcnt lgkmcnt(1)\n\t"
    "ds_read_b128 v[188:191], v184 offset:1280\n\t"
    "ds_read_b128 v[204:207], v184 offset:1408\n\t"
    "ds_read_b128 v[192:195], v184 offset:1312\n\t"
    "ds_read_b128 v[208:211], v184 offset:1440\n\t"
    "ds_read_b128 v[196:199], v184 offset:1344\n\t"
    "ds_read_b128 v[212:215], v184 offset:1472\n\t"
    "ds_read_b128 v[200:203], v184 offset:1376\n\t"
    "ds_read_b128 v[216:219], v184 offset:1504\n\t"
    "v_pk_add_f32 v[24:25], v[24:25], v[40:41]\n\t"
    "v_pk_add_f32 v[26:27], v[26:27], v[42:43]\n\t"
    "v_pk_add_f32 v[28:29], v[28:29], v[44:45]\n\t"
    "v_pk_add_f32 v[30:31], v[30:31], v[46:47]\n\t"
    "v_pk_add_f32 v[32:33], v[32:33], v[48:49]\n\t"
    "v_pk_add_f32 v[34:35], v[34:35], v[50:51]\n\t"
    "v_pk_add_f32 v[36:37], v[36:37], v[52:53]\n\t"
    "v_pk_add_f32 v[38:39], v[38:39], v[54:55]\n\t"
    "v_pk_fma_f32 v[128:129], v[172:173], v[24:25], v[128:129] op_sel_hi:[0,1,1]\n\t"
    "v_pk_fma_f32 v[130:131], v[172:173], v[26:27], v[130:131] op_sel_hi:[0,1,1]\n\t"
    "v_pk_fma_f32 v[132:133], v[172:173], v[28:29], v[132:133] op_sel_hi:[0,1,1]\n\t"
    "v_pk_fma_f32 v[134:135], v[172:173], v[30:31], v[134:135] op_sel_hi:[0,1,1]\n\t"
    "v_pk_fma_f32 v[136:137], v[172:173], v[32:33], v[136:137] op_sel_hi:[0,1,1]\n\t"
    "v_pk_fma_f32 v[138:139], v[172:173], v[34:35], v[138:139] op_sel_hi:[0,1,1]\n\t"
    "v_pk_fma_f32 v[140:141], v[172:173], v[36:37], v[140:141] op_sel_hi:[0,1,1]\n\t"
    "v_pk_fma_f32 v[142:143], v[172:173], v[38:39], v[142:143] op_sel_hi:[0,1,1]\n\t"
    "v_pk_mul_f32 v[128:129], v[160:161], v[128:129] op_sel_hi:[0,1]\n\t"
    "v_pk_mul_f32 v[130:131], v[160:161], v[130:131] op_sel_hi:[0,1]\n\t"
    "v_pk_mul_f32 v[132:133], v[160:161], v[132:133] op_sel_hi:[0,1]\n\t"
    "v_pk_mul_f32 v[134:135], v[160:161], v[134:135] op_sel_hi:[0,1]\n\t"
    "v_pk_mul_f32 v[136:137], v[160:161], v[136:137] op_sel_hi:[0,1]\n\t"
    "v_pk_mul_f32 v[138:139], v[160:161], v[138:139] op_sel_hi:[0,1]\n\t"
    "v_pk_mul_f32 v[140:141], v[160:161], v[140:141] op_sel_hi:[0,1]\n\t"
    "v_pk_mul_f32 v[142:143], v[160:161], v[142:143] op_sel_hi:[0,1]\n\t"
    "v_cvt_pk_bf16_f32 v128, v128, v129\n\t"
    "s_add_u32 s60, %[ob], 0x200000\n\t"
    "v_cvt_pk_bf16_f32 v129, v130, v131\n\t"
    "v_cvt_pk_bf16_f32 v130, v132, v133\n\t"
    "v_cvt_pk_bf16_f32 v131, v134, v135\n\t"
    "v_cvt_pk_bf16_f32 v132, v136, v137\n\t"
    "s_add_u32 s61, %[ob], 0x210000\n\t"
    "v_cvt_pk_bf16_f32 v133, v138, v139\n\t"
    "v_cvt_pk_bf16_f32 v134, v140, v141\n\t"
    "v_cvt_pk_bf16_f32 v135, v142, v143\n\t"
    "buffer_store_dwordx4 v[128:131], v185, %[sws], s60 offen sc1\n\t"
    "buffer_store_dwordx4 v[132:135], v185, %[sws], s61 offen sc1\n\t"
    "ds_read_b128 v[56:59], v184 offset:512\n\t"
    "ds_read_b128 v[72:75], v184 offset:768\n\t"
    "ds_read_b128 v[60:63], v184 offset:544\n\t"
    "ds_read_b128 v[76:79], v184 offset:800\n\t"
    "ds_read_b128 v[64:67], v184 offset:576\n\t"
    "ds_read_b128 v[80:83], v184 offset:832\n\t"
    "ds_read_b128 v[68:71], v184 offset:608\n\t"
    "ds_read_b128 v[84:87], v184 offset:864\n\t"
    "s_waitcnt lgkmcnt(8)\n\t"
    "v_pk_fma_f32 v[144:145], v[168:169], v[188:189], v[144:145] op_sel_hi:[0,1,1]\n\t"
    "v_pk_fma_f32 v[146:147], v[168:169], v[190:191], v[146:147] op_sel_hi:[0,1,1]\n\t"
    "v_pk_fma_f32 v[148:149], v[168:169], v[192:193], v[148:149] op_sel_hi:[0,1,1]\n\t"
    "v_pk_fma_f32 v[150:151], v[168:169], v[194:195], v[150:151] op_sel_hi:[0,1,1]\n\t"
    "v_pk_fma_f32 v[152:153], v[168:169], v[196:197], v[152:153] op_sel_hi:[0,1,1]\n\t"
    "v_pk_fma_f32 v[154:155], v[168:169], v[198:199], v[154:155] op_sel_hi:[0,1,1]\n\t"
    "v_pk_fma_f32 v[156:157], v[168:169], v[200:201], v[156:157] op_sel_hi:[0,1,1]\n\t"
    "v_pk_fma_f32 v[158:159], v[168:169], v[202:203], v[158:159] op_sel_hi:[0,1,1]\n\t"
    "v_pk_mul_f32 v[144:145], v[204:205], v[144:145]\n\t"
    "v_pk_mul_f32 v[146:147], v[206:207], v[146:147]\n\t"
    "v_pk_mul_f32 v[148:149], v[208:209], v[148:149]\n\t"
    "v_pk_mul_f32 v[150:151], v[210:211], v[150:151]\n\t"
    "v_pk_mul_f32 v[152:153], v[212:213], v[152:153]\n\t"
    "v_pk_mul_f32 v[154:155], v[214:215], v[154:155]\n\t"
    "v_pk_mul_f32 v[156:157], v[216:217], v[156:157]\n\t"
    "v_pk_mul_f32 v[158:159], v[218:219], v[158:159]\n\t"
    "v_cvt_pk_bf16_f32 v220, v144, v145\n\t"
    "v_cvt_pk_bf16_f32 v224, v146, v147\n\t"
    "v_cvt_pk_bf16_f32 v221, v148, v149\n\t"
    "v_cvt_pk_bf16_f32 v225, v150, v151\n\t"
    "v_cvt_pk_bf16_f32 v222, v152, v153\n\t"
    "v_cvt_pk_bf16_f32 v226, v154, v155\n\t"
    "v_cvt_pk_bf16_f32 v223, v156, v157\n\t"
    "v_cvt_pk_bf16_f32 v227, v158, v159\n\t"
    "s_add_u32 s60, %[ob], 0x400000\n\t"
    "s_nop 0\n\t"
    "v_permlane32_swap_b32 v220, v222\n\t"
    "v_permlane32_swap_b32 v221, v223\n\t"
    "v_permlane32_swap_b32 v224, v226\n\t"
    "v_permlane32_swap_b32 v225, v227\n\t"
    "buffer_store_dwordx4 v[220:223], v186, %[sws], s60 offen offset:0 sc1\n\t"
    "buffer_store_dwordx4 v[224:227], v186, %[sws], s60 offen offset:256 sc1\n\t"
    "s_waitcnt lgkmcnt(0)\n\t"
    "v_pk_add_f32 v[56:57], v[56:57], v[72:73]\n\t"
    "v_pk_add_f32 v[58:59], v[58:59], v[74:75]\n\t"
    "v_pk_add_f32 v[60:61], v[60:61], v[76:77]\n\t"
    "v_pk_add_f32 v[62:63], v[62:63], v[78:79]\n\t"
    "v_pk_add_f32 v[64:65], v[64:65], v[80:81]\n\t"
    "v_pk_add_f32 v[66:67], v[66:67], v[82:83]\n\t"
    "v_pk_add_f32 v[68:69], v[68:69], v[84:85]\n\t"
    "v_pk_add_f32 v[70:71], v[70:71], v[86:87]\n\t"
    "v_pk_fma_f32 v[112:113], v[172:173], v[56:57], v[112:113] op_sel_hi:[0,1,1]\n\t"
    "v_pk_fma_f32 v[114:115], v[172:173], v[58:59], v[114:115] op_sel_hi:[0,1,1]\n\t"
    "v_pk_fma_f32 v[116:117], v[172:173], v[60:61], v[116:117] op_sel_hi:[0,1,1]\n\t"
    "v_pk_fma_f32 v[118:119], v[172:173], v[62:63], v[118:119] op_sel_hi:[0,1,1]\n\t"
    "v_pk_fma_f32 v[120:121], v[172:173], v[64:65], v[120:121] op_sel_hi:[0,1,1]\n\t"
    "v_pk_fma_f32 v[122:123], v[172:173], v[66:67], v[122:123] op_sel_hi:[0,1,1]\n\t"
    "v_pk_fma_f32 v[124:125], v[172:173], v[68:69], v[124:125] op_sel_hi:[0,1,1]\n\t"
    "v_pk_fma_f32 v[126:127], v[172:173], v[70:71], v[126:127] op_sel_hi:[0,1,1]\n\t"
    "v_pk_mul_f32 v[112:113], v[164:165], v[112:113] op_sel_hi:[0,1]\n\t"
    "v_pk_mul_f32 v[114:115], v[164:165], v[114:115] op_sel_hi:[0,1]\n\t"
    "v_pk_mul_f32 v[116:117], v[164:165], v[116:117] op_sel_hi:[0,1]\n\t"
    "v_pk_mul_f32 v[118:119], v[164:165], v[118:119] op_sel_hi:[0,1]\n\t"
    "v_pk_mul_f32 v[120:121], v[164:165], v[120:121] op_sel_hi:[0,1]\n\t"
    "v_pk_mul_f32 v[122:123], v[164:165], v[122:123] op_sel_hi:[0,1]\n\t"
    "v_pk_mul_f32 v[124:125], v[164:165], v[124:125] op_sel_hi:[0,1]\n\t"
    "v_pk_mul_f32 v[126:127], v[164:165], v[126:127] op_sel_hi:[0,1]\n\t"
    "v_cvt_pk_bf16_f32 v112, v112, v113\n\t"
    "s_add_u32 s60, %[ob], 0x0\n\t"
    "v_cvt_pk_bf16_f32 v113, v114, v115\n\t"
    "v_cvt_pk_bf16_f32 v114, v116, v117\n\t"
    "v_cvt_pk_bf16_f32 v115, v118, v119\n\t"
    "v_cvt_pk_bf16_f32 v116, v120, v121\n\t"
    "s_add_u32 s61, %[ob], 0x10000\n\t"
    "v_cvt_pk_bf16_f32 v117, v122, v123\n\t"
    "v_cvt_pk_bf16_f32 v118, v124, v125\n\t"
    "v_cvt_pk_bf16_f32 v119, v126, v127\n\t"
    "buffer_store_dwordx4 v[112:115], v185, %[sws], s60 offen sc1\n\t"
    "buffer_store_dwordx4 v[116:119], v185, %[sws], s61 offen sc1\n\t"
    "s_nop 1\n\t"
            : "+v"(epoch) ASTAMP_OPS
            : [xo0] "v"(xo[0]), [xo1] "v"(xo[1]), [wof] "v"(wof), "{v[88:95]}"(xad), "{v[96:99]}"(wad), "{v[104:107]}"(la), "{v[108:109]}"(ix),
              "{v[180:187]}"(ea), [sx] "s"(sx), [sw] "s"(sw), [sws] "s"(sws), [c0] "s"(c0), [mb] "s"(lds0 + wv * 2048), [ob] "s"(pair * 131072 + tile * 1024)
            : "v24", "v25", "v26", "v27", "v28", "v29", "v30", "v31", "v32", "v33", "v34", "v35", "v36", "v37", "v38", "v39", "v40", "v41", "v42", "v43", "v44", "v45", "v46", "v47", "v48", "v49", "v50", "v51", "v52", "v53", "v54", "v55", "v56", "v57", "v58", "v59", "v60", "v61", "v62", "v63", "v64", "v65", "v66", "v67", "v68", "v69", "v70", "v71", "v72", "v73", "v74", "v75", "v76", "v77", "v78", "v79", "v80", "v81", "v82", "v83", "v84", "v85", "v86", "v87", "v112", "v113", "v114", "v115", "v116", "v117", "v118", "v119", "v120", "v121", "v122", "v123", "v124", "v125", "v126", "v127", "v128", "v129", "v130", "v131", "v132", "v133", "v134", "v135", "v136", "v137", "v138", "v139", "v140", "v141", "v142", "v143", "v144", "v145", "v146", "v147", "v148", "v149", "v150", "v151", "v152", "v153", "v154", "v155", "v156", "v157", "v158", "v159", "v160", "v161", "v162", "v163", "v164", "v165", "v166", "v167", "v168", "v169", "v170", "v171", "v172", "v173", "v174", "v175", "v176", "v177", "v178", "v179", "v188", "v189", "v190", "v191", "v192", "v193", "v194", "v195", "v196", "v197", "v198", "v199", "v200", "v201", "v202", "v203", "v204", "v205", "v206", "v207", "v208", "v209", "v210", "v211", "v212", "v213", "v214", "v215", "v216", "v217", "v218", "v219", "v220", "v221", "v222", "v223", "v224", "v225", "v226", "v227", "a254", "s60", "s61", "s62", "s63", "scc", "memory" ASTAMP_CLOB);
        tag = (((u64)(unsigned)__builtin_amdgcn_readfirstlane((unsigned)(epoch >> 32)) << 32) | (unsigned)__builtin_amdgcn_readfirstlane((unsigned)epoch)) + 1;
        kvflag = FL_KV + (pair * 16 + (tile & 15)) * 128 + (tile >> 4) * 32;
        STAMP(11);
    }

    u32x32 woA, woB;
    u32x8 xres;
    {
        const int lane = lane_id(), r32 = lane & 31, hh = lane >> 5, q4 = (lane & 15) >> 2;
        const unsigned scr = lds0 + LDS_SCR;
        kv = WS_K + head * 65536 + kh * 32768 + lane * 16;
        q0 = WS_Q + head * 65536 + (qt + 2 * kh) * 1024 + lane * 16;
        tv = (lane >> 3) * 4096 + ct * 128 + (lane & 7) * 16;
#pragma unroll
        for (int i = 0; i < 7; ++i) {
            const unsigned p = lane + 8 * i, ge = p >= 56, c = p - 56 * ge, hi = c >= 48;
            const unsigned q = hi ? (c >> 1) & 3 : (c >> 2) & 3, cb = (c >> 4) * 4 + (c & (hi ? 1 : 3));
            w1v[i] = __umul24(ge * 4 + q, 3584) + cb * 16 + ((wv * 128 + i * 4) * 3584 + ct4 * 224 - (i & 3) * 1024);
        }
        w1v[7] = tv + hh * (65536 - 16384);
        const unsigned t0 = q4 * 1024 + hh * 128 + (wv & 1) * 64 + ((lane >> 4) & 1) * 32 + (lane & 3) * 8;
        tr2 = (u32x4){mA + t0, mA + (t0 ^ 64), mB + t0, mB + (t0 ^ 64)};
        fl = FL_KV + ((head >> 1) * 16 + wv * 4) * 128 + (lane & 15) * 32;
        xv = (tt5 * 32 + r32) * 1024 + nt5 * 64 + hh * 8;
        ov = WS_O + ((qt + 2 * kh) * 32 + opos) * 1024 + lane * 16;
        xw = mB + lane * 16; xr = scr + (wv ^ 1) * 12288 + lane * 16;
        const unsigned o0 = WS_O + (rt64 * 64 + wv * 8) * 1024, a0 = WS_A + (rt64 * 224 + wv * 28) * 1024;
        sp = (u32x16){FL_O + ((g * 4 + wv) * 4 + (head & 3)) * 128 + (head >> 2) * 16, FL_O + (rt64 * 4 + wv) * 128,
                      o0 + (wv >> 1) * 32768, o0 + ((wv >> 1) ^ 1) * 32768,
                      WS_H + (tt5 * 32 + nt5 * 2) * 1024, WS_ST + (tt5 * 16 + nt5) * 256, FL_H + (rt64 * 8 + ct) * 128, FL_H + (tile4 >> 1) * 1024,
                      WS_H + tile4 * 32768, WS_ST + tile4 * 4096, WS_A + (tile4 * 112 + ct4 * 7) * 1024, FL_A + (tile4 * 16 + ct4) * 128,
                      FL_A + (rt64 * 32 + wv * 4) * 128, a0 + (wv >> 1) * 114688, a0 + ((wv >> 1) ^ 1) * 114688, (unsigned)wv};
        sq = (u32x4){scr + (wv ^ 1) * 12288, scr + (wv ^ 2) * 12288, scr + (wv ^ 3) * 12288, (b == 0 && wv == 0) ? FL_EPOCH : 0u};
        asm volatile("" : "+{v[16:23]}"(w1v), "+{v[12:15]}"(tr2), "+v"(kv), "+v"(q0), "+v"(tv), "+v"(fl), "+v"(xv), "+v"(ov), "+v"(xw), "+v"(xr),
                     "+{s[68:83]}"(sp), "+{s[84:87]}"(sq));
        const u32x4 sx = mksrd(x, 2048 * 1024), swo = mksrd(wo, 512 * 1024), sw1 = mksrd(w1, 512 * 3584), sw2 = mksrd(w2, 1792 * 1024);
        const unsigned twv = lane < 7 ? ((b >> 6) * 1792 + (b * 4 + wv) * 7 + lane) * 128 : 0x80000000u;
        const unsigned cdv = lane < 4 ? ((r * 4 + wv) * 4 + lane) * 128 : 0x80000000u;
        publish(ws, kvflag, tag, lane);
        STAMP(25);
        u64 bad;
        asm volatile(
    "s_mov_b32 s60, 0\n\t"
    "s_mov_b32 s61, 0x200000\n\t"
    "v_mov_b32 v112, 0\n\t"
    "v_mov_b32 v113, 0\n\t"
    "v_mov_b32 v114, 0\n\t"
    "v_mov_b32 v115, 0\n\t"
    "v_mov_b32 v116, 0\n\t"
    "v_mov_b32 v117, 0\n\t"
    "v_mov_b32 v118, 0\n\t"
    "v_mov_b32 v119, 0\n\t"
    "v_mov_b32 v120, 0\n\t"
    "v_mov_b32 v121, 0\n\t"
    "v_mov_b32 v122, 0\n\t"
    "v_mov_b32 v123, 0\n\t"
    "v_mov_b32 v124, 0\n\t"
    "v_mov_b32 v125, 0\n\t"
    "v_mov_b32 v126, 0\n\t"
    "v_mov_b32 v127, 0\n\t"
    "v_mov_b32 v128, 0\n\t"
    "v_mov_b32 v129, 0\n\t"
    "v_mov_b32 v130, 0\n\t"
    "v_mov_b32 v131, 0\n\t"
    "v_mov_b32 v132, 0\n\t"
    "v_mov_b32 v133, 0\n\t"
    "v_mov_b32 v134, 0\n\t"
    "v_mov_b32 v135, 0\n\t"
    "v_mov_b32 v136, 0\n\t"
    "v_mov_b32 v137, 0\n\t"
    "v_mov_b32 v138, 0\n\t"
    "v_mov_b32 v139, 0\n\t"
    "v_mov_b32 v140, 0\n\t"
    "v_mov_b32 v141, 0\n\t"
    "v_mov_b32 v142, 0\n\t"
    "v_mov_b32 v143, 0\n\t"
    "v_mov_b32 v144, 0\n\t"
    "v_mov_b32 v145, 0\n\t"
    "v_mov_b32 v146, 0\n\t"
    "v_mov_b32 v147, 0\n\t"
    "v_mov_b32 v148, 0\n\t"
    "v_mov_b32 v149, 0\n\t"
    "v_mov_b32 v150, 0\n\t"
    "v_mov_b32 v151, 0\n\t"
    "v_mov_b32 v152, 0\n\t"
    "v_mov_b32 v153, 0\n\t"
    "v_mov_b32 v154, 0\n\t"
    "v_mov_b32 v155, 0\n\t"
    "v_mov_b32 v156, 0\n\t"
    "v_mov_b32 v157, 0\n\t"
    "v_mov_b32 v158, 0\n\t"
    "v_mov_b32 v159, 0\n\t"
    "v_mov_b32 v160, 0\n\t"
    "v_mov_b32 v161, 0\n\t"
    "v_mov_b32 v162, 0\n\t"
    "v_mov_b32 v163, 0\n\t"
    "v_mov_b32 v164, 0\n\t"
    "v_mov_b32 v165, 0\n\t"
    "v_mov_b32 v166, 0\n\t"
    "v_mov_b32 v167, 0\n\t"
    "v_mov_b32 v168, 0\n\t"
    "v_mov_b32 v169, 0\n\t"
    "v_mov_b32 v170, 0\n\t"
    "v_mov_b32 v171, 0\n\t"
    "v_mov_b32 v172, 0\n\t"
    "v_mov_b32 v173, 0\n\t"
    "v_mov_b32 v174, 0\n\t"
    "v_mov_b32 v175, 0\n\t"
    "v_mov_b32 v240, 0\n\t"
    "v_mov_b32 v241, 0\n\t"
    "v_mov_b32 v242, 0\n\t"
    "v_mov_b32 v243, 0\n\t"
    "v_mov_b32 v244, 0\n\t"
    "v_mov_b32 v245, 0\n\t"
    "v_mov_b32 v246, 0\n\t"
    "v_mov_b32 v247, 0\n\t"
    "v_mov_b32 v248, 0\n\t"
    "v_mov_b32 v249, 0\n\t"
    "v_mov_b32 v250, 0\n\t"
    "v_mov_b32 v251, 0\n\t"
    "v_mov_b32 v252, 0\n\t"
    "v_mov_b32 v253, 0\n\t"
    "v_mov_b32 v254, 0\n\t"
    "v_mov_b32 v255, 0\n\t"
    "s_mov_b32 s63, 0x40000\n\t"
    "attn_poll:\n\t"
    "buffer_load_dwordx2 v[64:65], %[fl], %[sws], 0 offen sc1\n\t"
    "s_waitcnt vmcnt(0)\n\t"
    "v_cmp_ne_u64 vcc, %[tag], v[64:65]\n\t"
    "s_cbranch_vccz attn_go\n\t"
    "s_sub_u32 s63, s63, 1\n\t"
    "s_cbranch_scc0 attn_poll\n\t"
    "attn_go:\n\t"
    "s_barrier\n\t"
    ASTAMP(12)
    "buffer_load_dwordx4 a[240:243], %[kv], %[sws], s60 offen offset:0 sc1\n\t"
    "buffer_load_dwordx4 a[224:227], %[q0], %[sws], 0 offen offset:0 sc1\n\t"
    "s_nop 11\n\t"
    "buffer_load_dwordx4 a[228:231], %[q0], %[sws], 0 offen offset:1024 sc1\n\t"
    "s_nop 11\n\t"
    "buffer_load_dwordx4 a[232:235], %[q1], %[sws], 0 offen offset:0 sc1\n\t"
    "s_nop 11\n\t"
    "buffer_load_dwordx4 v[80:83], %[kv], %[sws], s61 offen offset:0 sc1\n\t"
    "s_nop 11\n\t"
    "buffer_load_dwordx4 a[236:239], %[q1], %[sws], 0 offen offset:1024 sc1\n\t"
    "s_nop 11\n\t"
    "buffer_load_dwordx4 a[244:247], %[kv], %[sws], s60 offen offset:1024 sc1\n\t"
    "s_nop 11\n\t"
    "buffer_load_dwordx4 v[84:87], %[kv], %[sws], s61 offen offset:1024 sc1\n\t"
    "s_nop 11\n\t"
    "s_mov_b32 s63, 0x38000\n\t"
    "s_waitcnt vmcnt(5)\n\t"
    "v_mfma_f32_32x32x16_bf16 v[24:39], a[240:243], a[224:227], 0\n\t"
    "s_nop 11\n\t"
    "v_mfma_f32_32x32x16_bf16 v[40:55], a[240:243], a[228:231], 0\n\t"
    "s_add_u32 m0, s90, 0x0\n\t"
    "v_exp_f32 v24, v24\n\t"
    "v_exp_f32 v25, v25\n\t"
    "v_exp_f32 v26, v26\n\t"
    "v_exp_f32 v27, v27\n\t"
    "v_exp_f32 v28, v28\n\t"
    "v_exp_f32 v29, v29\n\t"
    "buffer_load_dwordx4 a[248:251], %[kv], %[sws], s60 offen offset:2048 sc1\n\t"
    "v_exp_f32 v30, v30\n\t"
    "v_exp_f32 v31, v31\n\t"
    "v_exp_f32 v32, v32\n\t"
    "v_cvt_pk_bf16_f32 v56, v24, v25\n\t"
    "v_cvt_pk_bf16_f32 v57, v26, v27\n\t"
    "v_cvt_pk_bf16_f32 v58, v28, v29\n\t"
    "v_cvt_pk_bf16_f32 v59, v30, v31\n\t"
    "v_exp_f32 v33, v33\n\t"
    "v_exp_f32 v34, v34\n\t"
    "buffer_load_dwordx4 v[88:91], %[kv], %[sws], s61 offen offset:2048 sc1\n\t"
    "v_exp_f32 v35, v35\n\t"
    "v_exp_f32 v36, v36\n\t"
    "v_exp_f32 v37, v37\n\t"
    "v_exp_f32 v38, v38\n\t"
    "v_exp_f32 v39, v39\n\t"
    "v_cvt_pk_bf16_f32 v60, v32, v33\n\t"
    "v_cvt_pk_bf16_f32 v61, v34, v35\n\t"
    "v_cvt_pk_bf16_f32 v62, v36, v37\n\t"
    "v_cvt_pk_bf16_f32 v63, v38, v39\n\t"
    "s_waitcnt vmcnt(4)\n\t"
    "v_mfma_f32_32x32x16_bf16 v[24:39], a[240:243], a[232:235], 0\n\t"
    "s_add_u32 s62, s88, 0x0\n\t"
    "v_exp_f32 v40, v40\n\t"
    "v_exp_f32 v41, v41\n\t"
    "v_exp_f32 v42, v42\n\t"
    "v_exp_f32 v43, v43\n\t"
    "v_exp_f32 v44, v44\n\t"
    "v_exp_f32 v45, v45\n\t"
    "v_smfmac_f32_32x32x32_bf16 v[112:127], v[80:83], v[56:63], v108\n\t"
    "buffer_load_dwordx4 a[252:255], %[kv], %[sws], s60 offen offset:3072 sc1\n\t"
    "v_exp_f32 v46, v46\n\t"
    "v_exp_f32 v47, v47\n\t"
    "v_exp_f32 v48, v48\n\t"
    "v_cvt_pk_bf16_f32 v64, v40, v41\n\t"
    "v_cvt_pk_bf16_f32 v65, v42, v43\n\t"
    "v_cvt_pk_bf16_f32 v66, v44, v45\n\t"
    "v_cvt_pk_bf16_f32 v67, v46, v47\n\t"
    "v_exp_f32 v49, v49\n\t"
    "v_exp_f32 v50, v50\n\t"
    "v_smfmac_f32_16x16x64_bf16 v[240:243], v[104:107], v[56:63], v109\n\t"
    "buffer_load_dwordx4 v[92:95], %[kv], %[sws], s61 offen offset:3072 sc1\n\t"
    "v_exp_f32 v51, v51\n\t"
    "v_exp_f32 v52, v52\n\t"
    "v_exp_f32 v53, v53\n\t"
    "v_exp_f32 v54, v54\n\t"
    "v_exp_f32 v55, v55\n\t"
    "v_cvt_pk_bf16_f32 v68, v48, v49\n\t"
    "v_cvt_pk_bf16_f32 v69, v50, v51\n\t"
    "v_cvt_pk_bf16_f32 v70, v52, v53\n\t"
    "v_cvt_pk_bf16_f32 v71, v54, v55\n\t"
    "v_mfma_f32_32x32x16_bf16 v[40:55], a[240:243], a[236:239], 0\n\t"
    "s_waitcnt vmcnt(4)\n\t"
    "v_exp_f32 v24, v24\n\t"
    "v_exp_f32 v25, v25\n\t"
    "v_exp_f32 v26, v26\n\t"
    "v_exp_f32 v27, v27\n\t"
    "v_exp_f32 v28, v28\n\t"
    "v_exp_f32 v29, v29\n\t"
    "v_smfmac_f32_32x32x32_bf16 v[128:143], v[80:83], v[64:71], v108\n\t"
    "s_mov_b32 s60, 0x1000\n\t"
    "s_mov_b32 s61, 0x201000\n\t"
    "v_exp_f32 v30, v30\n\t"
    "v_exp_f32 v31, v31\n\t"
    "v_exp_f32 v32, v32\n\t"
    "v_cvt_pk_bf16_f32 v56, v24, v25\n\t"
    "v_cvt_pk_bf16_f32 v57, v26, v27\n\t"
    "v_cvt_pk_bf16_f32 v58, v28, v29\n\t"
    "v_cvt_pk_bf16_f32 v59, v30, v31\n\t"
    "v_exp_f32 v33, v33\n\t"
    "v_exp_f32 v34, v34\n\t"
    "v_smfmac_f32_16x16x64_bf16 v[244:247], v[104:107], v[64:71], v109\n\t"
    "buffer_load_dwordx4 v[72:75], %[kv], %[sws], s60 offen offset:0 sc1\n\t"
    "v_exp_f32 v35, v35\n\t"
    "v_exp_f32 v36, v36\n\t"
    "v_exp_f32 v37, v37\n\t"
    "v_exp_f32 v38, v38\n\t"
    "v_exp_f32 v39, v39\n\t"
    "v_cvt_pk_bf16_f32 v60, v32, v33\n\t"
    "v_cvt_pk_bf16_f32 v61, v34, v35\n\t"
    "v_cvt_pk_bf16_f32 v62, v36, v37\n\t"
    "v_cvt_pk_bf16_f32 v63, v38, v39\n\t"
    "v_mfma_f32_32x32x16_bf16 v[24:39], a[244:247], a[224:227], 0\n\t"
    "v_exp_f32 v40, v40\n\t"
    "v_exp_f32 v41, v41\n\t"
    "v_exp_f32 v42, v42\n\t"
    "v_exp_f32 v43, v43\n\t"
    "v_exp_f32 v44, v44\n\t"
    "v_exp_f32 v45, v45\n\t"
    "v_smfmac_f32_32x32x32_bf16 v[144:159], v[80:83], v[56:63], v108\n\t"
    "buffer_load_dwordx4 v[96:99], %[kv], %[sws], s61 offen offset:0 sc1\n\t"
    "v_exp_f32 v46, v46\n\t"
    "v_exp_f32 v47, v47\n\t"
    "v_exp_f32 v48, v48\n\t"
    "v_cvt_pk_bf16_f32 v64, v40, v41\n\t"
    "v_cvt_pk_bf16_f32 v65, v42, v43\n\t"
    "v_cvt_pk_bf16_f32 v66, v44, v45\n\t"
    "v_cvt_pk_bf16_f32 v67, v46, v47\n\t"
    "v_exp_f32 v49, v49\n\t"
    "v_exp_f32 v50, v50\n\t"
    "v_smfmac_f32_16x16x64_bf16 v[248:251], v[104:107], v[56:63], v109\n\t"
    "buffer_load_dwordx4 v[100:103], %[kv], %[sws], s61 offen offset:1024 sc1\n\t"
    "v_exp_f32 v51, v51\n\t"
    "v_exp_f32 v52, v52\n\t"
    "v_exp_f32 v53, v53\n\t"
    "v_exp_f32 v54, v54\n\t"
    "v_exp_f32 v55, v55\n\t"
    "v_cvt_pk_bf16_f32 v68, v48, v49\n\t"
    "v_cvt_pk_bf16_f32 v69, v50, v51\n\t"
    "v_cvt_pk_bf16_f32 v70, v52, v53\n\t"
    "v_cvt_pk_bf16_f32 v71, v54, v55\n\t"
    "v_mfma_f32_32x32x16_bf16 v[40:55], a[244:247], a[228:231], 0\n\t"
    "v_exp_f32 v24, v24\n\t"
    "v_exp_f32 v25, v25\n\t"
    "v_exp_f32 v26, v26\n\t"
    "v_exp_f32 v27, v27\n\t"
    "v_exp_f32 v28, v28\n\t"
    "v_exp_f32 v29, v29\n\t"
    "v_smfmac_f32_32x32x32_bf16 v[160:175], v[80:83], v[64:71], v108\n\t"
    "buffer_load_dwordx4 v[76:79], %[kv], %[sws], s60 offen offset:1024 sc1\n\t"
    "v_exp_f32 v30, v30\n\t"
    "v_exp_f32 v31, v31\n\t"
    "v_exp_f32 v32, v32\n\t"
    "v_cvt_pk_bf16_f32 v56, v24, v25\n\t"
    "v_cvt_pk_bf16_f32 v57, v26, v27\n\t"
    "v_cvt_pk_bf16_f32 v58, v28, v29\n\t"
    "v_cvt_pk_bf16_f32 v59, v30, v31\n\t"
    "v_exp_f32 v33, v33\n\t"
    "v_exp_f32 v34, v34\n\t"
    "v_smfmac_f32_16x16x64_bf16 v[252:255], v[104:107], v[64:71], v109\n\t"
    "buffer_load_dwordx4 v[80:83], %[kv], %[sws], s61 offen offset:2048 sc1\n\t"
    "v_exp_f32 v35, v35\n\t"
    "v_exp_f32 v36, v36\n\t"
    "v_exp_f32 v37, v37\n\t"
    "v_exp_f32 v38, v38\n\t"
    "v_exp_f32 v39, v39\n\t"
    "v_cvt_pk_bf16_f32 v60, v32, v33\n\t"
    "v_cvt_pk_bf16_f32 v61, v34, v35\n\t"
    "v_cvt_pk_bf16_f32 v62, v36, v37\n\t"
    "v_cvt_pk_bf16_f32 v63, v38, v39\n\t"
    "v_mfma_f32_32x32x16_bf16 v[24:39], a[244:247], a[232:235], 0\n\t"
    "v_exp_f32 v40, v40\n\t"
    "v_exp_f32 v41, v41\n\t"
    "v_exp_f32 v42, v42\n\t"
    "v_exp_f32 v43, v43\n\t"
    "v_exp_f32 v44, v44\n\t"
    "v_exp_f32 v45, v45\n\t"
    "v_smfmac_f32_32x32x32_bf16 v[112:127], v[84:87], v[56:63], v108\n\t"
    "buffer_load_dwordx4 a[240:243], %[kv], %[sws], s60 offen offset:2048 sc1\n\t"
    "v_exp_f32 v46, v46\n\t"
    "v_exp_f32 v47, v47\n\t"
    "v_exp_f32 v48, v48\n\t"
    "v_cvt_pk_bf16_f32 v64, v40, v41\n\t"
    "v_cvt_pk_bf16_f32 v65, v42, v43\n\t"
    "v_cvt_pk_bf16_f32 v66, v44, v45\n\t"
    "v_cvt_pk_bf16_f32 v67, v46, v47\n\t"
    "v_exp_f32 v49, v49\n\t"
    "v_exp_f32 v50, v50\n\t"
    "v_smfmac_f32_16x16x64_bf16 v[240:243], v[104:107], v[56:63], v109\n\t"
    "buffer_load_dword %[fl], %[twv], %[swo], 0 offen\n\t"
    "v_exp_f32 v51, v51\n\t"
    "v_exp_f32 v52, v52\n\t"
    "v_exp_f32 v53, v53\n\t"
    "v_exp_f32 v54, v54\n\t"
    "v_exp_f32 v55, v55\n\t"
    "v_cvt_pk_bf16_f32 v68, v48, v49\n\t"
    "v_cvt_pk_bf16_f32 v69, v50, v51\n\t"
    "v_cvt_pk_bf16_f32 v70, v52, v53\n\t"
    "v_cvt_pk_bf16_f32 v71, v54, v55\n\t"
    "v_mfma_f32_32x32x16_bf16 v[40:55], a[244:247], a[236:239], 0\n\t"
    "s_waitcnt vmcnt(9)\n\t"
    "v_exp_f32 v24, v24\n\t"
    "v_exp_f32 v25, v25\n\t"
    "v_exp_f32 v26, v26\n\t"
    "v_exp_f32 v27, v27\n\t"
    "v_exp_f32 v28, v28\n\t"
    "v_exp_f32 v29, v29\n\t"
    "v_smfmac_f32_32x32x32_bf16 v[128:143], v[84:87], v[64:71], v108\n\t"
    "v_exp_f32 v30, v30\n\t"
    "v_exp_f32 v31, v31\n\t"
    "v_exp_f32 v32, v32\n\t"
    "v_cvt_pk_bf16_f32 v56, v24, v25\n\t"
    "v_cvt_pk_bf16_f32 v57, v26, v27\n\t"
    "v_cvt_pk_bf16_f32 v58, v28, v29\n\t"
    "v_cvt_pk_bf16_f32 v59, v30, v31\n\t"
    "v_exp_f32 v33, v33\n\t"
    "v_exp_f32 v34, v34\n\t"
    "v_smfmac_f32_16x16x64_bf16 v[244:247], v[104:107], v[64:71], v109\n\t"
    "buffer_load_dword %[fl], %[twv], %[swo], s63 offen\n\t"
    "v_exp_f32 v35, v35\n\t"
    "v_exp_f32 v36, v36\n\t"
    "v_exp_f32 v37, v37\n\t"
    "v_exp_f32 v38, v38\n\t"
    "v_exp_f32 v39, v39\n\t"
    "v_cvt_pk_bf16_f32 v60, v32, v33\n\t"
    "v_cvt_pk_bf16_f32 v61, v34, v35\n\t"
    "v_cvt_pk_bf16_f32 v62, v36, v37\n\t"
    "v_cvt_pk_bf16_f32 v63, v38, v39\n\t"
    "v_mfma_f32_32x32x16_bf16 v[24:39], a[248:251], a[224:227], 0\n\t"
    "v_exp_f32 v40, v40\n\t"
    "v_exp_f32 v41, v41\n\t"
    "v_exp_f32 v42, v42\n\t"
    "v_exp_f32 v43, v43\n\t"
    "v_exp_f32 v44, v44\n\t"
    "v_exp_f32 v45, v45\n\t"
    "v_smfmac_f32_32x32x32_bf16 v[144:159], v[84:87], v[56:63], v108\n\t"
    "v_exp_f32 v46, v46\n\t"
    "v_exp_f32 v47, v47\n\t"
    "v_exp_f32 v48, v48\n\t"
    "v_cvt_pk_bf16_f32 v64, v40, v41\n\t"
    "v_cvt_pk_bf16_f32 v65, v42, v43\n\t"
    "v_cvt_pk_bf16_f32 v66, v44, v45\n\t"
    "v_cvt_pk_bf16_f32 v67, v46, v47\n\t"
    "v_exp_f32 v49, v49\n\t"
    "v_exp_f32 v50, v50\n\t"
    "v_smfmac_f32_16x16x64_bf16 v[248:251], v[104:107], v[56:63], v109\n\t"
    "v_exp_f32 v51, v51\n\t"
    "v_exp_f32 v52, v52\n\t"
    "v_exp_f32 v53, v53\n\t"
    "v_exp_f32 v54, v54\n\t"
    "v_exp_f32 v55, v55\n\t"
    "v_cvt_pk_bf16_f32 v68, v48, v49\n\t"
    "v_cvt_pk_bf16_f32 v69, v50, v51\n\t"
    "v_cvt_pk_bf16_f32 v70, v52, v53\n\t"
    "v_cvt_pk_bf16_f32 v71, v54, v55\n\t"
    "v_mfma_f32_32x32x16_bf16 v[40:55], a[248:251], a[228:231], 0\n\t"
    "s_waitcnt vmcnt(8)\n\t"
    "v_exp_f32 v24, v24\n\t"
    "v_exp_f32 v25, v25\n\t"
    "v_exp_f32 v26, v26\n\t"
    "v_exp_f32 v27, v27\n\t"
    "v_exp_f32 v28, v28\n\t"
    "v_exp_f32 v29, v29\n\t"
    "v_smfmac_f32_32x32x32_bf16 v[160:175], v[84:87], v[64:71], v108\n\t"
    "buffer_load_dwordx4 v[84:87], %[kv], %[sws], s61 offen offset:3072 sc1\n\t"
    "v_exp_f32 v30, v30\n\t"
    "v_exp_f32 v31, v31\n\t"
    "v_exp_f32 v32, v32\n\t"
    "v_cvt_pk_bf16_f32 v56, v24, v25\n\t"
    "v_cvt_pk_bf16_f32 v57, v26, v27\n\t"
    "v_cvt_pk_bf16_f32 v58, v28, v29\n\t"
    "v_cvt_pk_bf16_f32 v59, v30, v31\n\t"
    "v_exp_f32 v33, v33\n\t"
    "v_exp_f32 v34, v34\n\t"
    "v_smfmac_f32_16x16x64_bf16 v[252:255], v[104:107], v[64:71], v109\n\t"
    "buffer_load_dword %[fl], %[cdv], s[92:95], 0 offen\n\t"
    "v_exp_f32 v35, v35\n\t"
    "v_exp_f32 v36, v36\n\t"
    "v_exp_f32 v37, v37\n\t"
    "v_exp_f32 v38, v38\n\t"
    "v_exp_f32 v39, v39\n\t"
    "v_cvt_pk_bf16_f32 v60, v32, v33\n\t"
    "v_cvt_pk_bf16_f32 v61, v34, v35\n\t"
    "v_cvt_pk_bf16_f32 v62, v36, v37\n\t"
    "v_cvt_pk_bf16_f32 v63, v38, v39\n\t"
    "v_mfma_f32_32x32x16_bf16 v[24:39], a[248:251], a[232:235], 0\n\t"
    "v_exp_f32 v40, v40\n\t"
    "v_exp_f32 v41, v41\n\t"
    "v_exp_f32 v42, v42\n\t"
    "v_exp_f32 v43, v43\n\t"
    "v_exp_f32 v44, v44\n\t"
    "v_exp_f32 v45, v45\n\t"
    "v_smfmac_f32_32x32x32_bf16 v[112:127], v[88:91], v[56:63], v108\n\t"
    "buffer_load_dwordx4 a[244:247], %[kv], %[sws], s60 offen offset:3072 sc1\n\t"
    "v_exp_f32 v46, v46\n\t"
    "v_exp_f32 v47, v47\n\t"
    "v_exp_f32 v48, v48\n\t"
    "v_cvt_pk_bf16_f32 v64, v40, v41\n\t"
    "v_cvt_pk_bf16_f32 v65, v42, v43\n\t"
    "v_cvt_pk_bf16_f32 v66, v44, v45\n\t"
    "v_cvt_pk_bf16_f32 v67, v46, v47\n\t"
    "v_exp_f32 v49, v49\n\t"
    "v_exp_f32 v50, v50\n\t"
    "v_smfmac_f32_16x16x64_bf16 v[240:243], v[104:107], v[56:63], v109\n\t"
    "v_exp_f32 v51, v51\n\t"
    "v_exp_f32 v52, v52\n\t"
    "v_exp_f32 v53, v53\n\t"
    "v_exp_f32 v54, v54\n\t"
    "v_exp_f32 v55, v55\n\t"
    "v_cvt_pk_bf16_f32 v68, v48, v49\n\t"
    "v_cvt_pk_bf16_f32 v69, v50, v51\n\t"
    "v_cvt_pk_bf16_f32 v70, v52, v53\n\t"
    "v_cvt_pk_bf16_f32 v71, v54, v55\n\t"
    "v_mfma_f32_32x32x16_bf16 v[40:55], a[248:251], a[236:239], 0\n\t"
    "s_mov_b32 s61, 0x202000\n\t"
    "v_exp_f32 v24, v24\n\t"
    "v_exp_f32 v25, v25\n\t"
    "v_exp_f32 v26, v26\n\t"
    "v_exp_f32 v27, v27\n\t"
    "v_exp_f32 v28, v28\n\t"
    "v_exp_f32 v29, v29\n\t"
    "v_smfmac_f32_32x32x32_bf16 v[128:143], v[88:91], v[64:71], v108\n\t"
    "s_mov_b32 s60, 0x2000\n\t"
    "v_exp_f32 v30, v30\n\t"
    "v_exp_f32 v31, v31\n\t"
    "v_exp_f32 v32, v32\n\t"
    "v_cvt_pk_bf16_f32 v56, v24, v25\n\t"
    "v_cvt_pk_bf16_f32 v57, v26, v27\n\t"
    "v_cvt_pk_bf16_f32 v58, v28, v29\n\t"
    "v_cvt_pk_bf16_f32 v59, v30, v31\n\t"
    "v_exp_f32 v33, v33\n\t"
    "v_exp_f32 v34, v34\n\t"
    "v_smfmac_f32_16x16x64_bf16 v[244:247], v[104:107], v[64:71], v109\n\t"
    "buffer_load_dword %[fl], %[twv], %[sw2], 0 offen\n\t"
    "v_exp_f32 v35, v35\n\t"
    "v_exp_f32 v36, v36\n\t"
    "v_exp_f32 v37, v37\n\t"
    "v_exp_f32 v38, v38\n\t"
    "v_exp_f32 v39, v39\n\t"
    "v_cvt_pk_bf16_f32 v60, v32, v33\n\t"
    "v_cvt_pk_bf16_f32 v61, v34, v35\n\t"
    "v_cvt_pk_bf16_f32 v62, v36, v37\n\t"
    "v_cvt_pk_bf16_f32 v63, v38, v39\n\t"
    "v_mfma_f32_32x32x16_bf16 v[24:39], a[252:255], a[224:227], 0\n\t"
    "v_exp_f32 v40, v40\n\t"
    "v_exp_f32 v41, v41\n\t"
    "v_exp_f32 v42, v42\n\t"
    "v_exp_f32 v43, v43\n\t"
    "v_exp_f32 v44, v44\n\t"
    "v_exp_f32 v45, v45\n\t"
    "v_smfmac_f32_32x32x32_bf16 v[144:159], v[88:91], v[56:63], v108\n\t"
    "v_exp_f32 v46, v46\n\t"
    "v_exp_f32 v47, v47\n\t"
    "v_exp_f32 v48, v48\n\t"
    "v_cvt_pk_bf16_f32 v64, v40, v41\n\t"
    "v_cvt_pk_bf16_f32 v65, v42, v43\n\t"
    "v_cvt_pk_bf16_f32 v66, v44, v45\n\t"
    "v_cvt_pk_bf16_f32 v67, v46, v47\n\t"
    "v_exp_f32 v49, v49\n\t"
    "v_exp_f32 v50, v50\n\t"
    "v_smfmac_f32_16x16x64_bf16 v[248:251], v[104:107], v[56:63], v109\n\t"
    "v_exp_f32 v51, v51\n\t"
    "v_exp_f32 v52, v52\n\t"
    "v_exp_f32 v53, v53\n\t"
    "v_exp_f32 v54, v54\n\t"
    "v_exp_f32 v55, v55\n\t"
    "v_cvt_pk_bf16_f32 v68, v48, v49\n\t"
    "v_cvt_pk_bf16_f32 v69, v50, v51\n\t"
    "v_cvt_pk_bf16_f32 v70, v52, v53\n\t"
    "v_cvt_pk_bf16_f32 v71, v54, v55\n\t"
    "v_mfma_f32_32x32x16_bf16 v[40:55], a[252:255], a[228:231], 0\n\t"
    "s_waitcnt vmcnt(10)\n\t"
    "v_exp_f32 v24, v24\n\t"
    "v_exp_f32 v25, v25\n\t"
    "v_exp_f32 v26, v26\n\t"
    "v_exp_f32 v27, v27\n\t"
    "v_exp_f32 v28, v28\n\t"
    "v_exp_f32 v29, v29\n\t"
    "v_smfmac_f32_32x32x32_bf16 v[160:175], v[88:91], v[64:71], v108\n\t"
    "buffer_load_dwordx4 v[88:91], %[kv], %[sws], s61 offen offset:0 sc1\n\t"
    "v_exp_f32 v30, v30\n\t"
    "v_exp_f32 v31, v31\n\t"
    "v_exp_f32 v32, v32\n\t"
    "v_cvt_pk_bf16_f32 v56, v24, v25\n\t"
    "v_cvt_pk_bf16_f32 v57, v26, v27\n\t"
    "v_cvt_pk_bf16_f32 v58, v28, v29\n\t"
    "v_cvt_pk_bf16_f32 v59, v30, v31\n\t"
    "v_exp_f32 v33, v33\n\t"
    "v_exp_f32 v34, v34\n\t"
    "v_smfmac_f32_16x16x64_bf16 v[252:255], v[104:107], v[64:71], v109\n\t"
    "buffer_load_dwordx4 v23, %[swo], s62 offen offset:0 lds\n\t"
    "v_exp_f32 v35, v35\n\t"
    "v_exp_f32 v36, v36\n\t"
    "v_exp_f32 v37, v37\n\t"
    "v_exp_f32 v38, v38\n\t"
    "v_exp_f32 v39, v39\n\t"
    "v_cvt_pk_bf16_f32 v60, v32, v33\n\t"
    "v_cvt_pk_bf16_f32 v61, v34, v35\n\t"
    "v_cvt_pk_bf16_f32 v62, v36, v37\n\t"
    "v_cvt_pk_bf16_f32 v63, v38, v39\n\t"
    "v_mfma_f32_32x32x16_bf16 v[24:39], a[252:255], a[232:235], 0\n\t"
    "v_exp_f32 v40, v40\n\t"
    "v_exp_f32 v41, v41\n\t"
    "v_exp_f32 v42, v42\n\t"
    "v_exp_f32 v43, v43\n\t"
    "v_exp_f32 v44, v44\n\t"
    "v_exp_f32 v45, v45\n\t"
    "v_smfmac_f32_32x32x32_bf16 v[112:127], v[92:95], v[56:63], v108\n\t"
    "buffer_load_dwordx4 a[248:251], %[kv], %[sws], s60 offen offset:0 sc1\n\t"
    "v_exp_f32 v46, v46\n\t"
    "v_exp_f32 v47, v47\n\t"
    "v_exp_f32 v48, v48\n\t"
    "v_cvt_pk_bf16_f32 v64, v40, v41\n\t"
    "v_cvt_pk_bf16_f32 v65, v42, v43\n\t"
    "v_cvt_pk_bf16_f32 v66, v44, v45\n\t"
    "v_cvt_pk_bf16_f32 v67, v46, v47\n\t"
    "v_exp_f32 v49, v49\n\t"
    "v_exp_f32 v50, v50\n\t"
    "v_smfmac_f32_16x16x64_bf16 v[240:243], v[104:107], v[56:63], v109\n\t"
    "buffer_load_dwordx4 v23, %[swo], s62 offen offset:1024 lds\n\t"
    "v_exp_f32 v51, v51\n\t"
    "v_exp_f32 v52, v52\n\t"
    "v_exp_f32 v53, v53\n\t"
    "v_exp_f32 v54, v54\n\t"
    "v_exp_f32 v55, v55\n\t"
    "v_cvt_pk_bf16_f32 v68, v48, v49\n\t"
    "v_cvt_pk_bf16_f32 v69, v50, v51\n\t"
    "v_cvt_pk_bf16_f32 v70, v52, v53\n\t"
    "v_cvt_pk_bf16_f32 v71, v54, v55\n\t"
    "v_mfma_f32_32x32x16_bf16 v[40:55], a[252:255], a[236:239], 0\n\t"
    "v_exp_f32 v24, v24\n\t"
    "v_exp_f32 v25, v25\n\t"
    "v_exp_f32 v26, v26\n\t"
    "v_exp_f32 v27, v27\n\t"
    "v_exp_f32 v28, v28\n\t"
    "v_exp_f32 v29, v29\n\t"
    "v_smfmac_f32_32x32x32_bf16 v[128:143], v[92:95], v[64:71], v108\n\t"
    "buffer_load_dwordx4 v23, %[swo], s62 offen offset:2048 lds\n\t"
    "v_exp_f32 v30, v30\n\t"
    "v_exp_f32 v31, v31\n\t"
    "v_exp_f32 v32, v32\n\t"
    "v_cvt_pk_bf16_f32 v56, v24, v25\n\t"
    "v_cvt_pk_bf16_f32 v57, v26, v27\n\t"
    "v_cvt_pk_bf16_f32 v58, v28, v29\n\t"
    "v_cvt_pk_bf16_f32 v59, v30, v31\n\t"
    "v_exp_f32 v33, v33\n\t"
    "v_exp_f32 v34, v34\n\t"
    "v_smfmac_f32_16x16x64_bf16 v[244:247], v[104:107], v[64:71], v109\n\t"
    "buffer_load_dwordx4 v23, %[swo], s62 offen offset:3072 lds\n\t"
    "v_exp_f32 v35, v35\n\t"
    "v_exp_f32 v36, v36\n\t"
    "v_exp_f32 v37, v37\n\t"
    "v_exp_f32 v38, v38\n\t"
    "v_exp_f32 v39, v39\n\t"
    "v_cvt_pk_bf16_f32 v60, v32, v33\n\t"
    "v_cvt_pk_bf16_f32 v61, v34, v35\n\t"
    "v_cvt_pk_bf16_f32 v62, v36, v37\n\t"
    "v_cvt_pk_bf16_f32 v63, v38, v39\n\t"
    "v_mfma_f32_32x32x16_bf16 v[24:39], v[72:75], a[224:227], 0\n\t"
    "s_add_u32 m0, s90, 0x1000\n\t"
    "v_exp_f32 v40, v40\n\t"
    "v_exp_f32 v41, v41\n\t"
    "v_exp_f32 v42, v42\n\t"
    "v_exp_f32 v43, v43\n\t"
    "v_exp_f32 v44, v44\n\t"
    "v_exp_f32 v45, v45\n\t"
    "v_smfmac_f32_32x32x32_bf16 v[144:159], v[92:95], v[56:63], v108\n\t"
    "s_add_u32 s62, s88, 0x20000\n\t"
    "v_exp_f32 v46, v46\n\t"
    "v_exp_f32 v47, v47\n\t"
    "v_exp_f32 v48, v48\n\t"
    "v_cvt_pk_bf16_f32 v64, v40, v41\n\t"
    "v_cvt_pk_bf16_f32 v65, v42, v43\n\t"
    "v_cvt_pk_bf16_f32 v66, v44, v45\n\t"
    "v_cvt_pk_bf16_f32 v67, v46, v47\n\t"
    "v_exp_f32 v49, v49\n\t"
    "v_exp_f32 v50, v50\n\t"
    "v_smfmac_f32_16x16x64_bf16 v[248:251], v[104:107], v[56:63], v109\n\t"
    "buffer_load_dwordx4 v23, %[swo], s62 offen offset:0 lds\n\t"
    "v_exp_f32 v51, v51\n\t"
    "v_exp_f32 v52, v52\n\t"
    "v_exp_f32 v53, v53\n\t"
    "v_exp_f32 v54, v54\n\t"
    "v_exp_f32 v55, v55\n\t"
    "v_cvt_pk_bf16_f32 v68, v48, v49\n\t"
    "v_cvt_pk_bf16_f32 v69, v50, v51\n\t"
    "v_cvt_pk_bf16_f32 v70, v52, v53\n\t"
    "v_cvt_pk_bf16_f32 v71, v54, v55\n\t"
    "v_mfma_f32_32x32x16_bf16 v[40:55], v[72:75], a[228:231], 0\n\t"
    "s_waitcnt vmcnt(15)\n\t"
    "v_exp_f32 v24, v24\n\t"
    "v_exp_f32 v25, v25\n\t"
    "v_exp_f32 v26, v26\n\t"
    "v_exp_f32 v27, v27\n\t"
    "v_exp_f32 v28, v28\n\t"
    "v_exp_f32 v29, v29\n\t"
    "v_smfmac_f32_32x32x32_bf16 v[160:175], v[92:95], v[64:71], v108\n\t"
    "buffer_load_dwordx4 v[92:95], %[kv], %[sws], s61 offen offset:1024 sc1\n\t"
    "v_exp_f32 v30, v30\n\t"
    "v_exp_f32 v31, v31\n\t"
    "v_exp_f32 v32, v32\n\t"
    "v_cvt_pk_bf16_f32 v56, v24, v25\n\t"
    "v_cvt_pk_bf16_f32 v57, v26, v27\n\t"
    "v_cvt_pk_bf16_f32 v58, v28, v29\n\t"
    "v_cvt_pk_bf16_f32 v59, v30, v31\n\t"
    "v_exp_f32 v33, v33\n\t"
    "v_exp_f32 v34, v34\n\t"
    "v_smfmac_f32_16x16x64_bf16 v[252:255], v[104:107], v[64:71], v109\n\t"
    "buffer_load_dwordx4 v23, %[swo], s62 offen offset:1024 lds\n\t"
    "v_exp_f32 v35, v35\n\t"
    "v_exp_f32 v36, v36\n\t"
    "v_exp_f32 v37, v37\n\t"
    "v_exp_f32 v38, v38\n\t"
    "v_exp_f32 v39, v39\n\t"
    "v_cvt_pk_bf16_f32 v60, v32, v33\n\t"
    "v_cvt_pk_bf16_f32 v61, v34, v35\n\t"
    "v_cvt_pk_bf16_f32 v62, v36, v37\n\t"
    "v_cvt_pk_bf16_f32 v63, v38, v39\n\t"
    "v_mfma_f32_32x32x16_bf16 v[24:39], v[72:75], a[232:235], 0\n\t"
    "v_exp_f32 v40, v40\n\t"
    "v_exp_f32 v41, v41\n\t"
    "v_exp_f32 v42, v42\n\t"
    "v_exp_f32 v43, v43\n\t"
    "v_exp_f32 v44, v44\n\t"
    "v_exp_f32 v45, v45\n\t"
    "v_smfmac_f32_32x32x32_bf16 v[112:127], v[96:99], v[56:63], v108\n\t"
    "buffer_load_dwordx4 a[252:255], %[kv], %[sws], s60 offen offset:1024 sc1\n\t"
    "v_exp_f32 v46, v46\n\t"
    "v_exp_f32 v47, v47\n\t"
    "v_exp_f32 v48, v48\n\t"
    "v_cvt_pk_bf16_f32 v64, v40, v41\n\t"
    "v_cvt_pk_bf16_f32 v65, v42, v43\n\t"
    "v_cvt_pk_bf16_f32 v66, v44, v45\n\t"
    "v_cvt_pk_bf16_f32 v67, v46, v47\n\t"
    "v_exp_f32 v49, v49\n\t"
    "v_exp_f32 v50, v50\n\t"
    "v_smfmac_f32_16x16x64_bf16 v[240:243], v[104:107], v[56:63], v109\n\t"
    "buffer_load_dwordx4 v23, %[swo], s62 offen offset:2048 lds\n\t"
    "v_exp_f32 v51, v51\n\t"
    "v_exp_f32 v52, v52\n\t"
    "v_exp_f32 v53, v53\n\t"
    "v_exp_f32 v54, v54\n\t"
    "v_exp_f32 v55, v55\n\t"
    "v_cvt_pk_bf16_f32 v68, v48, v49\n\t"
    "v_cvt_pk_bf16_f32 v69, v50, v51\n\t"
    "v_cvt_pk_bf16_f32 v70, v52, v53\n\t"
    "v_cvt_pk_bf16_f32 v71, v54, v55\n\t"
    "v_mfma_f32_32x32x16_bf16 v[40:55], v[72:75], a[236:239], 0\n\t"
    "v_exp_f32 v24, v24\n\t"
    "v_exp_f32 v25, v25\n\t"
    "v_exp_f32 v26, v26\n\t"
    "v_exp_f32 v27, v27\n\t"
    "v_exp_f32 v28, v28\n\t"
    "v_exp_f32 v29, v29\n\t"
    "v_smfmac_f32_32x32x32_bf16 v[128:143], v[96:99], v[64:71], v108\n\t"
    "buffer_load_dwordx4 v23, %[swo], s62 offen offset:3072 lds\n\t"
    "v_exp_f32 v30, v30\n\t"
    "v_exp_f32 v31, v31\n\t"
    "v_exp_f32 v32, v32\n\t"
    "v_cvt_pk_bf16_f32 v56, v24, v25\n\t"
    "v_cvt_pk_bf16_f32 v57, v26, v27\n\t"
    "v_cvt_pk_bf16_f32 v58, v28, v29\n\t"
    "v_cvt_pk_bf16_f32 v59, v30, v31\n\t"
    "v_exp_f32 v33, v33\n\t"
    "v_exp_f32 v34, v34\n\t"
    "v_smfmac_f32_16x16x64_bf16 v[244:247], v[104:107], v[64:71], v109\n\t"
    "s_add_u32 m0, s90, 0x2000\n\t"
    "s_add_u32 s62, s88, 0x40000\n\t"
    "v_exp_f32 v35, v35\n\t"
    "v_exp_f32 v36, v36\n\t"
    "v_exp_f32 v37, v37\n\t"
    "v_exp_f32 v38, v38\n\t"
    "v_exp_f32 v39, v39\n\t"
    "v_cvt_pk_bf16_f32 v60, v32, v33\n\t"
    "v_cvt_pk_bf16_f32 v61, v34, v35\n\t"
    "v_cvt_pk_bf16_f32 v62, v36, v37\n\t"
    "v_cvt_pk_bf16_f32 v63, v38, v39\n\t"
    "v_mfma_f32_32x32x16_bf16 v[24:39], v[76:79], a[224:227], 0\n\t"
    "v_exp_f32 v40, v40\n\t"
    "v_exp_f32 v41, v41\n\t"
    "v_exp_f32 v42, v42\n\t"
    "v_exp_f32 v43, v43\n\t"
    "v_exp_f32 v44, v44\n\t"
    "v_exp_f32 v45, v45\n\t"
    "v_smfmac_f32_32x32x32_bf16 v[144:159], v[96:99], v[56:63], v108\n\t"
    "buffer_load_dwordx4 v23, %[swo], s62 offen offset:0 lds\n\t"
    "v_exp_f32 v46, v46\n\t"
    "v_exp_f32 v47, v47\n\t"
    "v_exp_f32 v48, v48\n\t"
    "v_cvt_pk_bf16_f32 v64, v40, v41\n\t"
    "v_cvt_pk_bf16_f32 v65, v42, v43\n\t"
    "v_cvt_pk_bf16_f32 v66, v44, v45\n\t"
    "v_cvt_pk_bf16_f32 v67, v46, v47\n\t"
    "v_exp_f32 v49, v49\n\t"
    "v_exp_f32 v50, v50\n\t"
    "v_smfmac_f32_16x16x64_bf16 v[248:251], v[104:107], v[56:63], v109\n\t"
    "buffer_load_dwordx4 v23, %[swo], s62 offen offset:1024 lds\n\t"
    "v_exp_f32 v51, v51\n\t"
    "v_exp_f32 v52, v52\n\t"
    "v_exp_f32 v53, v53\n\t"
    "v_exp_f32 v54, v54\n\t"
    "v_exp_f32 v55, v55\n\t"
    "v_cvt_pk_bf16_f32 v68, v48, v49\n\t"
    "v_cvt_pk_bf16_f32 v69, v50, v51\n\t"
    "v_cvt_pk_bf16_f32 v70, v52, v53\n\t"
    "v_cvt_pk_bf16_f32 v71, v54, v55\n\t"
    "v_mfma_f32_32x32x16_bf16 v[40:55], v[76:79], a[228:231], 0\n\t"
    "s_waitcnt vmcnt(20)\n\t"
    "v_exp_f32 v24, v24\n\t"
    "v_exp_f32 v25, v25\n\t"
    "v_exp_f32 v26, v26\n\t"
    "v_exp_f32 v27, v27\n\t"
    "v_exp_f32 v28, v28\n\t"
    "v_exp_f32 v29, v29\n\t"
    "v_smfmac_f32_32x32x32_bf16 v[160:175], v[96:99], v[64:71], v108\n\t"
    "buffer_load_dwordx4 v[96:99], %[kv], %[sws], s61 offen offset:2048 sc1\n\t"
    "v_exp_f32 v30, v30\n\t"
    "v_exp_f32 v31, v31\n\t"
    "v_exp_f32 v32, v32\n\t"
    "v_cvt_pk_bf16_f32 v56, v24, v25\n\t"
    "v_cvt_pk_bf16_f32 v57, v26, v27\n\t"
    "v_cvt_pk_bf16_f32 v58, v28, v29\n\t"
    "v_cvt_pk_bf16_f32 v59, v30, v31\n\t"
    "v_exp_f32 v33, v33\n\t"
    "v_exp_f32 v34, v34\n\t"
    "v_smfmac_f32_16x16x64_bf16 v[252:255], v[104:107], v[64:71], v109\n\t"
    "buffer_load_dwordx4 v23, %[swo], s62 offen offset:2048 lds\n\t"
    "v_exp_f32 v35, v35\n\t"
    "v_exp_f32 v36, v36\n\t"
    "v_exp_f32 v37, v37\n\t"
    "v_exp_f32 v38, v38\n\t"
    "v_exp_f32 v39, v39\n\t"
    "v_cvt_pk_bf16_f32 v60, v32, v33\n\t"
    "v_cvt_pk_bf16_f32 v61, v34, v35\n\t"
    "v_cvt_pk_bf16_f32 v62, v36, v37\n\t"
    "v_cvt_pk_bf16_f32 v63, v38, v39\n\t"
    "v_mfma_f32_32x32x16_bf16 v[24:39], v[76:79], a[232:235], 0\n\t"
    "v_exp_f32 v40, v40\n\t"
    "v_exp_f32 v41, v41\n\t"
    "v_exp_f32 v42, v42\n\t"
    "v_exp_f32 v43, v43\n\t"
    "v_exp_f32 v44, v44\n\t"
    "v_exp_f32 v45, v45\n\t"
    "v_smfmac_f32_32x32x32_bf16 v[112:127], v[100:103], v[56:63], v108\n\t"
    "buffer_load_dwordx4 v[72:75], %[kv], %[sws], s60 offen offset:2048 sc1\n\t"
    "v_exp_f32 v46, v46\n\t"
    "v_exp_f32 v47, v47\n\t"
    "v_exp_f32 v48, v48\n\t"
    "v_cvt_pk_bf16_f32 v64, v40, v41\n\t"
    "v_cvt_pk_bf16_f32 v65, v42, v43\n\t"
    "v_cvt_pk_bf16_f32 v66, v44, v45\n\t"
    "v_cvt_pk_bf16_f32 v67, v46, v47\n\t"
    "v_exp_f32 v49, v49\n\t"
    "v_exp_f32 v50, v50\n\t"
    "v_smfmac_f32_16x16x64_bf16 v[240:243], v[104:107], v[56:63], v109\n\t"
    "buffer_load_dwordx4 v23, %[swo], s62 offen offset:3072 lds\n\t"
    "v_exp_f32 v51, v51\n\t"
    "v_exp_f32 v52, v52\n\t"
    "v_exp_f32 v53, v53\n\t"
    "v_exp_f32 v54, v54\n\t"
    "v_exp_f32 v55, v55\n\t"
    "v_cvt_pk_bf16_f32 v68, v48, v49\n\t"
    "v_cvt_pk_bf16_f32 v69, v50, v51\n\t"
    "v_cvt_pk_bf16_f32 v70, v52, v53\n\t"
    "v_cvt_pk_bf16_f32 v71, v54, v55\n\t"
    "v_mfma_f32_32x32x16_bf16 v[40:55], v[76:79], a[236:239], 0\n\t"
    "s_add_u32 m0, s90, 0x3000\n\t"
    "v_exp_f32 v24, v24\n\t"
    "v_exp_f32 v25, v25\n\t"
    "v_exp_f32 v26, v26\n\t"
    "v_exp_f32 v27, v27\n\t"
    "v_exp_f32 v28, v28\n\t"
    "v_exp_f32 v29, v29\n\t"
    "v_smfmac_f32_32x32x32_bf16 v[128:143], v[100:103], v[64:71], v108\n\t"
    "s_add_u32 s62, s88, 0x60000\n\t"
    "v_exp_f32 v30, v30\n\t"
    "v_exp_f32 v31, v31\n\t"
    "v_exp_f32 v32, v32\n\t"
    "v_cvt_pk_bf16_f32 v56, v24, v25\n\t"
    "v_cvt_pk_bf16_f32 v57, v26, v27\n\t"
    "v_cvt_pk_bf16_f32 v58, v28, v29\n\t"
    "v_cvt_pk_bf16_f32 v59, v30, v31\n\t"
    "v_exp_f32 v33, v33\n\t"
    "v_exp_f32 v34, v34\n\t"
    "v_smfmac_f32_16x16x64_bf16 v[244:247], v[104:107], v[64:71], v109\n\t"
    "buffer_load_dwordx4 v23, %[swo], s62 offen offset:0 lds\n\t"
    "v_exp_f32 v35, v35\n\t"
    "v_exp_f32 v36, v36\n\t"
    "v_exp_f32 v37, v37\n\t"
    "v_exp_f32 v38, v38\n\t"
    "v_exp_f32 v39, v39\n\t"
    "v_cvt_pk_bf16_f32 v60, v32, v33\n\t"
    "v_cvt_pk_bf16_f32 v61, v34, v35\n\t"
    "v_cvt_pk_bf16_f32 v62, v36, v37\n\t"
    "v_cvt_pk_bf16_f32 v63, v38, v39\n\t"
    "v_mfma_f32_32x32x16_bf16 v[24:39], a[240:243], a[224:227], 0\n\t"
    "v_exp_f32 v40, v40\n\t"
    "v_exp_f32 v41, v41\n\t"
    "v_exp_f32 v42, v42\n\t"
    "v_exp_f32 v43, v43\n\t"
    "v_exp_f32 v44, v44\n\t"
    "v_exp_f32 v45, v45\n\t"
    "v_smfmac_f32_32x32x32_bf16 v[144:159], v[100:103], v[56:63], v108\n\t"
    "buffer_load_dwordx4 v23, %[swo], s62 offen offset:1024 lds\n\t"
    "v_exp_f32 v46, v46\n\t"
    "v_exp_f32 v47, v47\n\t"
    "v_exp_f32 v48, v48\n\t"
    "v_cvt_pk_bf16_f32 v64, v40, v41\n\t"
    "v_cvt_pk_bf16_f32 v65, v42, v43\n\t"
    "v_cvt_pk_bf16_f32 v66, v44, v45\n\t"
    "v_cvt_pk_bf16_f32 v67, v46, v47\n\t"
    "v_exp_f32 v49, v49\n\t"
    "v_exp_f32 v50, v50\n\t"
    "v_smfmac_f32_16x16x64_bf16 v[248:251], v[104:107], v[56:63], v109\n\t"
    "buffer_load_dwordx4 v23, %[swo], s62 offen offset:2048 lds\n\t"
    "v_exp_f32 v51, v51\n\t"
    "v_exp_f32 v52, v52\n\t"
    "v_exp_f32 v53, v53\n\t"
    "v_exp_f32 v54, v54\n\t"
    "v_exp_f32 v55, v55\n\t"
    "v_cvt_pk_bf16_f32 v68, v48, v49\n\t"
    "v_cvt_pk_bf16_f32 v69, v50, v51\n\t"
    "v_cvt_pk_bf16_f32 v70, v52, v53\n\t"
    "v_cvt_pk_bf16_f32 v71, v54, v55\n\t"
    "v_mfma_f32_32x32x16_bf16 v[40:55], a[240:243], a[228:231], 0\n\t"
    "s_waitcnt vmcnt(22)\n\t"
    "v_exp_f32 v24, v24\n\t"
    "v_exp_f32 v25, v25\n\t"
    "v_exp_f32 v26, v26\n\t"
    "v_exp_f32 v27, v27\n\t"
    "v_exp_f32 v28, v28\n\t"
    "v_exp_f32 v29, v29\n\t"
    "v_smfmac_f32_32x32x32_bf16 v[160:175], v[100:103], v[64:71], v108\n\t"
    "buffer_load_dwordx4 v[100:103], %[kv], %[sws], s61 offen offset:3072 sc1\n\t"
    "v_exp_f32 v30, v30\n\t"
    "v_exp_f32 v31, v31\n\t"
    "v_exp_f32 v32, v32\n\t"
    "v_cvt_pk_bf16_f32 v56, v24, v25\n\t"
    "v_cvt_pk_bf16_f32 v57, v26, v27\n\t"
    "v_cvt_pk_bf16_f32 v58, v28, v29\n\t"
    "v_cvt_pk_bf16_f32 v59, v30, v31\n\t"
    "v_exp_f32 v33, v33\n\t"
    "v_exp_f32 v34, v34\n\t"
    "v_smfmac_f32_16x16x64_bf16 v[252:255], v[104:107], v[64:71], v109\n\t"
    "buffer_load_dwordx4 v23, %[swo], s62 offen offset:3072 lds\n\t"
    "v_exp_f32 v35, v35\n\t"
    "v_exp_f32 v36, v36\n\t"
    "v_exp_f32 v37, v37\n\t"
    "v_exp_f32 v38, v38\n\t"
    "v_exp_f32 v39, v39\n\t"
    "v_cvt_pk_bf16_f32 v60, v32, v33\n\t"
    "v_cvt_pk_bf16_f32 v61, v34, v35\n\t"
    "v_cvt_pk_bf16_f32 v62, v36, v37\n\t"
    "v_cvt_pk_bf16_f32 v63, v38, v39\n\t"
    "v_mfma_f32_32x32x16_bf16 v[24:39], a[240:243], a[232:235], 0\n\t"
    "s_add_u32 m0, s90, 0x4000\n\t"
    "v_exp_f32 v40, v40\n\t"
    "v_exp_f32 v41, v41\n\t"
    "v_exp_f32 v42, v42\n\t"
    "v_exp_f32 v43, v43\n\t"
    "v_exp_f32 v44, v44\n\t"
    "v_exp_f32 v45, v45\n\t"
    "v_smfmac_f32_32x32x32_bf16 v[112:127], v[80:83], v[56:63], v108\n\t"
    "buffer_load_dwordx4 v[76:79], %[kv], %[sws], s60 offen offset:3072 sc1\n\t"
    "v_exp_f32 v46, v46\n\t"
    "v_exp_f32 v47, v47\n\t"
    "v_exp_f32 v48, v48\n\t"
    "v_cvt_pk_bf16_f32 v64, v40, v41\n\t"
    "v_cvt_pk_bf16_f32 v65, v42, v43\n\t"
    "v_cvt_pk_bf16_f32 v66, v44, v45\n\t"
    "v_cvt_pk_bf16_f32 v67, v46, v47\n\t"
    "v_exp_f32 v49, v49\n\t"
    "v_exp_f32 v50, v50\n\t"
    "v_smfmac_f32_16x16x64_bf16 v[240:243], v[104:107], v[56:63], v109\n\t"
    "s_add_u32 s62, s89, 0x0\n\t"
    "v_exp_f32 v51, v51\n\t"
    "v_exp_f32 v52, v52\n\t"
    "v_exp_f32 v53, v53\n\t"
    "v_exp_f32 v54, v54\n\t"
    "v_exp_f32 v55, v55\n\t"
    "v_cvt_pk_bf16_f32 v68, v48, v49\n\t"
    "v_cvt_pk_bf16_f32 v69, v50, v51\n\t"
    "v_cvt_pk_bf16_f32 v70, v52, v53\n\t"
    "v_cvt_pk_bf16_f32 v71, v54, v55\n\t"
    "v_mfma_f32_32x32x16_bf16 v[40:55], a[240:243], a[236:239], 0\n\t"
    "s_mov_b32 s61, 0x203000\n\t"
    "v_exp_f32 v24, v24\n\t"
    "v_exp_f32 v25, v25\n\t"
    "v_exp_f32 v26, v26\n\t"
    "v_exp_f32 v27, v27\n\t"
    "v_exp_f32 v28, v28\n\t"
    "v_exp_f32 v29, v29\n\t"
    "v_smfmac_f32_32x32x32_bf16 v[128:143], v[80:83], v[64:71], v108\n\t"
    "buffer_load_dwordx4 %[tv], %[sw2], s62 offen offset:0 lds\n\t"
    "v_exp_f32 v30, v30\n\t"
    "v_exp_f32 v31, v31\n\t"
    "v_exp_f32 v32, v32\n\t"
    "v_cvt_pk_bf16_f32 v56, v24, v25\n\t"
    "v_cvt_pk_bf16_f32 v57, v26, v27\n\t"
    "v_cvt_pk_bf16_f32 v58, v28, v29\n\t"
    "v_cvt_pk_bf16_f32 v59, v30, v31\n\t"
    "v_exp_f32 v33, v33\n\t"
    "v_exp_f32 v34, v34\n\t"
    "v_smfmac_f32_16x16x64_bf16 v[244:247], v[104:107], v[64:71], v109\n\t"
    "s_mov_b32 s60, 0x3000\n\t"
    "v_exp_f32 v35, v35\n\t"
    "v_exp_f32 v36, v36\n\t"
    "v_exp_f32 v37, v37\n\t"
    "v_exp_f32 v38, v38\n\t"
    "v_exp_f32 v39, v39\n\t"
    "v_cvt_pk_bf16_f32 v60, v32, v33\n\t"
    "v_cvt_pk_bf16_f32 v61, v34, v35\n\t"
    "v_cvt_pk_bf16_f32 v62, v36, v37\n\t"
    "v_cvt_pk_bf16_f32 v63, v38, v39\n\t"
    "v_mfma_f32_32x32x16_bf16 v[24:39], a[244:247], a[224:227], 0\n\t"
    "v_exp_f32 v40, v40\n\t"
    "v_exp_f32 v41, v41\n\t"
    "v_exp_f32 v42, v42\n\t"
    "v_exp_f32 v43, v43\n\t"
    "v_exp_f32 v44, v44\n\t"
    "v_exp_f32 v45, v45\n\t"
    "v_smfmac_f32_32x32x32_bf16 v[144:159], v[80:83], v[56:63], v108\n\t"
    "buffer_load_dwordx4 %[tv], %[sw2], s62 offen offset:1024 lds\n\t"
    "v_exp_f32 v46, v46\n\t"
    "v_exp_f32 v47, v47\n\t"
    "v_exp_f32 v48, v48\n\t"
    "v_cvt_pk_bf16_f32 v64, v40, v41\n\t"
    "v_cvt_pk_bf16_f32 v65, v42, v43\n\t"
    "v_cvt_pk_bf16_f32 v66, v44, v45\n\t"
    "v_cvt_pk_bf16_f32 v67, v46, v47\n\t"
    "v_exp_f32 v49, v49\n\t"
    "v_exp_f32 v50, v50\n\t"
    "v_smfmac_f32_16x16x64_bf16 v[248:251], v[104:107], v[56:63], v109\n\t"
    "buffer_load_dwordx4 %[tv], %[sw2], s62 offen offset:2048 lds\n\t"
    "v_exp_f32 v51, v51\n\t"
    "v_exp_f32 v52, v52\n\t"
    "v_exp_f32 v53, v53\n\t"
    "v_exp_f32 v54, v54\n\t"
    "v_exp_f32 v55, v55\n\t"
    "v_cvt_pk_bf16_f32 v68, v48, v49\n\t"
    "v_cvt_pk_bf16_f32 v69, v50, v51\n\t"
    "v_cvt_pk_bf16_f32 v70, v52, v53\n\t"
    "v_cvt_pk_bf16_f32 v71, v54, v55\n\t"
    "v_mfma_f32_32x32x16_bf16 v[40:55], a[244:247], a[228:231], 0\n\t"
    "s_waitcnt vmcnt(24)\n\t"
    "v_exp_f32 v24, v24\n\t"
    "v_exp_f32 v25, v25\n\t"
    "v_exp_f32 v26, v26\n\t"
    "v_exp_f32 v27, v27\n\t"
    "v_exp_f32 v28, v28\n\t"
    "v_exp_f32 v29, v29\n\t"
    "v_smfmac_f32_32x32x32_bf16 v[160:175], v[80:83], v[64:71], v108\n\t"
    "buffer_load_dwordx4 v[80:83], %[kv], %[sws], s61 offen offset:0 sc1\n\t"
    "v_exp_f32 v30, v30\n\t"
    "v_exp_f32 v31, v31\n\t"
    "v_exp_f32 v32, v32\n\t"
    "v_cvt_pk_bf16_f32 v56, v24, v25\n\t"
    "v_cvt_pk_bf16_f32 v57, v26, v27\n\t"
    "v_cvt_pk_bf16_f32 v58, v28, v29\n\t"
    "v_cvt_pk_bf16_f32 v59, v30, v31\n\t"
    "v_exp_f32 v33, v33\n\t"
    "v_exp_f32 v34, v34\n\t"
    "v_smfmac_f32_16x16x64_bf16 v[252:255], v[104:107], v[64:71], v109\n\t"
    "buffer_load_dwordx4 %[tv], %[sw2], s62 offen offset:3072 lds\n\t"
    "v_exp_f32 v35, v35\n\t"
    "v_exp_f32 v36, v36\n\t"
    "v_exp_f32 v37, v37\n\t"
    "v_exp_f32 v38, v38\n\t"
    "v_exp_f32 v39, v39\n\t"
    "v_cvt_pk_bf16_f32 v60, v32, v33\n\t"
    "v_cvt_pk_bf16_f32 v61, v34, v35\n\t"
    "v_cvt_pk_bf16_f32 v62, v36, v37\n\t"
    "v_cvt_pk_bf16_f32 v63, v38, v39\n\t"
    "v_mfma_f32_32x32x16_bf16 v[24:39], a[244:247], a[232:235], 0\n\t"
    "s_add_u32 m0, s90, 0x5000\n\t"
    "v_exp_f32 v40, v40\n\t"
    "v_exp_f32 v41, v41\n\t"
    "v_exp_f32 v42, v42\n\t"
    "v_exp_f32 v43, v43\n\t"
    "v_exp_f32 v44, v44\n\t"
    "v_exp_f32 v45, v45\n\t"
    "v_smfmac_f32_32x32x32_bf16 v[112:127], v[84:87], v[56:63], v108\n\t"
    "buffer_load_dwordx4 a[240:243], %[kv], %[sws], s60 offen offset:0 sc1\n\t"
    "v_exp_f32 v46, v46\n\t"
    "v_exp_f32 v47, v47\n\t"
    "v_exp_f32 v48, v48\n\t"
    "v_cvt_pk_bf16_f32 v64, v40, v41\n\t"
    "v_cvt_pk_bf16_f32 v65, v42, v43\n\t"
    "v_cvt_pk_bf16_f32 v66, v44, v45\n\t"
    "v_cvt_pk_bf16_f32 v67, v46, v47\n\t"
    "v_exp_f32 v49, v49\n\t"
    "v_exp_f32 v50, v50\n\t"
    "v_smfmac_f32_16x16x64_bf16 v[240:243], v[104:107], v[56:63], v109\n\t"
    "s_add_u32 s62, s89, 0x8000\n\t"
    "v_exp_f32 v51, v51\n\t"
    "v_exp_f32 v52, v52\n\t"
    "v_exp_f32 v53, v53\n\t"
    "v_exp_f32 v54, v54\n\t"
    "v_exp_f32 v55, v55\n\t"
    "v_cvt_pk_bf16_f32 v68, v48, v49\n\t"
    "v_cvt_pk_bf16_f32 v69, v50, v51\n\t"
    "v_cvt_pk_bf16_f32 v70, v52, v53\n\t"
    "v_cvt_pk_bf16_f32 v71, v54, v55\n\t"
    "v_mfma_f32_32x32x16_bf16 v[40:55], a[244:247], a[236:239], 0\n\t"
    "v_exp_f32 v24, v24\n\t"
    "v_exp_f32 v25, v25\n\t"
    "v_exp_f32 v26, v26\n\t"
    "v_exp_f32 v27, v27\n\t"
    "v_exp_f32 v28, v28\n\t"
    "v_exp_f32 v29, v29\n\t"
    "v_smfmac_f32_32x32x32_bf16 v[128:143], v[84:87], v[64:71], v108\n\t"
    "buffer_load_dwordx4 %[tv], %[sw2], s62 offen offset:0 lds\n\t"
    "v_exp_f32 v30, v30\n\t"
    "v_exp_f32 v31, v31\n\t"
    "v_exp_f32 v32, v32\n\t"
    "v_cvt_pk_bf16_f32 v56, v24, v25\n\t"
    "v_cvt_pk_bf16_f32 v57, v26, v27\n\t"
    "v_cvt_pk_bf16_f32 v58, v28, v29\n\t"
    "v_cvt_pk_bf16_f32 v59, v30, v31\n\t"
    "v_exp_f32 v33, v33\n\t"
    "v_exp_f32 v34, v34\n\t"
    "v_smfmac_f32_16x16x64_bf16 v[244:247], v[104:107], v[64:71], v109\n\t"
    "buffer_load_dwordx4 %[tv], %[sw2], s62 offen offset:1024 lds\n\t"
    "v_exp_f32 v35, v35\n\t"
    "v_exp_f32 v36, v36\n\t"
    "v_exp_f32 v37, v37\n\t"
    "v_exp_f32 v38, v38\n\t"
    "v_exp_f32 v39, v39\n\t"
    "v_cvt_pk_bf16_f32 v60, v32, v33\n\t"
    "v_cvt_pk_bf16_f32 v61, v34, v35\n\t"
    "v_cvt_pk_bf16_f32 v62, v36, v37\n\t"
    "v_cvt_pk_bf16_f32 v63, v38, v39\n\t"
    "v_mfma_f32_32x32x16_bf16 v[24:39], a[248:251], a[224:227], 0\n\t"
    "v_exp_f32 v40, v40\n\t"
    "v_exp_f32 v41, v41\n\t"
    "v_exp_f32 v42, v42\n\t"
    "v_exp_f32 v43, v43\n\t"
    "v_exp_f32 v44, v44\n\t"
    "v_exp_f32 v45, v45\n\t"
    "v_smfmac_f32_32x32x32_bf16 v[144:159], v[84:87], v[56:63], v108\n\t"
    "buffer_load_dwordx4 %[tv], %[sw2], s62 offen offset:2048 lds\n\t"
    "v_exp_f32 v46, v46\n\t"
    "v_exp_f32 v47, v47\n\t"
    "v_exp_f32 v48, v48\n\t"
    "v_cvt_pk_bf16_f32 v64, v40, v41\n\t"
    "v_cvt_pk_bf16_f32 v65, v42, v43\n\t"
    "v_cvt_pk_bf16_f32 v66, v44, v45\n\t"
    "v_cvt_pk_bf16_f32 v67, v46, v47\n\t"
    "v_exp_f32 v49, v49\n\t"
    "v_exp_f32 v50, v50\n\t"
    "v_smfmac_f32_16x16x64_bf16 v[248:251], v[104:107], v[56:63], v109\n\t"
    "buffer_load_dwordx4 %[tv], %[sw2], s62 offen offset:3072 lds\n\t"
    "v_exp_f32 v51, v51\n\t"
    "v_exp_f32 v52, v52\n\t"
    "v_exp_f32 v53, v53\n\t"
    "v_exp_f32 v54, v54\n\t"
    "v_exp_f32 v55, v55\n\t"
    "v_cvt_pk_bf16_f32 v68, v48, v49\n\t"
    "v_cvt_pk_bf16_f32 v69, v50, v51\n\t"
    "v_cvt_pk_bf16_f32 v70, v52, v53\n\t"
    "v_cvt_pk_bf16_f32 v71, v54, v55\n\t"
    "v_mfma_f32_32x32x16_bf16 v[40:55], a[248:251], a[228:231], 0\n\t"
    "s_waitcnt vmcnt(24)\n\t"
    "ds_read_b64_tr_b16 v[176:177], v12 offset:0\n\t"
    "v_exp_f32 v24, v24\n\t"
    "v_exp_f32 v25, v25\n\t"
    "v_exp_f32 v26, v26\n\t"
    "v_exp_f32 v27, v27\n\t"
    "v_exp_f32 v28, v28\n\t"
    "v_exp_f32 v29, v29\n\t"
    "v_smfmac_f32_32x32x32_bf16 v[160:175], v[84:87], v[64:71], v108\n\t"
    "buffer_load_dwordx4 v[84:87], %[kv], %[sws], s61 offen offset:1024 sc1\n\t"
    "v_exp_f32 v30, v30\n\t"
    "v_exp_f32 v31, v31\n\t"
    "v_exp_f32 v32, v32\n\t"
    "v_cvt_pk_bf16_f32 v56, v24, v25\n\t"
    "v_cvt_pk_bf16_f32 v57, v26, v27\n\t"
    "v_cvt_pk_bf16_f32 v58, v28, v29\n\t"
    "v_cvt_pk_bf16_f32 v59, v30, v31\n\t"
    "v_exp_f32 v33, v33\n\t"
    "v_exp_f32 v34, v34\n\t"
    "v_smfmac_f32_16x16x64_bf16 v[252:255], v[104:107], v[64:71], v109\n\t"
    "buffer_load_dword %[fl], %[cdv], s[92:95], 0 offen\n\t"
    "v_exp_f32 v35, v35\n\t"
    "v_exp_f32 v36, v36\n\t"
    "v_exp_f32 v37, v37\n\t"
    "v_exp_f32 v38, v38\n\t"
    "v_exp_f32 v39, v39\n\t"
    "v_cvt_pk_bf16_f32 v60, v32, v33\n\t"
    "v_cvt_pk_bf16_f32 v61, v34, v35\n\t"
    "v_cvt_pk_bf16_f32 v62, v36, v37\n\t"
    "v_cvt_pk_bf16_f32 v63, v38, v39\n\t"
    "v_mfma_f32_32x32x16_bf16 v[24:39], a[248:251], a[232:235], 0\n\t"
    "s_add_u32 m0, s90, 0x6000\n\t"
    "ds_read_b64_tr_b16 v[180:181], v13 offset:0\n\t"
    "v_exp_f32 v40, v40\n\t"
    "v_exp_f32 v41, v41\n\t"
    "v_exp_f32 v42, v42\n\t"
    "v_exp_f32 v43, v43\n\t"
    "v_exp_f32 v44, v44\n\t"
    "v_exp_f32 v45, v45\n\t"
    "v_smfmac_f32_32x32x32_bf16 v[112:127], v[88:91], v[56:63], v108\n\t"
    "buffer_load_dwordx4 a[244:247], %[kv], %[sws], s60 offen offset:1024 sc1\n\t"
    "v_exp_f32 v46, v46\n\t"
    "v_exp_f32 v47, v47\n\t"
    "v_exp_f32 v48, v48\n\t"
    "v_cvt_pk_bf16_f32 v64, v40, v41\n\t"
    "v_cvt_pk_bf16_f32 v65, v42, v43\n\t"
    "v_cvt_pk_bf16_f32 v66, v44, v45\n\t"
    "v_cvt_pk_bf16_f32 v67, v46, v47\n\t"
    "v_exp_f32 v49, v49\n\t"
    "v_exp_f32 v50, v50\n\t"
    "v_smfmac_f32_16x16x64_bf16 v[240:243], v[104:107], v[56:63], v109\n\t"
    "s_add_u32 s62, s89, 0x10000\n\t"
    "ds_read_b64_tr_b16 v[178:179], v12 offset:256\n\t"
    "v_exp_f32 v51, v51\n\t"
    "v_exp_f32 v52, v52\n\t"
    "v_exp_f32 v53, v53\n\t"
    "v_exp_f32 v54, v54\n\t"
    "v_exp_f32 v55, v55\n\t"
    "v_cvt_pk_bf16_f32 v68, v48, v49\n\t"
    "v_cvt_pk_bf16_f32 v69, v50, v51\n\t"
    "v_cvt_pk_bf16_f32 v70, v52, v53\n\t"
    "v_cvt_pk_bf16_f32 v71, v54, v55\n\t"
    "v_mfma_f32_32x32x16_bf16 v[40:55], a[248:251], a[236:239], 0\n\t"
    "ds_read_b64_tr_b16 v[182:183], v13 offset:256\n\t"
    "v_exp_f32 v24, v24\n\t"
    "v_exp_f32 v25, v25\n\t"
    "v_exp_f32 v26, v26\n\t"
    "v_exp_f32 v27, v27\n\t"
    "v_exp_f32 v28, v28\n\t"
    "v_exp_f32 v29, v29\n\t"
    "v_smfmac_f32_32x32x32_bf16 v[128:143], v[88:91], v[64:71], v108\n\t"
    "buffer_load_dwordx4 %[tv], %[sw2], s62 offen offset:0 lds\n\t"
    "v_exp_f32 v30, v30\n\t"
    "v_exp_f32 v31, v31\n\t"
    "v_exp_f32 v32, v32\n\t"
    "v_cvt_pk_bf16_f32 v56, v24, v25\n\t"
    "v_cvt_pk_bf16_f32 v57, v26, v27\n\t"
    "v_cvt_pk_bf16_f32 v58, v28, v29\n\t"
    "v_cvt_pk_bf16_f32 v59, v30, v31\n\t"
    "v_exp_f32 v33, v33\n\t"
    "v_exp_f32 v34, v34\n\t"
    "v_smfmac_f32_16x16x64_bf16 v[244:247], v[104:107], v[64:71], v109\n\t"
    "buffer_load_dword %[fl], %[twv], %[sw2], s63 offen\n\t"
    "v_exp_f32 v35, v35\n\t"
    "v_exp_f32 v36, v36\n\t"
    "v_exp_f32 v37, v37\n\t"
    "v_exp_f32 v38, v38\n\t"
    "v_exp_f32 v39, v39\n\t"
    "v_cvt_pk_bf16_f32 v60, v32, v33\n\t"
    "v_cvt_pk_bf16_f32 v61, v34, v35\n\t"
    "v_cvt_pk_bf16_f32 v62, v36, v37\n\t"
    "v_cvt_pk_bf16_f32 v63, v38, v39\n\t"
    "v_mfma_f32_32x32x16_bf16 v[24:39], a[252:255], a[224:227], 0\n\t"
    "ds_read_b64_tr_b16 v[184:185], v12 offset:512\n\t"
    "v_exp_f32 v40, v40\n\t"
    "v_exp_f32 v41, v41\n\t"
    "v_exp_f32 v42, v42\n\t"
    "v_exp_f32 v43, v43\n\t"
    "v_exp_f32 v44, v44\n\t"
    "v_exp_f32 v45, v45\n\t"
    "v_smfmac_f32_32x32x32_bf16 v[144:159], v[88:91], v[56:63], v108\n\t"
    "buffer_load_dwordx4 %[tv], %[sw2], s62 offen offset:1024 lds\n\t"
    "v_exp_f32 v46, v46\n\t"
    "v_exp_f32 v47, v47\n\t"
    "v_exp_f32 v48, v48\n\t"
    "v_cvt_pk_bf16_f32 v64, v40, v41\n\t"
    "v_cvt_pk_bf16_f32 v65, v42, v43\n\t"
    "v_cvt_pk_bf16_f32 v66, v44, v45\n\t"
    "v_cvt_pk_bf16_f32 v67, v46, v47\n\t"
    "v_exp_f32 v49, v49\n\t"
    "v_exp_f32 v50, v50\n\t"
    "v_smfmac_f32_16x16x64_bf16 v[248:251], v[104:107], v[56:63], v109\n\t"
    "ds_read_b64_tr_b16 v[188:189], v13 offset:512\n\t"
    "v_exp_f32 v51, v51\n\t"
    "v_exp_f32 v52, v52\n\t"
    "v_exp_f32 v53, v53\n\t"
    "v_exp_f32 v54, v54\n\t"
    "v_exp_f32 v55, v55\n\t"
    "v_cvt_pk_bf16_f32 v68, v48, v49\n\t"
    "v_cvt_pk_bf16_f32 v69, v50, v51\n\t"
    "v_cvt_pk_bf16_f32 v70, v52, v53\n\t"
    "v_cvt_pk_bf16_f32 v71, v54, v55\n\t"
    "v_mfma_f32_32x32x16_bf16 v[40:55], a[252:255], a[228:231], 0\n\t"
    "s_waitcnt vmcnt(23)\n\t"
    "ds_read_b64_tr_b16 v[186:187], v12 offset:768\n\t"
    "v_exp_f32 v24, v24\n\t"
    "v_exp_f32 v25, v25\n\t"
    "v_exp_f32 v26, v26\n\t"
    "v_exp_f32 v27, v27\n\t"
    "v_exp_f32 v28, v28\n\t"
    "v_exp_f32 v29, v29\n\t"
    "v_smfmac_f32_32x32x32_bf16 v[160:175], v[88:91], v[64:71], v108\n\t"
    "buffer_load_dwordx4 v[88:91], %[kv], %[sws], s61 offen offset:2048 sc1\n\t"
    "v_exp_f32 v30, v30\n\t"
    "v_exp_f32 v31, v31\n\t"
    "v_exp_f32 v32, v32\n\t"
    "v_cvt_pk_bf16_f32 v56, v24, v25\n\t"
    "v_cvt_pk_bf16_f32 v57, v26, v27\n\t"
    "v_cvt_pk_bf16_f32 v58, v28, v29\n\t"
    "v_cvt_pk_bf16_f32 v59, v30, v31\n\t"
    "v_exp_f32 v33, v33\n\t"
    "v_exp_f32 v34, v34\n\t"
    "v_smfmac_f32_16x16x64_bf16 v[252:255], v[104:107], v[64:71], v109\n\t"
    "ds_read_b64_tr_b16 v[190:191], v13 offset:768\n\t"
    "v_exp_f32 v35, v35\n\t"
    "v_exp_f32 v36, v36\n\t"
    "v_exp_f32 v37, v37\n\t"
    "v_exp_f32 v38, v38\n\t"
    "v_exp_f32 v39, v39\n\t"
    "v_cvt_pk_bf16_f32 v60, v32, v33\n\t"
    "v_cvt_pk_bf16_f32 v61, v34, v35\n\t"
    "v_cvt_pk_bf16_f32 v62, v36, v37\n\t"
    "v_cvt_pk_bf16_f32 v63, v38, v39\n\t"
    "v_mfma_f32_32x32x16_bf16 v[24:39], a[252:255], a[232:235], 0\n\t"
    "ds_read_b64_tr_b16 v[192:193], v12 offset:4096\n\t"
    "v_exp_f32 v40, v40\n\t"
    "v_exp_f32 v41, v41\n\t"
    "v_exp_f32 v42, v42\n\t"
    "v_exp_f32 v43, v43\n\t"
    "v_exp_f32 v44, v44\n\t"
    "v_exp_f32 v45, v45\n\t"
    "v_smfmac_f32_32x32x32_bf16 v[112:127], v[92:95], v[56:63], v108\n\t"
    "buffer_load_dwordx4 a[248:251], %[kv], %[sws], s60 offen offset:2048 sc1\n\t"
    "v_exp_f32 v46, v46\n\t"
    "v_exp_f32 v47, v47\n\t"
    "v_exp_f32 v48, v48\n\t"
    "v_cvt_pk_bf16_f32 v64, v40, v41\n\t"
    "v_cvt_pk_bf16_f32 v65, v42, v43\n\t"
    "v_cvt_pk_bf16_f32 v66, v44, v45\n\t"
    "v_cvt_pk_bf16_f32 v67, v46, v47\n\t"
    "v_exp_f32 v49, v49\n\t"
    "v_exp_f32 v50, v50\n\t"
    "v_smfmac_f32_16x16x64_bf16 v[240:243], v[104:107], v[56:63], v109\n\t"
    "ds_read_b64_tr_b16 v[196:197], v13 offset:4096\n\t"
    "v_exp_f32 v51, v51\n\t"
    "v_exp_f32 v52, v52\n\t"
    "v_exp_f32 v53, v53\n\t"
    "v_exp_f32 v54, v54\n\t"
    "v_exp_f32 v55, v55\n\t"
    "v_cvt_pk_bf16_f32 v68, v48, v49\n\t"
    "v_cvt_pk_bf16_f32 v69, v50, v51\n\t"
    "v_cvt_pk_bf16_f32 v70, v52, v53\n\t"
    "v_cvt_pk_bf16_f32 v71, v54, v55\n\t"
    "v_mfma_f32_32x32x16_bf16 v[40:55], a[252:255], a[236:239], 0\n\t"
    "s_waitcnt lgkmcnt(2)\n\t"
    "ds_read_b64_tr_b16 v[194:195], v12 offset:4352\n\t"
    "v_exp_f32 v24, v24\n\t"
    "v_exp_f32 v25, v25\n\t"
    "v_exp_f32 v26, v26\n\t"
    "v_exp_f32 v27, v27\n\t"
    "v_exp_f32 v28, v28\n\t"
    "v_exp_f32 v29, v29\n\t"
    "v_smfmac_f32_32x32x32_bf16 v[128:143], v[92:95], v[64:71], v108\n\t"
    "buffer_load_dwordx4 %[tv], %[sw2], s62 offen offset:2048 lds\n\t"
    "v_exp_f32 v30, v30\n\t"
    "v_exp_f32 v31, v31\n\t"
    "v_exp_f32 v32, v32\n\t"
    "v_cvt_pk_bf16_f32 v56, v24, v25\n\t"
    "v_cvt_pk_bf16_f32 v57, v26, v27\n\t"
    "v_cvt_pk_bf16_f32 v58, v28, v29\n\t"
    "v_cvt_pk_bf16_f32 v59, v30, v31\n\t"
    "v_exp_f32 v33, v33\n\t"
    "v_exp_f32 v34, v34\n\t"
    "v_smfmac_f32_16x16x64_bf16 v[244:247], v[104:107], v[64:71], v109\n\t"
    "ds_read_b64_tr_b16 v[198:199], v13 offset:4352\n\t"
    "v_exp_f32 v35, v35\n\t"
    "v_exp_f32 v36, v36\n\t"
    "v_exp_f32 v37, v37\n\t"
    "v_exp_f32 v38, v38\n\t"
    "v_exp_f32 v39, v39\n\t"
    "v_cvt_pk_bf16_f32 v60, v32, v33\n\t"
    "v_cvt_pk_bf16_f32 v61, v34, v35\n\t"
    "v_cvt_pk_bf16_f32 v62, v36, v37\n\t"
    "v_cvt_pk_bf16_f32 v63, v38, v39\n\t"
    "v_mfma_f32_32x32x16_bf16 v[24:39], v[72:75], a[224:227], 0\n\t"
    "ds_read_b64_tr_b16 v[200:201], v12 offset:4608\n\t"
    "v_exp_f32 v40, v40\n\t"
    "v_exp_f32 v41, v41\n\t"
    "v_exp_f32 v42, v42\n\t"
    "v_exp_f32 v43, v43\n\t"
    "v_exp_f32 v44, v44\n\t"
    "v_exp_f32 v45, v45\n\t"
    "v_smfmac_f32_32x32x32_bf16 v[144:159], v[92:95], v[56:63], v108\n\t"
    "buffer_load_dwordx4 %[tv], %[sw2], s62 offen offset:3072 lds\n\t"
    "v_exp_f32 v46, v46\n\t"
    "v_exp_f32 v47, v47\n\t"
    "v_exp_f32 v48, v48\n\t"
    "v_cvt_pk_bf16_f32 v64, v40, v41\n\t"
    "v_cvt_pk_bf16_f32 v65, v42, v43\n\t"
    "v_cvt_pk_bf16_f32 v66, v44, v45\n\t"
    "v_cvt_pk_bf16_f32 v67, v46, v47\n\t"
    "v_exp_f32 v49, v49\n\t"
    "v_exp_f32 v50, v50\n\t"
    "v_smfmac_f32_16x16x64_bf16 v[248:251], v[104:107], v[56:63], v109\n\t"
    "s_add_u32 m0, s91, 0x0\n\t"
    "ds_read_b64_tr_b16 v[204:205], v13 offset:4608\n\t"
    "v_exp_f32 v51, v51\n\t"
    "v_exp_f32 v52, v52\n\t"
    "v_exp_f32 v53, v53\n\t"
    "v_exp_f32 v54, v54\n\t"
    "v_exp_f32 v55, v55\n\t"
    "v_cvt_pk_bf16_f32 v68, v48, v49\n\t"
    "v_cvt_pk_bf16_f32 v69, v50, v51\n\t"
    "v_cvt_pk_bf16_f32 v70, v52, v53\n\t"
    "v_cvt_pk_bf16_f32 v71, v54, v55\n\t"
    "v_mfma_f32_32x32x16_bf16 v[40:55], v[72:75], a[228:231], 0\n\t"
    "s_waitcnt vmcnt(20)\n\t"
    "ds_read_b64_tr_b16 v[202:203], v12 offset:4864\n\t"
    "v_exp_f32 v24, v24\n\t"
    "v_exp_f32 v25, v25\n\t"
    "v_exp_f32 v26, v26\n\t"
    "v_exp_f32 v27, v27\n\t"
    "v_exp_f32 v28, v28\n\t"
    "v_exp_f32 v29, v29\n\t"
    "v_smfmac_f32_32x32x32_bf16 v[160:175], v[92:95], v[64:71], v108\n\t"
    "buffer_load_dwordx4 v[92:95], %[kv], %[sws], s61 offen offset:3072 sc1\n\t"
    "v_exp_f32 v30, v30\n\t"
    "v_exp_f32 v31, v31\n\t"
    "v_exp_f32 v32, v32\n\t"
    "v_cvt_pk_bf16_f32 v56, v24, v25\n\t"
    "v_cvt_pk_bf16_f32 v57, v26, v27\n\t"
    "v_cvt_pk_bf16_f32 v58, v28, v29\n\t"
    "v_cvt_pk_bf16_f32 v59, v30, v31\n\t"
    "v_exp_f32 v33, v33\n\t"
    "v_exp_f32 v34, v34\n\t"
    "v_smfmac_f32_16x16x64_bf16 v[252:255], v[104:107], v[64:71], v109\n\t"
    "s_add_u32 s62, s89, 0x18000\n\t"
    "ds_read_b64_tr_b16 v[206:207], v13 offset:4864\n\t"
    "v_exp_f32 v35, v35\n\t"
    "v_exp_f32 v36, v36\n\t"
    "v_exp_f32 v37, v37\n\t"
    "v_exp_f32 v38, v38\n\t"
    "v_exp_f32 v39, v39\n\t"
    "v_cvt_pk_bf16_f32 v60, v32, v33\n\t"
    "v_cvt_pk_bf16_f32 v61, v34, v35\n\t"
    "v_cvt_pk_bf16_f32 v62, v36, v37\n\t"
    "v_cvt_pk_bf16_f32 v63, v38, v39\n\t"
    "v_mfma_f32_32x32x16_bf16 v[24:39], v[72:75], a[232:235], 0\n\t"
    "ds_read_b64_tr_b16 v[208:209], v12 offset:8192\n\t"
    "v_exp_f32 v40, v40\n\t"
    "v_exp_f32 v41, v41\n\t"
    "v_exp_f32 v42, v42\n\t"
    "v_exp_f32 v43, v43\n\t"
    "v_exp_f32 v44, v44\n\t"
    "v_exp_f32 v45, v45\n\t"
    "v_smfmac_f32_32x32x32_bf16 v[112:127], v[96:99], v[56:63], v108\n\t"
    "buffer_load_dwordx4 a[252:255], %[kv], %[sws], s60 offen offset:3072 sc1\n\t"
    "v_exp_f32 v46, v46\n\t"
    "v_exp_f32 v47, v47\n\t"
    "v_exp_f32 v48, v48\n\t"
    "v_cvt_pk_bf16_f32 v64, v40, v41\n\t"
    "v_cvt_pk_bf16_f32 v65, v42, v43\n\t"
    "v_cvt_pk_bf16_f32 v66, v44, v45\n\t"
    "v_cvt_pk_bf16_f32 v67, v46, v47\n\t"
    "v_exp_f32 v49, v49\n\t"
    "v_exp_f32 v50, v50\n\t"
    "v_smfmac_f32_16x16x64_bf16 v[240:243], v[104:107], v[56:63], v109\n\t"
    "ds_read_b64_tr_b16 v[212:213], v13 offset:8192\n\t"
    "v_exp_f32 v51, v51\n\t"
    "v_exp_f32 v52, v52\n\t"
    "v_exp_f32 v53, v53\n\t"
    "v_exp_f32 v54, v54\n\t"
    "v_exp_f32 v55, v55\n\t"
    "v_cvt_pk_bf16_f32 v68, v48, v49\n\t"
    "v_cvt_pk_bf16_f32 v69, v50, v51\n\t"
    "v_cvt_pk_bf16_f32 v70, v52, v53\n\t"
    "v_cvt_pk_bf16_f32 v71, v54, v55\n\t"
    "v_mfma_f32_32x32x16_bf16 v[40:55], v[72:75], a[236:239], 0\n\t"
    "s_mov_b32 s61, 0x204000\n\t"
    "ds_read_b64_tr_b16 v[210:211], v12 offset:8448\n\t"
    "v_exp_f32 v24, v24\n\t"
    "v_exp_f32 v25, v25\n\t"
    "v_exp_f32 v26, v26\n\t"
    "v_exp_f32 v27, v27\n\t"
    "v_exp_f32 v28, v28\n\t"
    "v_exp_f32 v29, v29\n\t"
    "v_smfmac_f32_32x32x32_bf16 v[128:143], v[96:99], v[64:71], v108\n\t"
    "buffer_load_dwordx4 %[tv], %[sw2], s62 offen offset:0 lds\n\t"
    "v_exp_f32 v30, v30\n\t"
    "v_exp_f32 v31, v31\n\t"
    "v_exp_f32 v32, v32\n\t"
    "v_cvt_pk_bf16_f32 v56, v24, v25\n\t"
    "v_cvt_pk_bf16_f32 v57, v26, v27\n\t"
    "v_cvt_pk_bf16_f32 v58, v28, v29\n\t"
    "v_cvt_pk_bf16_f32 v59, v30, v31\n\t"
    "v_exp_f32 v33, v33\n\t"
    "v_exp_f32 v34, v34\n\t"
    "v_smfmac_f32_16x16x64_bf16 v[244:247], v[104:107], v[64:71], v109\n\t"
    "s_mov_b32 s60, 0x4000\n\t"
    "ds_read_b64_tr_b16 v[214:215], v13 offset:8448\n\t"
    "v_exp_f32 v35, v35\n\t"
    "v_exp_f32 v36, v36\n\t"
    "v_exp_f32 v37, v37\n\t"
    "v_exp_f32 v38, v38\n\t"
    "v_exp_f32 v39, v39\n\t"
    "v_cvt_pk_bf16_f32 v60, v32, v33\n\t"
    "v_cvt_pk_bf16_f32 v61, v34, v35\n\t"
    "v_cvt_pk_bf16_f32 v62, v36, v37\n\t"
    "v_cvt_pk_bf16_f32 v63, v38, v39\n\t"
    "v_mfma_f32_32x32x16_bf16 v[24:39], v[76:79], a[224:227], 0\n\t"
    "s_waitcnt lgkmcnt(4)\n\t"
    "ds_read_b64_tr_b16 v[216:217], v12 offset:8704\n\t"
    "v_exp_f32 v40, v40\n\t"
    "v_exp_f32 v41, v41\n\t"
    "v_exp_f32 v42, v42\n\t"
    "v_exp_f32 v43, v43\n\t"
    "v_exp_f32 v44, v44\n\t"
    "v_exp_f32 v45, v45\n\t"
    "v_smfmac_f32_32x32x32_bf16 v[144:159], v[96:99], v[56:63], v108\n\t"
    "buffer_load_dwordx4 %[tv], %[sw2], s62 offen offset:1024 lds\n\t"
    "v_exp_f32 v46, v46\n\t"
    "v_exp_f32 v47, v47\n\t"
    "v_exp_f32 v48, v48\n\t"
    "v_cvt_pk_bf16_f32 v64, v40, v41\n\t"
    "v_cvt_pk_bf16_f32 v65, v42, v43\n\t"
    "v_cvt_pk_bf16_f32 v66, v44, v45\n\t"
    "v_cvt_pk_bf16_f32 v67, v46, v47\n\t"
    "v_exp_f32 v49, v49\n\t"
    "v_exp_f32 v50, v50\n\t"
    "v_smfmac_f32_16x16x64_bf16 v[248:251], v[104:107], v[56:63], v109\n\t"
    "ds_read_b64_tr_b16 v[220:221], v13 offset:8704\n\t"
    "v_exp_f32 v51, v51\n\t"
    "v_exp_f32 v52, v52\n\t"
    "v_exp_f32 v53, v53\n\t"
    "v_exp_f32 v54, v54\n\t"
    "v_exp_f32 v55, v55\n\t"
    "v_cvt_pk_bf16_f32 v68, v48, v49\n\t"
    "v_cvt_pk_bf16_f32 v69, v50, v51\n\t"
    "v_cvt_pk_bf16_f32 v70, v52, v53\n\t"
    "v_cvt_pk_bf16_f32 v71, v54, v55\n\t"
    "v_mfma_f32_32x32x16_bf16 v[40:55], v[76:79], a[228:231], 0\n\t"
    "buffer_load_dwordx4 %[tv], %[sw2], s62 offen offset:2048 lds\n\t"
    "v_exp_f32 v24, v24\n\t"
    "v_exp_f32 v25, v25\n\t"
    "v_exp_f32 v26, v26\n\t"
    "v_exp_f32 v27, v27\n\t"
    "v_exp_f32 v28, v28\n\t"
    "v_exp_f32 v29, v29\n\t"
    "v_smfmac_f32_32x32x32_bf16 v[160:175], v[96:99], v[64:71], v108\n\t"
    "buffer_load_dwordx4 v[96:99], %[kv], %[sws], s61 offen offset:0 sc1\n\t"
    "v_exp_f32 v30, v30\n\t"
    "v_exp_f32 v31, v31\n\t"
    "v_exp_f32 v32, v32\n\t"
    "v_cvt_pk_bf16_f32 v56, v24, v25\n\t"
    "v_cvt_pk_bf16_f32 v57, v26, v27\n\t"
    "v_cvt_pk_bf16_f32 v58, v28, v29\n\t"
    "v_cvt_pk_bf16_f32 v59, v30, v31\n\t"
    "v_exp_f32 v33, v33\n\t"
    "v_exp_f32 v34, v34\n\t"
    "v_smfmac_f32_16x16x64_bf16 v[252:255], v[104:107], v[64:71], v109\n\t"
    "s_waitcnt vmcnt(20)\n\t"
    "ds_read_b64_tr_b16 v[218:219], v12 offset:8960\n\t"
    "v_exp_f32 v35, v35\n\t"
    "v_exp_f32 v36, v36\n\t"
    "v_exp_f32 v37, v37\n\t"
    "v_exp_f32 v38, v38\n\t"
    "v_exp_f32 v39, v39\n\t"
    "v_cvt_pk_bf16_f32 v60, v32, v33\n\t"
    "v_cvt_pk_bf16_f32 v61, v34, v35\n\t"
    "v_cvt_pk_bf16_f32 v62, v36, v37\n\t"
    "v_cvt_pk_bf16_f32 v63, v38, v39\n\t"
    "v_mfma_f32_32x32x16_bf16 v[24:39], v[76:79], a[232:235], 0\n\t"
    "buffer_load_dwordx4 %[tv], %[sw2], s62 offen offset:3072 lds\n\t"
    "v_exp_f32 v40, v40\n\t"
    "v_exp_f32 v41, v41\n\t"
    "v_exp_f32 v42, v42\n\t"
    "v_exp_f32 v43, v43\n\t"
    "v_exp_f32 v44, v44\n\t"
    "v_exp_f32 v45, v45\n\t"
    "v_smfmac_f32_32x32x32_bf16 v[112:127], v[100:103], v[56:63], v108\n\t"
    "buffer_load_dwordx4 v[72:75], %[kv], %[sws], s60 offen offset:0 sc1\n\t"
    "v_exp_f32 v46, v46\n\t"
    "v_exp_f32 v47, v47\n\t"
    "v_exp_f32 v48, v48\n\t"
    "v_cvt_pk_bf16_f32 v64, v40, v41\n\t"
    "v_cvt_pk_bf16_f32 v65, v42, v43\n\t"
    "v_cvt_pk_bf16_f32 v66, v44, v45\n\t"
    "v_cvt_pk_bf16_f32 v67, v46, v47\n\t"
    "v_exp_f32 v49, v49\n\t"
    "v_exp_f32 v50, v50\n\t"
    "v_smfmac_f32_16x16x64_bf16 v[240:243], v[104:107], v[56:63], v109\n\t"
    "s_add_u32 m0, s91, 0x1000\n\t"
    "ds_read_b64_tr_b16 v[222:223], v13 offset:8960\n\t"
    "v_exp_f32 v51, v51\n\t"
    "v_exp_f32 v52, v52\n\t"
    "v_exp_f32 v53, v53\n\t"
    "v_exp_f32 v54, v54\n\t"
    "v_exp_f32 v55, v55\n\t"
    "v_cvt_pk_bf16_f32 v68, v48, v49\n\t"
    "v_cvt_pk_bf16_f32 v69, v50, v51\n\t"
    "v_cvt_pk_bf16_f32 v70, v52, v53\n\t"
    "v_cvt_pk_bf16_f32 v71, v54, v55\n\t"
    "v_mfma_f32_32x32x16_bf16 v[40:55], v[76:79], a[236:239], 0\n\t"
    "s_add_u32 s62, s89, 0x20000\n\t"
    "ds_read_b64_tr_b16 v[224:225], v12 offset:12288\n\t"
    "v_exp_f32 v24, v24\n\t"
    "v_exp_f32 v25, v25\n\t"
    "v_exp_f32 v26, v26\n\t"
    "v_exp_f32 v27, v27\n\t"
    "v_exp_f32 v28, v28\n\t"
    "v_exp_f32 v29, v29\n\t"
    "v_smfmac_f32_32x32x32_bf16 v[128:143], v[100:103], v[64:71], v108\n\t"
    "buffer_load_dwordx4 %[tv], %[sw2], s62 offen offset:0 lds\n\t"
    "v_exp_f32 v30, v30\n\t"
    "v_exp_f32 v31, v31\n\t"
    "v_exp_f32 v32, v32\n\t"
    "v_cvt_pk_bf16_f32 v56, v24, v25\n\t"
    "v_cvt_pk_bf16_f32 v57, v26, v27\n\t"
    "v_cvt_pk_bf16_f32 v58, v28, v29\n\t"
    "v_cvt_pk_bf16_f32 v59, v30, v31\n\t"
    "v_exp_f32 v33, v33\n\t"
    "v_exp_f32 v34, v34\n\t"
    "v_smfmac_f32_16x16x64_bf16 v[244:247], v[104:107], v[64:71], v109\n\t"
    "ds_read_b64_tr_b16 v[228:229], v13 offset:12288\n\t"
    "v_exp_f32 v35, v35\n\t"
    "v_exp_f32 v36, v36\n\t"
    "v_exp_f32 v37, v37\n\t"
    "v_exp_f32 v38, v38\n\t"
    "v_exp_f32 v39, v39\n\t"
    "v_cvt_pk_bf16_f32 v60, v32, v33\n\t"
    "v_cvt_pk_bf16_f32 v61, v34, v35\n\t"
    "v_cvt_pk_bf16_f32 v62, v36, v37\n\t"
    "v_cvt_pk_bf16_f32 v63, v38, v39\n\t"
    "v_mfma_f32_32x32x16_bf16 v[24:39], a[240:243], a[224:227], 0\n\t"
    "s_waitcnt lgkmcnt(2)\n\t"
    "ds_read_b64_tr_b16 v[226:227], v12 offset:12544\n\t"
    "v_exp_f32 v40, v40\n\t"
    "v_exp_f32 v41, v41\n\t"
    "v_exp_f32 v42, v42\n\t"
    "v_exp_f32 v43, v43\n\t"
    "v_exp_f32 v44, v44\n\t"
    "v_exp_f32 v45, v45\n\t"
    "v_smfmac_f32_32x32x32_bf16 v[144:159], v[100:103], v[56:63], v108\n\t"
    "buffer_load_dwordx4 %[tv], %[sw2], s62 offen offset:1024 lds\n\t"
    "v_exp_f32 v46, v46\n\t"
    "v_exp_f32 v47, v47\n\t"
    "v_exp_f32 v48, v48\n\t"
    "v_cvt_pk_bf16_f32 v64, v40, v41\n\t"
    "v_cvt_pk_bf16_f32 v65, v42, v43\n\t"
    "v_cvt_pk_bf16_f32 v66, v44, v45\n\t"
    "v_cvt_pk_bf16_f32 v67, v46, v47\n\t"
    "v_exp_f32 v49, v49\n\t"
    "v_exp_f32 v50, v50\n\t"
    "v_smfmac_f32_16x16x64_bf16 v[248:251], v[104:107], v[56:63], v109\n\t"
    "ds_read_b64_tr_b16 v[230:231], v13 offset:12544\n\t"
    "v_exp_f32 v51, v51\n\t"
    "v_exp_f32 v52, v52\n\t"
    "v_exp_f32 v53, v53\n\t"
    "v_exp_f32 v54, v54\n\t"
    "v_exp_f32 v55, v55\n\t"
    "v_cvt_pk_bf16_f32 v68, v48, v49\n\t"
    "v_cvt_pk_bf16_f32 v69, v50, v51\n\t"
    "v_cvt_pk_bf16_f32 v70, v52, v53\n\t"
    "v_cvt_pk_bf16_f32 v71, v54, v55\n\t"
    "v_mfma_f32_32x32x16_bf16 v[40:55], a[240:243], a[228:231], 0\n\t"
    "buffer_load_dwordx4 %[tv], %[sw2], s62 offen offset:2048 lds\n\t"
    "v_exp_f32 v24, v24\n\t"
    "v_exp_f32 v25, v25\n\t"
    "v_exp_f32 v26, v26\n\t"
    "v_exp_f32 v27, v27\n\t"
    "v_exp_f32 v28, v28\n\t"
    "v_exp_f32 v29, v29\n\t"
    "v_smfmac_f32_32x32x32_bf16 v[160:175], v[100:103], v[64:71], v108\n\t"
    "buffer_load_dwordx4 v[100:103], %[kv], %[sws], s61 offen offset:1024 sc1\n\t"
    "v_exp_f32 v30, v30\n\t"
    "v_exp_f32 v31, v31\n\t"
    "v_exp_f32 v32, v32\n\t"
    "v_cvt_pk_bf16_f32 v56, v24, v25\n\t"
    "v_cvt_pk_bf16_f32 v57, v26, v27\n\t"
    "v_cvt_pk_bf16_f32 v58, v28, v29\n\t"
    "v_cvt_pk_bf16_f32 v59, v30, v31\n\t"
    "v_exp_f32 v33, v33\n\t"
    "v_exp_f32 v34, v34\n\t"
    "v_smfmac_f32_16x16x64_bf16 v[252:255], v[104:107], v[64:71], v109\n\t"
    "s_waitcnt vmcnt(19)\n\t"
    "ds_read_b64_tr_b16 v[232:233], v12 offset:12800\n\t"
    "v_exp_f32 v35, v35\n\t"
    "v_exp_f32 v36, v36\n\t"
    "v_exp_f32 v37, v37\n\t"
    "v_exp_f32 v38, v38\n\t"
    "v_exp_f32 v39, v39\n\t"
    "v_cvt_pk_bf16_f32 v60, v32, v33\n\t"
    "v_cvt_pk_bf16_f32 v61, v34, v35\n\t"
    "v_cvt_pk_bf16_f32 v62, v36, v37\n\t"
    "v_cvt_pk_bf16_f32 v63, v38, v39\n\t"
    "v_mfma_f32_32x32x16_bf16 v[24:39], a[240:243], a[232:235], 0\n\t"
    "buffer_load_dwordx4 %[tv], %[sw2], s62 offen offset:3072 lds\n\t"
    "v_exp_f32 v40, v40\n\t"
    "v_exp_f32 v41, v41\n\t"
    "v_exp_f32 v42, v42\n\t"
    "v_exp_f32 v43, v43\n\t"
    "v_exp_f32 v44, v44\n\t"
    "v_exp_f32 v45, v45\n\t"
    "v_smfmac_f32_32x32x32_bf16 v[112:127], v[80:83], v[56:63], v108\n\t"
    "buffer_load_dwordx4 v[76:79], %[kv], %[sws], s60 offen offset:1024 sc1\n\t"
    "v_exp_f32 v46, v46\n\t"
    "v_exp_f32 v47, v47\n\t"
    "v_exp_f32 v48, v48\n\t"
    "v_cvt_pk_bf16_f32 v64, v40, v41\n\t"
    "v_cvt_pk_bf16_f32 v65, v42, v43\n\t"
    "v_cvt_pk_bf16_f32 v66, v44, v45\n\t"
    "v_cvt_pk_bf16_f32 v67, v46, v47\n\t"
    "v_exp_f32 v49, v49\n\t"
    "v_exp_f32 v50, v50\n\t"
    "v_smfmac_f32_16x16x64_bf16 v[240:243], v[104:107], v[56:63], v109\n\t"
    "s_add_u32 m0, s91, 0x2000\n\t"
    "ds_read_b64_tr_b16 v[236:237], v13 offset:12800\n\t"
    "v_exp_f32 v51, v51\n\t"
    "v_exp_f32 v52, v52\n\t"
    "v_exp_f32 v53, v53\n\t"
    "v_exp_f32 v54, v54\n\t"
    "v_exp_f32 v55, v55\n\t"
    "v_cvt_pk_bf16_f32 v68, v48, v49\n\t"
    "v_cvt_pk_bf16_f32 v69, v50, v51\n\t"
    "v_cvt_pk_bf16_f32 v70, v52, v53\n\t"
    "v_cvt_pk_bf16_f32 v71, v54, v55\n\t"
    "v_mfma_f32_32x32x16_bf16 v[40:55], a[240:243], a[236:239], 0\n\t"
    "s_add_u32 s62, s89, 0x28000\n\t"
    "ds_read_b64_tr_b16 v[234:235], v12 offset:13056\n\t"
    "v_exp_f32 v24, v24\n\t"
    "v_exp_f32 v25, v25\n\t"
    "v_exp_f32 v26, v26\n\t"
    "v_exp_f32 v27, v27\n\t"
    "v_exp_f32 v28, v28\n\t"
    "v_exp_f32 v29, v29\n\t"
    "v_smfmac_f32_32x32x32_bf16 v[128:143], v[80:83], v[64:71], v108\n\t"
    "buffer_load_dwordx4 %[tv], %[sw2], s62 offen offset:0 lds\n\t"
    "v_exp_f32 v30, v30\n\t"
    "v_exp_f32 v31, v31\n\t"
    "v_exp_f32 v32, v32\n\t"
    "v_cvt_pk_bf16_f32 v56, v24, v25\n\t"
    "v_cvt_pk_bf16_f32 v57, v26, v27\n\t"
    "v_cvt_pk_bf16_f32 v58, v28, v29\n\t"
    "v_cvt_pk_bf16_f32 v59, v30, v31\n\t"
    "v_exp_f32 v33, v33\n\t"
    "v_exp_f32 v34, v34\n\t"
    "v_smfmac_f32_16x16x64_bf16 v[244:247], v[104:107], v[64:71], v109\n\t"
    "ds_read_b64_tr_b16 v[238:239], v13 offset:13056\n\t"
    "v_exp_f32 v35, v35\n\t"
    "v_exp_f32 v36, v36\n\t"
    "v_exp_f32 v37, v37\n\t"
    "v_exp_f32 v38, v38\n\t"
    "v_exp_f32 v39, v39\n\t"
    "v_cvt_pk_bf16_f32 v60, v32, v33\n\t"
    "v_cvt_pk_bf16_f32 v61, v34, v35\n\t"
    "v_cvt_pk_bf16_f32 v62, v36, v37\n\t"
    "v_cvt_pk_bf16_f32 v63, v38, v39\n\t"
    "v_mfma_f32_32x32x16_bf16 v[24:39], a[244:247], a[224:227], 0\n\t"
    "ds_read_b64_tr_b16 a[0:1], v12 offset:16384\n\t"
    "v_exp_f32 v40, v40\n\t"
    "v_exp_f32 v41, v41\n\t"
    "v_exp_f32 v42, v42\n\t"
    "v_exp_f32 v43, v43\n\t"
    "v_exp_f32 v44, v44\n\t"
    "v_exp_f32 v45, v45\n\t"
    "v_smfmac_f32_32x32x32_bf16 v[144:159], v[80:83], v[56:63], v108\n\t"
    "buffer_load_dwordx4 %[tv], %[sw2], s62 offen offset:1024 lds\n\t"
    "v_exp_f32 v46, v46\n\t"
    "v_exp_f32 v47, v47\n\t"
    "v_exp_f32 v48, v48\n\t"
    "v_cvt_pk_bf16_f32 v64, v40, v41\n\t"
    "v_cvt_pk_bf16_f32 v65, v42, v43\n\t"
    "v_cvt_pk_bf16_f32 v66, v44, v45\n\t"
    "v_cvt_pk_bf16_f32 v67, v46, v47\n\t"
    "v_exp_f32 v49, v49\n\t"
    "v_exp_f32 v50, v50\n\t"
    "v_smfmac_f32_16x16x64_bf16 v[248:251], v[104:107], v[56:63], v109\n\t"
    "ds_read_b64_tr_b16 a[4:5], v13 offset:16384\n\t"
    "v_exp_f32 v51, v51\n\t"
    "v_exp_f32 v52, v52\n\t"
    "v_exp_f32 v53, v53\n\t"
    "v_exp_f32 v54, v54\n\t"
    "v_exp_f32 v55, v55\n\t"
    "v_cvt_pk_bf16_f32 v68, v48, v49\n\t"
    "v_cvt_pk_bf16_f32 v69, v50, v51\n\t"
    "v_cvt_pk_bf16_f32 v70, v52, v53\n\t"
    "v_cvt_pk_bf16_f32 v71, v54, v55\n\t"
    "v_mfma_f32_32x32x16_bf16 v[40:55], a[244:247], a[228:231], 0\n\t"
    "buffer_load_dwordx4 %[tv], %[sw2], s62 offen offset:2048 lds\n\t"
    "v_exp_f32 v24, v24\n\t"
    "v_exp_f32 v25, v25\n\t"
    "v_exp_f32 v26, v26\n\t"
    "v_exp_f32 v27, v27\n\t"
    "v_exp_f32 v28, v28\n\t"
    "v_exp_f32 v29, v29\n\t"
    "v_smfmac_f32_32x32x32_bf16 v[160:175], v[80:83], v[64:71], v108\n\t"
    "buffer_load_dwordx4 v[80:83], %[kv], %[sws], s61 offen offset:2048 sc1\n\t"
    "v_exp_f32 v30, v30\n\t"
    "v_exp_f32 v31, v31\n\t"
    "v_exp_f32 v32, v32\n\t"
    "v_cvt_pk_bf16_f32 v56, v24, v25\n\t"
    "v_cvt_pk_bf16_f32 v57, v26, v27\n\t"
    "v_cvt_pk_bf16_f32 v58, v28, v29\n\t"
    "v_cvt_pk_bf16_f32 v59, v30, v31\n\t"
    "v_exp_f32 v33, v33\n\t"
    "v_exp_f32 v34, v34\n\t"
    "v_smfmac_f32_16x16x64_bf16 v[252:255], v[104:107], v[64:71], v109\n\t"
    "s_waitcnt vmcnt(20)\n\t"
    "ds_read_b64_tr_b16 a[2:3], v12 offset:16640\n\t"
    "v_exp_f32 v35, v35\n\t"
    "v_exp_f32 v36, v36\n\t"
    "v_exp_f32 v37, v37\n\t"
    "v_exp_f32 v38, v38\n\t"
    "v_exp_f32 v39, v39\n\t"
    "v_cvt_pk_bf16_f32 v60, v32, v33\n\t"
    "v_cvt_pk_bf16_f32 v61, v34, v35\n\t"
    "v_cvt_pk_bf16_f32 v62, v36, v37\n\t"
    "v_cvt_pk_bf16_f32 v63, v38, v39\n\t"
    "v_mfma_f32_32x32x16_bf16 v[24:39], a[244:247], a[232:235], 0\n\t"
    "buffer_load_dwordx4 %[tv], %[sw2], s62 offen offset:3072 lds\n\t"
    "v_exp_f32 v40, v40\n\t"
    "v_exp_f32 v41, v41\n\t"
    "v_exp_f32 v42, v42\n\t"
    "v_exp_f32 v43, v43\n\t"
    "v_exp_f32 v44, v44\n\t"
    "v_exp_f32 v45, v45\n\t"
    "v_smfmac_f32_32x32x32_bf16 v[112:127], v[84:87], v[56:63], v108\n\t"
    "buffer_load_dwordx4 a[240:243], %[kv], %[sws], s60 offen offset:2048 sc1\n\t"
    "v_exp_f32 v46, v46\n\t"
    "v_exp_f32 v47, v47\n\t"
    "v_exp_f32 v48, v48\n\t"
    "v_cvt_pk_bf16_f32 v64, v40, v41\n\t"
    "v_cvt_pk_bf16_f32 v65, v42, v43\n\t"
    "v_cvt_pk_bf16_f32 v66, v44, v45\n\t"
    "v_cvt_pk_bf16_f32 v67, v46, v47\n\t"
    "v_exp_f32 v49, v49\n\t"
    "v_exp_f32 v50, v50\n\t"
    "v_smfmac_f32_16x16x64_bf16 v[240:243], v[104:107], v[56:63], v109\n\t"
    "s_waitcnt lgkmcnt(3)\n\t"
    "ds_read_b64_tr_b16 a[6:7], v13 offset:16640\n\t"
    "v_exp_f32 v51, v51\n\t"
    "v_exp_f32 v52, v52\n\t"
    "v_exp_f32 v53, v53\n\t"
    "v_exp_f32 v54, v54\n\t"
    "v_exp_f32 v55, v55\n\t"
    "v_cvt_pk_bf16_f32 v68, v48, v49\n\t"
    "v_cvt_pk_bf16_f32 v69, v50, v51\n\t"
    "v_cvt_pk_bf16_f32 v70, v52, v53\n\t"
    "v_cvt_pk_bf16_f32 v71, v54, v55\n\t"
    "v_mfma_f32_32x32x16_bf16 v[40:55], a[244:247], a[236:239], 0\n\t"
    "s_add_u32 m0, s90, 0x0\n\t"
    "ds_read_b64_tr_b16 a[8:9], v12 offset:16896\n\t"
    "v_exp_f32 v24, v24\n\t"
    "v_exp_f32 v25, v25\n\t"
    "v_exp_f32 v26, v26\n\t"
    "v_exp_f32 v27, v27\n\t"
    "v_exp_f32 v28, v28\n\t"
    "v_exp_f32 v29, v29\n\t"
    "v_smfmac_f32_32x32x32_bf16 v[128:143], v[84:87], v[64:71], v108\n\t"
    "s_add_u32 s62, s89, 0x30000\n\t"
    "v_exp_f32 v30, v30\n\t"
    "v_exp_f32 v31, v31\n\t"
    "v_exp_f32 v32, v32\n\t"
    "v_cvt_pk_bf16_f32 v56, v24, v25\n\t"
    "v_cvt_pk_bf16_f32 v57, v26, v27\n\t"
    "v_cvt_pk_bf16_f32 v58, v28, v29\n\t"
    "v_cvt_pk_bf16_f32 v59, v30, v31\n\t"
    "v_exp_f32 v33, v33\n\t"
    "v_exp_f32 v34, v34\n\t"
    "v_smfmac_f32_16x16x64_bf16 v[244:247], v[104:107], v[64:71], v109\n\t"
    "ds_read_b64_tr_b16 a[12:13], v13 offset:16896\n\t"
    "v_exp_f32 v35, v35\n\t"
    "v_exp_f32 v36, v36\n\t"
    "v_exp_f32 v37, v37\n\t"
    "v_exp_f32 v38, v38\n\t"
    "v_exp_f32 v39, v39\n\t"
    "v_cvt_pk_bf16_f32 v60, v32, v33\n\t"
    "v_cvt_pk_bf16_f32 v61, v34, v35\n\t"
    "v_cvt_pk_bf16_f32 v62, v36, v37\n\t"
    "v_cvt_pk_bf16_f32 v63, v38, v39\n\t"
    "v_mfma_f32_32x32x16_bf16 v[24:39], a[248:251], a[224:227], 0\n\t"
    "ds_read_b64_tr_b16 a[10:11], v12 offset:17152\n\t"
    "v_exp_f32 v40, v40\n\t"
    "v_exp_f32 v41, v41\n\t"
    "v_exp_f32 v42, v42\n\t"
    "v_exp_f32 v43, v43\n\t"
    "v_exp_f32 v44, v44\n\t"
    "v_exp_f32 v45, v45\n\t"
    "v_smfmac_f32_32x32x32_bf16 v[144:159], v[84:87], v[56:63], v108\n\t"
    "buffer_load_dwordx4 %[tv], %[sw2], s62 offen offset:0 lds\n\t"
    "v_exp_f32 v46, v46\n\t"
    "v_exp_f32 v47, v47\n\t"
    "v_exp_f32 v48, v48\n\t"
    "v_cvt_pk_bf16_f32 v64, v40, v41\n\t"
    "v_cvt_pk_bf16_f32 v65, v42, v43\n\t"
    "v_cvt_pk_bf16_f32 v66, v44, v45\n\t"
    "v_cvt_pk_bf16_f32 v67, v46, v47\n\t"
    "v_exp_f32 v49, v49\n\t"
    "v_exp_f32 v50, v50\n\t"
    "v_smfmac_f32_16x16x64_bf16 v[248:251], v[104:107], v[56:63], v109\n\t"
    "ds_read_b64_tr_b16 a[14:15], v13 offset:17152\n\t"
    "v_exp_f32 v51, v51\n\t"
    "v_exp_f32 v52, v52\n\t"
    "v_exp_f32 v53, v53\n\t"
    "v_exp_f32 v54, v54\n\t"
    "v_exp_f32 v55, v55\n\t"
    "v_cvt_pk_bf16_f32 v68, v48, v49\n\t"
    "v_cvt_pk_bf16_f32 v69, v50, v51\n\t"
    "v_cvt_pk_bf16_f32 v70, v52, v53\n\t"
    "v_cvt_pk_bf16_f32 v71, v54, v55\n\t"
    "v_mfma_f32_32x32x16_bf16 v[40:55], a[248:251], a[228:231], 0\n\t"
    "buffer_load_dwordx4 %[tv], %[sw2], s62 offen offset:1024 lds\n\t"
    "v_exp_f32 v24, v24\n\t"
    "v_exp_f32 v25, v25\n\t"
    "v_exp_f32 v26, v26\n\t"
    "v_exp_f32 v27, v27\n\t"
    "v_exp_f32 v28, v28\n\t"
    "v_exp_f32 v29, v29\n\t"
    "v_smfmac_f32_32x32x32_bf16 v[160:175], v[84:87], v[64:71], v108\n\t"
    "buffer_load_dwordx4 v[84:87], %[kv], %[sws], s61 offen offset:3072 sc1\n\t"
    "v_exp_f32 v30, v30\n\t"
    "v_exp_f32 v31, v31\n\t"
    "v_exp_f32 v32, v32\n\t"
    "v_cvt_pk_bf16_f32 v56, v24, v25\n\t"
    "v_cvt_pk_bf16_f32 v57, v26, v27\n\t"
    "v_cvt_pk_bf16_f32 v58, v28, v29\n\t"
    "v_cvt_pk_bf16_f32 v59, v30, v31\n\t"
    "v_exp_f32 v33, v33\n\t"
    "v_exp_f32 v34, v34\n\t"
    "v_smfmac_f32_16x16x64_bf16 v[252:255], v[104:107], v[64:71], v109\n\t"
    "buffer_load_dword %[fl], %[cdv], s[92:95], 0 offen\n\t"
    "v_exp_f32 v35, v35\n\t"
    "v_exp_f32 v36, v36\n\t"
    "v_exp_f32 v37, v37\n\t"
    "v_exp_f32 v38, v38\n\t"
    "v_exp_f32 v39, v39\n\t"
    "v_cvt_pk_bf16_f32 v60, v32, v33\n\t"
    "v_cvt_pk_bf16_f32 v61, v34, v35\n\t"
    "v_cvt_pk_bf16_f32 v62, v36, v37\n\t"
    "v_cvt_pk_bf16_f32 v63, v38, v39\n\t"
    "v_mfma_f32_32x32x16_bf16 v[24:39], a[248:251], a[232:235], 0\n\t"
    "buffer_load_dwordx4 %[tv], %[sw2], s62 offen offset:2048 lds\n\t"
    "v_exp_f32 v40, v40\n\t"
    "v_exp_f32 v41, v41\n\t"
    "v_exp_f32 v42, v42\n\t"
    "v_exp_f32 v43, v43\n\t"
    "v_exp_f32 v44, v44\n\t"
    "v_exp_f32 v45, v45\n\t"
    "v_smfmac_f32_32x32x32_bf16 v[112:127], v[88:91], v[56:63], v108\n\t"
    "buffer_load_dwordx4 a[244:247], %[kv], %[sws], s60 offen offset:3072 sc1\n\t"
    "v_exp_f32 v46, v46\n\t"
    "v_exp_f32 v47, v47\n\t"
    "v_exp_f32 v48, v48\n\t"
    "v_cvt_pk_bf16_f32 v64, v40, v41\n\t"
    "v_cvt_pk_bf16_f32 v65, v42, v43\n\t"
    "v_cvt_pk_bf16_f32 v66, v44, v45\n\t"
    "v_cvt_pk_bf16_f32 v67, v46, v47\n\t"
    "v_exp_f32 v49, v49\n\t"
    "v_exp_f32 v50, v50\n\t"
    "v_smfmac_f32_16x16x64_bf16 v[240:243], v[104:107], v[56:63], v109\n\t"
    "s_waitcnt vmcnt(24)\n\t"
    "ds_read_b64_tr_b16 a[16:17], v12 offset:20480\n\t"
    "v_exp_f32 v51, v51\n\t"
    "v_exp_f32 v52, v52\n\t"
    "v_exp_f32 v53, v53\n\t"
    "v_exp_f32 v54, v54\n\t"
    "v_exp_f32 v55, v55\n\t"
    "v_cvt_pk_bf16_f32 v68, v48, v49\n\t"
    "v_cvt_pk_bf16_f32 v69, v50, v51\n\t"
    "v_cvt_pk_bf16_f32 v70, v52, v53\n\t"
    "v_cvt_pk_bf16_f32 v71, v54, v55\n\t"
    "v_mfma_f32_32x32x16_bf16 v[40:55], a[248:251], a[236:239], 0\n\t"
    "s_mov_b32 s61, 0x205000\n\t"
    "ds_read_b64_tr_b16 a[20:21], v13 offset:20480\n\t"
    "v_exp_f32 v24, v24\n\t"
    "v_exp_f32 v25, v25\n\t"
    "v_exp_f32 v26, v26\n\t"
    "v_exp_f32 v27, v27\n\t"
    "v_exp_f32 v28, v28\n\t"
    "v_exp_f32 v29, v29\n\t"
    "v_smfmac_f32_32x32x32_bf16 v[128:143], v[88:91], v[64:71], v108\n\t"
    "buffer_load_dwordx4 %[tv], %[sw2], s62 offen offset:3072 lds\n\t"
    "v_exp_f32 v30, v30\n\t"
    "v_exp_f32 v31, v31\n\t"
    "v_exp_f32 v32, v32\n\t"
    "v_cvt_pk_bf16_f32 v56, v24, v25\n\t"
    "v_cvt_pk_bf16_f32 v57, v26, v27\n\t"
    "v_cvt_pk_bf16_f32 v58, v28, v29\n\t"
    "v_cvt_pk_bf16_f32 v59, v30, v31\n\t"
    "v_exp_f32 v33, v33\n\t"
    "v_exp_f32 v34, v34\n\t"
    "v_smfmac_f32_16x16x64_bf16 v[244:247], v[104:107], v[64:71], v109\n\t"
    "buffer_load_dword %[fl], %[twv], %[sw1], s63 offen\n\t"
    "v_exp_f32 v35, v35\n\t"
    "v_exp_f32 v36, v36\n\t"
    "v_exp_f32 v37, v37\n\t"
    "v_exp_f32 v38, v38\n\t"
    "v_exp_f32 v39, v39\n\t"
    "v_cvt_pk_bf16_f32 v60, v32, v33\n\t"
    "v_cvt_pk_bf16_f32 v61, v34, v35\n\t"
    "v_cvt_pk_bf16_f32 v62, v36, v37\n\t"
    "v_cvt_pk_bf16_f32 v63, v38, v39\n\t"
    "v_mfma_f32_32x32x16_bf16 v[24:39], a[252:255], a[224:227], 0\n\t"
    "s_mov_b32 s60, 0x5000\n\t"
    "ds_read_b64_tr_b16 a[18:19], v12 offset:20736\n\t"
    "v_exp_f32 v40, v40\n\t"
    "v_exp_f32 v41, v41\n\t"
    "v_exp_f32 v42, v42\n\t"
    "v_exp_f32 v43, v43\n\t"
    "v_exp_f32 v44, v44\n\t"
    "v_exp_f32 v45, v45\n\t"
    "v_smfmac_f32_32x32x32_bf16 v[144:159], v[88:91], v[56:63], v108\n\t"
    "s_waitcnt lgkmcnt(3)\n\t"
    "s_add_u32 m0, s90, 0x1000\n\t"
    "v_exp_f32 v46, v46\n\t"
    "v_exp_f32 v47, v47\n\t"
    "v_exp_f32 v48, v48\n\t"
    "v_cvt_pk_bf16_f32 v64, v40, v41\n\t"
    "v_cvt_pk_bf16_f32 v65, v42, v43\n\t"
    "v_cvt_pk_bf16_f32 v66, v44, v45\n\t"
    "v_cvt_pk_bf16_f32 v67, v46, v47\n\t"
    "v_exp_f32 v49, v49\n\t"
    "v_exp_f32 v50, v50\n\t"
    "v_smfmac_f32_16x16x64_bf16 v[248:251], v[104:107], v[56:63], v109\n\t"
    "s_add_u32 s62, s89, 0x38000\n\t"
    "ds_read_b64_tr_b16 a[22:23], v13 offset:20736\n\t"
    "v_exp_f32 v51, v51\n\t"
    "v_exp_f32 v52, v52\n\t"
    "v_exp_f32 v53, v53\n\t"
    "v_exp_f32 v54, v54\n\t"
    "v_exp_f32 v55, v55\n\t"
    "v_cvt_pk_bf16_f32 v68, v48, v49\n\t"
    "v_cvt_pk_bf16_f32 v69, v50, v51\n\t"
    "v_cvt_pk_bf16_f32 v70, v52, v53\n\t"
    "v_cvt_pk_bf16_f32 v71, v54, v55\n\t"
    "v_mfma_f32_32x32x16_bf16 v[40:55], a[252:255], a[228:231], 0\n\t"
    "buffer_load_dwordx4 %[tv], %[sw2], s62 offen offset:0 lds\n\t"
    "v_exp_f32 v24, v24\n\t"
    "v_exp_f32 v25, v25\n\t"
    "v_exp_f32 v26, v26\n\t"
    "v_exp_f32 v27, v27\n\t"
    "v_exp_f32 v28, v28\n\t"
    "v_exp_f32 v29, v29\n\t"
    "v_smfmac_f32_32x32x32_bf16 v[160:175], v[88:91], v[64:71], v108\n\t"
    "buffer_load_dwordx4 v[88:91], %[kv], %[sws], s61 offen offset:0 sc1\n\t"
    "v_exp_f32 v30, v30\n\t"
    "v_exp_f32 v31, v31\n\t"
    "v_exp_f32 v32, v32\n\t"
    "v_cvt_pk_bf16_f32 v56, v24, v25\n\t"
    "v_cvt_pk_bf16_f32 v57, v26, v27\n\t"
    "v_cvt_pk_bf16_f32 v58, v28, v29\n\t"
    "v_cvt_pk_bf16_f32 v59, v30, v31\n\t"
    "v_exp_f32 v33, v33\n\t"
    "v_exp_f32 v34, v34\n\t"
    "v_smfmac_f32_16x16x64_bf16 v[252:255], v[104:107], v[64:71], v109\n\t"
    "s_waitcnt vmcnt(22)\n\t"
    "ds_read_b64_tr_b16 a[24:25], v12 offset:20992\n\t"
    "v_exp_f32 v35, v35\n\t"
    "v_exp_f32 v36, v36\n\t"
    "v_exp_f32 v37, v37\n\t"
    "v_exp_f32 v38, v38\n\t"
    "v_exp_f32 v39, v39\n\t"
    "v_cvt_pk_bf16_f32 v60, v32, v33\n\t"
    "v_cvt_pk_bf16_f32 v61, v34, v35\n\t"
    "v_cvt_pk_bf16_f32 v62, v36, v37\n\t"
    "v_cvt_pk_bf16_f32 v63, v38, v39\n\t"
    "v_mfma_f32_32x32x16_bf16 v[24:39], a[252:255], a[232:235], 0\n\t"
    "buffer_load_dwordx4 %[tv], %[sw2], s62 offen offset:1024 lds\n\t"
    "v_exp_f32 v40, v40\n\t"
    "v_exp_f32 v41, v41\n\t"
    "v_exp_f32 v42, v42\n\t"
    "v_exp_f32 v43, v43\n\t"
    "v_exp_f32 v44, v44\n\t"
    "v_exp_f32 v45, v45\n\t"
    "v_smfmac_f32_32x32x32_bf16 v[112:127], v[92:95], v[56:63], v108\n\t"
    "buffer_load_dwordx4 a[248:251], %[kv], %[sws], s60 offen offset:0 sc1\n\t"
    "v_exp_f32 v46, v46\n\t"
    "v_exp_f32 v47, v47\n\t"
    "v_exp_f32 v48, v48\n\t"
    "v_cvt_pk_bf16_f32 v64, v40, v41\n\t"
    "v_cvt_pk_bf16_f32 v65, v42, v43\n\t"
    "v_cvt_pk_bf16_f32 v66, v44, v45\n\t"
    "v_cvt_pk_bf16_f32 v67, v46, v47\n\t"
    "v_exp_f32 v49, v49\n\t"
    "v_exp_f32 v50, v50\n\t"
    "v_smfmac_f32_16x16x64_bf16 v[240:243], v[104:107], v[56:63], v109\n\t"
    "ds_read_b64_tr_b16 a[28:29], v13 offset:20992\n\t"
    "v_exp_f32 v51, v51\n\t"
    "v_exp_f32 v52, v52\n\t"
    "v_exp_f32 v53, v53\n\t"
    "v_exp_f32 v54, v54\n\t"
    "v_exp_f32 v55, v55\n\t"
    "v_cvt_pk_bf16_f32 v68, v48, v49\n\t"
    "v_cvt_pk_bf16_f32 v69, v50, v51\n\t"
    "v_cvt_pk_bf16_f32 v70, v52, v53\n\t"
    "v_cvt_pk_bf16_f32 v71, v54, v55\n\t"
    "v_mfma_f32_32x32x16_bf16 v[40:55], a[252:255], a[236:239], 0\n\t"
    "ds_read_b64_tr_b16 a[26:27], v12 offset:21248\n\t"
    "v_exp_f32 v24, v24\n\t"
    "v_exp_f32 v25, v25\n\t"
    "v_exp_f32 v26, v26\n\t"
    "v_exp_f32 v27, v27\n\t"
    "v_exp_f32 v28, v28\n\t"
    "v_exp_f32 v29, v29\n\t"
    "v_smfmac_f32_32x32x32_bf16 v[128:143], v[92:95], v[64:71], v108\n\t"
    "buffer_load_dwordx4 %[tv], %[sw2], s62 offen offset:2048 lds\n\t"
    "v_exp_f32 v30, v30\n\t"
    "v_exp_f32 v31, v31\n\t"
    "v_exp_f32 v32, v32\n\t"
    "v_cvt_pk_bf16_f32 v56, v24, v25\n\t"
    "v_cvt_pk_bf16_f32 v57, v26, v27\n\t"
    "v_cvt_pk_bf16_f32 v58, v28, v29\n\t"
    "v_cvt_pk_bf16_f32 v59, v30, v31\n\t"
    "v_exp_f32 v33, v33\n\t"
    "v_exp_f32 v34, v34\n\t"
    "v_smfmac_f32_16x16x64_bf16 v[244:247], v[104:107], v[64:71], v109\n\t"
    "ds_read_b64_tr_b16 a[30:31], v13 offset:21248\n\t"
    "v_exp_f32 v35, v35\n\t"
    "v_exp_f32 v36, v36\n\t"
    "v_exp_f32 v37, v37\n\t"
    "v_exp_f32 v38, v38\n\t"
    "v_exp_f32 v39, v39\n\t"
    "v_cvt_pk_bf16_f32 v60, v32, v33\n\t"
    "v_cvt_pk_bf16_f32 v61, v34, v35\n\t"
    "v_cvt_pk_bf16_f32 v62, v36, v37\n\t"
    "v_cvt_pk_bf16_f32 v63, v38, v39\n\t"
    "v_mfma_f32_32x32x16_bf16 v[24:39], v[72:75], a[224:227], 0\n\t"
    "ds_read_b64_tr_b16 a[32:33], v12 offset:24576\n\t"
    "v_exp_f32 v40, v40\n\t"
    "v_exp_f32 v41, v41\n\t"
    "v_exp_f32 v42, v42\n\t"
    "v_exp_f32 v43, v43\n\t"
    "v_exp_f32 v44, v44\n\t"
    "v_exp_f32 v45, v45\n\t"
    "v_smfmac_f32_32x32x32_bf16 v[144:159], v[92:95], v[56:63], v108\n\t"
    "buffer_load_dwordx4 %[tv], %[sw2], s62 offen offset:3072 lds\n\t"
    "v_exp_f32 v46, v46\n\t"
    "v_exp_f32 v47, v47\n\t"
    "v_exp_f32 v48, v48\n\t"
    "v_cvt_pk_bf16_f32 v64, v40, v41\n\t"
    "v_cvt_pk_bf16_f32 v65, v42, v43\n\t"
    "v_cvt_pk_bf16_f32 v66, v44, v45\n\t"
    "v_cvt_pk_bf16_f32 v67, v46, v47\n\t"
    "v_exp_f32 v49, v49\n\t"
    "v_exp_f32 v50, v50\n\t"
    "v_smfmac_f32_16x16x64_bf16 v[248:251], v[104:107], v[56:63], v109\n\t"
    "s_add_u32 m0, s90, 0x2000\n\t"
    "ds_read_b64_tr_b16 a[36:37], v13 offset:24576\n\t"
    "v_exp_f32 v51, v51\n\t"
    "v_exp_f32 v52, v52\n\t"
    "v_exp_f32 v53, v53\n\t"
    "v_exp_f32 v54, v54\n\t"
    "v_exp_f32 v55, v55\n\t"
    "v_cvt_pk_bf16_f32 v68, v48, v49\n\t"
    "v_cvt_pk_bf16_f32 v69, v50, v51\n\t"
    "v_cvt_pk_bf16_f32 v70, v52, v53\n\t"
    "v_cvt_pk_bf16_f32 v71, v54, v55\n\t"
    "v_mfma_f32_32x32x16_bf16 v[40:55], v[72:75], a[228:231], 0\n\t"
    "s_waitcnt vmcnt(20)\n\t"
    "ds_read_b64_tr_b16 a[34:35], v12 offset:24832\n\t"
    "v_exp_f32 v24, v24\n\t"
    "v_exp_f32 v25, v25\n\t"
    "v_exp_f32 v26, v26\n\t"
    "v_exp_f32 v27, v27\n\t"
    "v_exp_f32 v28, v28\n\t"
    "v_exp_f32 v29, v29\n\t"
    "v_smfmac_f32_32x32x32_bf16 v[160:175], v[92:95], v[64:71], v108\n\t"
    "buffer_load_dwordx4 v[92:95], %[kv], %[sws], s61 offen offset:1024 sc1\n\t"
    "v_exp_f32 v30, v30\n\t"
    "v_exp_f32 v31, v31\n\t"
    "v_exp_f32 v32, v32\n\t"
    "v_cvt_pk_bf16_f32 v56, v24, v25\n\t"
    "v_cvt_pk_bf16_f32 v57, v26, v27\n\t"
    "v_cvt_pk_bf16_f32 v58, v28, v29\n\t"
    "v_cvt_pk_bf16_f32 v59, v30, v31\n\t"
    "v_exp_f32 v33, v33\n\t"
    "v_exp_f32 v34, v34\n\t"
    "v_smfmac_f32_16x16x64_bf16 v[252:255], v[104:107], v[64:71], v109\n\t"
    "s_add_u32 s62, s89, 0x40000\n\t"
    "ds_read_b64_tr_b16 a[38:39], v13 offset:24832\n\t"
    "v_exp_f32 v35, v35\n\t"
    "v_exp_f32 v36, v36\n\t"
    "v_exp_f32 v37, v37\n\t"
    "v_exp_f32 v38, v38\n\t"
    "v_exp_f32 v39, v39\n\t"
    "v_cvt_pk_bf16_f32 v60, v32, v33\n\t"
    "v_cvt_pk_bf16_f32 v61, v34, v35\n\t"
    "v_cvt_pk_bf16_f32 v62, v36, v37\n\t"
    "v_cvt_pk_bf16_f32 v63, v38, v39\n\t"
    "v_mfma_f32_32x32x16_bf16 v[24:39], v[72:75], a[232:235], 0\n\t"
    "buffer_load_dwordx4 %[tv], %[sw2], s62 offen offset:0 lds\n\t"
    "v_exp_f32 v40, v40\n\t"
    "v_exp_f32 v41, v41\n\t"
    "v_exp_f32 v42, v42\n\t"
    "v_exp_f32 v43, v43\n\t"
    "v_exp_f32 v44, v44\n\t"
    "v_exp_f32 v45, v45\n\t"
    "v_smfmac_f32_32x32x32_bf16 v[112:127], v[96:99], v[56:63], v108\n\t"
    "buffer_load_dwordx4 a[252:255], %[kv], %[sws], s60 offen offset:1024 sc1\n\t"
    "v_exp_f32 v46, v46\n\t"
    "v_exp_f32 v47, v47\n\t"
    "v_exp_f32 v48, v48\n\t"
    "v_cvt_pk_bf16_f32 v64, v40, v41\n\t"
    "v_cvt_pk_bf16_f32 v65, v42, v43\n\t"
    "v_cvt_pk_bf16_f32 v66, v44, v45\n\t"
    "v_cvt_pk_bf16_f32 v67, v46, v47\n\t"
    "v_exp_f32 v49, v49\n\t"
    "v_exp_f32 v50, v50\n\t"
    "v_smfmac_f32_16x16x64_bf16 v[240:243], v[104:107], v[56:63], v109\n\t"
    "s_waitcnt lgkmcnt(4)\n\t"
    "ds_read_b64_tr_b16 a[40:41], v12 offset:25088\n\t"
    "v_exp_f32 v51, v51\n\t"
    "v_exp_f32 v52, v52\n\t"
    "v_exp_f32 v53, v53\n\t"
    "v_exp_f32 v54, v54\n\t"
    "v_exp_f32 v55, v55\n\t"
    "v_cvt_pk_bf16_f32 v68, v48, v49\n\t"
    "v_cvt_pk_bf16_f32 v69, v50, v51\n\t"
    "v_cvt_pk_bf16_f32 v70, v52, v53\n\t"
    "v_cvt_pk_bf16_f32 v71, v54, v55\n\t"
    "v_mfma_f32_32x32x16_bf16 v[40:55], v[72:75], a[236:239], 0\n\t"
    "ds_read_b64_tr_b16 a[44:45], v13 offset:25088\n\t"
    "v_exp_f32 v24, v24\n\t"
    "v_exp_f32 v25, v25\n\t"
    "v_exp_f32 v26, v26\n\t"
    "v_exp_f32 v27, v27\n\t"
    "v_exp_f32 v28, v28\n\t"
    "v_exp_f32 v29, v29\n\t"
    "v_smfmac_f32_32x32x32_bf16 v[128:143], v[96:99], v[64:71], v108\n\t"
    "buffer_load_dwordx4 %[tv], %[sw2], s62 offen offset:1024 lds\n\t"
    "v_exp_f32 v30, v30\n\t"
    "v_exp_f32 v31, v31\n\t"
    "v_exp_f32 v32, v32\n\t"
    "v_cvt_pk_bf16_f32 v56, v24, v25\n\t"
    "v_cvt_pk_bf16_f32 v57, v26, v27\n\t"
    "v_cvt_pk_bf16_f32 v58, v28, v29\n\t"
    "v_cvt_pk_bf16_f32 v59, v30, v31\n\t"
    "v_exp_f32 v33, v33\n\t"
    "v_exp_f32 v34, v34\n\t"
    "v_smfmac_f32_16x16x64_bf16 v[244:247], v[104:107], v[64:71], v109\n\t"
    "ds_read_b64_tr_b16 a[42:43], v12 offset:25344\n\t"
    "v_exp_f32 v35, v35\n\t"
    "v_exp_f32 v36, v36\n\t"
    "v_exp_f32 v37, v37\n\t"
    "v_exp_f32 v38, v38\n\t"
    "v_exp_f32 v39, v39\n\t"
    "v_cvt_pk_bf16_f32 v60, v32, v33\n\t"
    "v_cvt_pk_bf16_f32 v61, v34, v35\n\t"
    "v_cvt_pk_bf16_f32 v62, v36, v37\n\t"
    "v_cvt_pk_bf16_f32 v63, v38, v39\n\t"
    "v_mfma_f32_32x32x16_bf16 v[24:39], v[76:79], a[224:227], 0\n\t"
    "ds_read_b64_tr_b16 a[46:47], v13 offset:25344\n\t"
    "v_exp_f32 v40, v40\n\t"
    "v_exp_f32 v41, v41\n\t"
    "v_exp_f32 v42, v42\n\t"
    "v_exp_f32 v43, v43\n\t"
    "v_exp_f32 v44, v44\n\t"
    "v_exp_f32 v45, v45\n\t"
    "v_smfmac_f32_32x32x32_bf16 v[144:159], v[96:99], v[56:63], v108\n\t"
    "buffer_load_dwordx4 %[tv], %[sw2], s62 offen offset:2048 lds\n\t"
    "v_exp_f32 v46, v46\n\t"
    "v_exp_f32 v47, v47\n\t"
    "v_exp_f32 v48, v48\n\t"
    "v_cvt_pk_bf16_f32 v64, v40, v41\n\t"
    "v_cvt_pk_bf16_f32 v65, v42, v43\n\t"
    "v_cvt_pk_bf16_f32 v66, v44, v45\n\t"
    "v_cvt_pk_bf16_f32 v67, v46, v47\n\t"
    "v_exp_f32 v49, v49\n\t"
    "v_exp_f32 v50, v50\n\t"
    "v_smfmac_f32_16x16x64_bf16 v[248:251], v[104:107], v[56:63], v109\n\t"
    "ds_read_b64_tr_b16 a[48:49], v14 offset:0\n\t"
    "v_exp_f32 v51, v51\n\t"
    "v_exp_f32 v52, v52\n\t"
    "v_exp_f32 v53, v53\n\t"
    "v_exp_f32 v54, v54\n\t"
    "v_exp_f32 v55, v55\n\t"
    "v_cvt_pk_bf16_f32 v68, v48, v49\n\t"
    "v_cvt_pk_bf16_f32 v69, v50, v51\n\t"
    "v_cvt_pk_bf16_f32 v70, v52, v53\n\t"
    "v_cvt_pk_bf16_f32 v71, v54, v55\n\t"
    "v_mfma_f32_32x32x16_bf16 v[40:55], v[76:79], a[228:231], 0\n\t"
    "buffer_load_dwordx4 %[tv], %[sw2], s62 offen offset:3072 lds\n\t"
    "v_exp_f32 v24, v24\n\t"
    "v_exp_f32 v25, v25\n\t"
    "v_exp_f32 v26, v26\n\t"
    "v_exp_f32 v27, v27\n\t"
    "v_exp_f32 v28, v28\n\t"
    "v_exp_f32 v29, v29\n\t"
    "v_smfmac_f32_32x32x32_bf16 v[160:175], v[96:99], v[64:71], v108\n\t"
    "buffer_load_dwordx4 v[96:99], %[kv], %[sws], s61 offen offset:2048 sc1\n\t"
    "v_exp_f32 v30, v30\n\t"
    "v_exp_f32 v31, v31\n\t"
    "v_exp_f32 v32, v32\n\t"
    "v_cvt_pk_bf16_f32 v56, v24, v25\n\t"
    "v_cvt_pk_bf16_f32 v57, v26, v27\n\t"
    "v_cvt_pk_bf16_f32 v58, v28, v29\n\t"
    "v_cvt_pk_bf16_f32 v59, v30, v31\n\t"
    "v_exp_f32 v33, v33\n\t"
    "v_exp_f32 v34, v34\n\t"
    "v_smfmac_f32_16x16x64_bf16 v[252:255], v[104:107], v[64:71], v109\n\t"
    "s_waitcnt vmcnt(21)\n\t"
    "ds_read_b64_tr_b16 a[52:53], v15 offset:0\n\t"
    "v_exp_f32 v35, v35\n\t"
    "v_exp_f32 v36, v36\n\t"
    "v_exp_f32 v37, v37\n\t"
    "v_exp_f32 v38, v38\n\t"
    "v_exp_f32 v39, v39\n\t"
    "v_cvt_pk_bf16_f32 v60, v32, v33\n\t"
    "v_cvt_pk_bf16_f32 v61, v34, v35\n\t"
    "v_cvt_pk_bf16_f32 v62, v36, v37\n\t"
    "v_cvt_pk_bf16_f32 v63, v38, v39\n\t"
    "v_mfma_f32_32x32x16_bf16 v[24:39], v[76:79], a[232:235], 0\n\t"
    "s_waitcnt lgkmcnt(2)\n\t"
    "ds_read_b64_tr_b16 a[50:51], v14 offset:256\n\t"
    "v_exp_f32 v40, v40\n\t"
    "v_exp_f32 v41, v41\n\t"
    "v_exp_f32 v42, v42\n\t"
    "v_exp_f32 v43, v43\n\t"
    "v_exp_f32 v44, v44\n\t"
    "v_exp_f32 v45, v45\n\t"
    "v_smfmac_f32_32x32x32_bf16 v[112:127], v[100:103], v[56:63], v108\n\t"
    "buffer_load_dwordx4 v[72:75], %[kv], %[sws], s60 offen offset:2048 sc1\n\t"
    "v_exp_f32 v46, v46\n\t"
    "v_exp_f32 v47, v47\n\t"
    "v_exp_f32 v48, v48\n\t"
    "v_cvt_pk_bf16_f32 v64, v40, v41\n\t"
    "v_cvt_pk_bf16_f32 v65, v42, v43\n\t"
    "v_cvt_pk_bf16_f32 v66, v44, v45\n\t"
    "v_cvt_pk_bf16_f32 v67, v46, v47\n\t"
    "v_exp_f32 v49, v49\n\t"
    "v_exp_f32 v50, v50\n\t"
    "v_smfmac_f32_16x16x64_bf16 v[240:243], v[104:107], v[56:63], v109\n\t"
    "s_add_u32 m0, s90, 0x3000\n\t"
    "ds_read_b64_tr_b16 a[54:55], v15 offset:256\n\t"
    "v_exp_f32 v51, v51\n\t"
    "v_exp_f32 v52, v52\n\t"
    "v_exp_f32 v53, v53\n\t"
    "v_exp_f32 v54, v54\n\t"
    "v_exp_f32 v55, v55\n\t"
    "v_cvt_pk_bf16_f32 v68, v48, v49\n\t"
    "v_cvt_pk_bf16_f32 v69, v50, v51\n\t"
    "v_cvt_pk_bf16_f32 v70, v52, v53\n\t"
    "v_cvt_pk_bf16_f32 v71, v54, v55\n\t"
    "v_mfma_f32_32x32x16_bf16 v[40:55], v[76:79], a[236:239], 0\n\t"
    "s_add_u32 s62, s89, 0x48000\n\t"
    "ds_read_b64_tr_b16 a[56:57], v14 offset:512\n\t"
    "v_exp_f32 v24, v24\n\t"
    "v_exp_f32 v25, v25\n\t"
    "v_exp_f32 v26, v26\n\t"
    "v_exp_f32 v27, v27\n\t"
    "v_exp_f32 v28, v28\n\t"
    "v_exp_f32 v29, v29\n\t"
    "v_smfmac_f32_32x32x32_bf16 v[128:143], v[100:103], v[64:71], v108\n\t"
    "buffer_load_dwordx4 %[tv], %[sw2], s62 offen offset:0 lds\n\t"
    "v_exp_f32 v30, v30\n\t"
    "v_exp_f32 v31, v31\n\t"
    "v_exp_f32 v32, v32\n\t"
    "v_cvt_pk_bf16_f32 v56, v24, v25\n\t"
    "v_cvt_pk_bf16_f32 v57, v26, v27\n\t"
    "v_cvt_pk_bf16_f32 v58, v28, v29\n\t"
    "v_cvt_pk_bf16_f32 v59, v30, v31\n\t"
    "v_exp_f32 v33, v33\n\t"
    "v_exp_f32 v34, v34\n\t"
    "v_smfmac_f32_16x16x64_bf16 v[244:247], v[104:107], v[64:71], v109\n\t"
    "buffer_load_dword %[fl], %[twv], %[sw1], 0 offen\n\t"
    "v_exp_f32 v35, v35\n\t"
    "v_exp_f32 v36, v36\n\t"
    "v_exp_f32 v37, v37\n\t"
    "v_exp_f32 v38, v38\n\t"
    "v_exp_f32 v39, v39\n\t"
    "v_cvt_pk_bf16_f32 v60, v32, v33\n\t"
    "v_cvt_pk_bf16_f32 v61, v34, v35\n\t"
    "v_cvt_pk_bf16_f32 v62, v36, v37\n\t"
    "v_cvt_pk_bf16_f32 v63, v38, v39\n\t"
    "v_mfma_f32_32x32x16_bf16 v[24:39], a[240:243], a[224:227], 0\n\t"
    "ds_read_b64_tr_b16 a[60:61], v15 offset:512\n\t"
    "v_exp_f32 v40, v40\n\t"
    "v_exp_f32 v41, v41\n\t"
    "v_exp_f32 v42, v42\n\t"
    "v_exp_f32 v43, v43\n\t"
    "v_exp_f32 v44, v44\n\t"
    "v_exp_f32 v45, v45\n\t"
    "v_smfmac_f32_32x32x32_bf16 v[144:159], v[100:103], v[56:63], v108\n\t"
    "buffer_load_dwordx4 %[tv], %[sw2], s62 offen offset:1024 lds\n\t"
    "v_exp_f32 v46, v46\n\t"
    "v_exp_f32 v47, v47\n\t"
    "v_exp_f32 v48, v48\n\t"
    "v_cvt_pk_bf16_f32 v64, v40, v41\n\t"
    "v_cvt_pk_bf16_f32 v65, v42, v43\n\t"
    "v_cvt_pk_bf16_f32 v66, v44, v45\n\t"
    "v_cvt_pk_bf16_f32 v67, v46, v47\n\t"
    "v_exp_f32 v49, v49\n\t"
    "v_exp_f32 v50, v50\n\t"
    "v_smfmac_f32_16x16x64_bf16 v[248:251], v[104:107], v[56:63], v109\n\t"
    "ds_read_b64_tr_b16 a[58:59], v14 offset:768\n\t"
    "v_exp_f32 v51, v51\n\t"
    "v_exp_f32 v52, v52\n\t"
    "v_exp_f32 v53, v53\n\t"
    "v_exp_f32 v54, v54\n\t"
    "v_exp_f32 v55, v55\n\t"
    "v_cvt_pk_bf16_f32 v68, v48, v49\n\t"
    "v_cvt_pk_bf16_f32 v69, v50, v51\n\t"
    "v_cvt_pk_bf16_f32 v70, v52, v53\n\t"
    "v_cvt_pk_bf16_f32 v71, v54, v55\n\t"
    "v_mfma_f32_32x32x16_bf16 v[40:55], a[240:243], a[228:231], 0\n\t"
    "buffer_load_dwordx4 %[tv], %[sw2], s62 offen offset:2048 lds\n\t"
    "v_exp_f32 v24, v24\n\t"
    "v_exp_f32 v25, v25\n\t"
    "v_exp_f32 v26, v26\n\t"
    "v_exp_f32 v27, v27\n\t"
    "v_exp_f32 v28, v28\n\t"
    "v_exp_f32 v29, v29\n\t"
    "v_smfmac_f32_32x32x32_bf16 v[160:175], v[100:103], v[64:71], v108\n\t"
    "buffer_load_dwordx4 v[100:103], %[kv], %[sws], s61 offen offset:3072 sc1\n\t"
    "v_exp_f32 v30, v30\n\t"
    "v_exp_f32 v31, v31\n\t"
    "v_exp_f32 v32, v32\n\t"
    "v_cvt_pk_bf16_f32 v56, v24, v25\n\t"
    "v_cvt_pk_bf16_f32 v57, v26, v27\n\t"
    "v_cvt_pk_bf16_f32 v58, v28, v29\n\t"
    "v_cvt_pk_bf16_f32 v59, v30, v31\n\t"
    "v_exp_f32 v33, v33\n\t"
    "v_exp_f32 v34, v34\n\t"
    "v_smfmac_f32_16x16x64_bf16 v[252:255], v[104:107], v[64:71], v109\n\t"
    "s_waitcnt vmcnt(21)\n\t"
    "ds_read_b64_tr_b16 a[62:63], v15 offset:768\n\t"
    "v_exp_f32 v35, v35\n\t"
    "v_exp_f32 v36, v36\n\t"
    "v_exp_f32 v37, v37\n\t"
    "v_exp_f32 v38, v38\n\t"
    "v_exp_f32 v39, v39\n\t"
    "v_cvt_pk_bf16_f32 v60, v32, v33\n\t"
    "v_cvt_pk_bf16_f32 v61, v34, v35\n\t"
    "v_cvt_pk_bf16_f32 v62, v36, v37\n\t"
    "v_cvt_pk_bf16_f32 v63, v38, v39\n\t"
    "v_mfma_f32_32x32x16_bf16 v[24:39], a[240:243], a[232:235], 0\n\t"
    "buffer_load_dwordx4 %[tv], %[sw2], s62 offen offset:3072 lds\n\t"
    "v_exp_f32 v40, v40\n\t"
    "v_exp_f32 v41, v41\n\t"
    "v_exp_f32 v42, v42\n\t"
    "v_exp_f32 v43, v43\n\t"
    "v_exp_f32 v44, v44\n\t"
    "v_exp_f32 v45, v45\n\t"
    "v_smfmac_f32_32x32x32_bf16 v[112:127], v[80:83], v[56:63], v108\n\t"
    "buffer_load_dwordx4 v[76:79], %[kv], %[sws], s60 offen offset:3072 sc1\n\t"
    "v_exp_f32 v46, v46\n\t"
    "v_exp_f32 v47, v47\n\t"
    "v_exp_f32 v48, v48\n\t"
    "v_cvt_pk_bf16_f32 v64, v40, v41\n\t"
    "v_cvt_pk_bf16_f32 v65, v42, v43\n\t"
    "v_cvt_pk_bf16_f32 v66, v44, v45\n\t"
    "v_cvt_pk_bf16_f32 v67, v46, v47\n\t"
    "v_exp_f32 v49, v49\n\t"
    "v_exp_f32 v50, v50\n\t"
    "v_smfmac_f32_16x16x64_bf16 v[240:243], v[104:107], v[56:63], v109\n\t"
    "s_add_u32 m0, s90, 0x4000\n\t"
    "ds_read_b64_tr_b16 a[64:65], v14 offset:4096\n\t"
    "v_exp_f32 v51, v51\n\t"
    "v_exp_f32 v52, v52\n\t"
    "v_exp_f32 v53, v53\n\t"
    "v_exp_f32 v54, v54\n\t"
    "v_exp_f32 v55, v55\n\t"
    "v_cvt_pk_bf16_f32 v68, v48, v49\n\t"
    "v_cvt_pk_bf16_f32 v69, v50, v51\n\t"
    "v_cvt_pk_bf16_f32 v70, v52, v53\n\t"
    "v_cvt_pk_bf16_f32 v71, v54, v55\n\t"
    "v_mfma_f32_32x32x16_bf16 v[40:55], a[240:243], a[236:239], 0\n\t"
    "s_mov_b32 s61, 0x206000\n\t"
    "ds_read_b64_tr_b16 a[68:69], v15 offset:4096\n\t"
    "v_exp_f32 v24, v24\n\t"
    "v_exp_f32 v25, v25\n\t"
    "v_exp_f32 v26, v26\n\t"
    "v_exp_f32 v27, v27\n\t"
    "v_exp_f32 v28, v28\n\t"
    "v_exp_f32 v29, v29\n\t"
    "v_smfmac_f32_32x32x32_bf16 v[128:143], v[80:83], v[64:71], v108\n\t"
    "s_mov_b32 s60, 0x6000\n\t"
    "s_add_u32 s62, s89, 0x50000\n\t"
    "v_exp_f32 v30, v30\n\t"
    "v_exp_f32 v31, v31\n\t"
    "v_exp_f32 v32, v32\n\t"
    "v_cvt_pk_bf16_f32 v56, v24, v25\n\t"
    "v_cvt_pk_bf16_f32 v57, v26, v27\n\t"
    "v_cvt_pk_bf16_f32 v58, v28, v29\n\t"
    "v_cvt_pk_bf16_f32 v59, v30, v31\n\t"
    "v_exp_f32 v33, v33\n\t"
    "v_exp_f32 v34, v34\n\t"
    "v_smfmac_f32_16x16x64_bf16 v[244:247], v[104:107], v[64:71], v109\n\t"
    "s_waitcnt lgkmcnt(2)\n\t"
    "ds_read_b64_tr_b16 a[66:67], v14 offset:4352\n\t"
    "v_exp_f32 v35, v35\n\t"
    "v_exp_f32 v36, v36\n\t"
    "v_exp_f32 v37, v37\n\t"
    "v_exp_f32 v38, v38\n\t"
    "v_exp_f32 v39, v39\n\t"
    "v_cvt_pk_bf16_f32 v60, v32, v33\n\t"
    "v_cvt_pk_bf16_f32 v61, v34, v35\n\t"
    "v_cvt_pk_bf16_f32 v62, v36, v37\n\t"
    "v_cvt_pk_bf16_f32 v63, v38, v39\n\t"
    "v_mfma_f32_32x32x16_bf16 v[24:39], a[244:247], a[224:227], 0\n\t"
    "ds_read_b64_tr_b16 a[70:71], v15 offset:4352\n\t"
    "v_exp_f32 v40, v40\n\t"
    "v_exp_f32 v41, v41\n\t"
    "v_exp_f32 v42, v42\n\t"
    "v_exp_f32 v43, v43\n\t"
    "v_exp_f32 v44, v44\n\t"
    "v_exp_f32 v45, v45\n\t"
    "v_smfmac_f32_32x32x32_bf16 v[144:159], v[80:83], v[56:63], v108\n\t"
    "buffer_load_dwordx4 %[tv], %[sw2], s62 offen offset:0 lds\n\t"
    "v_exp_f32 v46, v46\n\t"
    "v_exp_f32 v47, v47\n\t"
    "v_exp_f32 v48, v48\n\t"
    "v_cvt_pk_bf16_f32 v64, v40, v41\n\t"
    "v_cvt_pk_bf16_f32 v65, v42, v43\n\t"
    "v_cvt_pk_bf16_f32 v66, v44, v45\n\t"
    "v_cvt_pk_bf16_f32 v67, v46, v47\n\t"
    "v_exp_f32 v49, v49\n\t"
    "v_exp_f32 v50, v50\n\t"
    "v_smfmac_f32_16x16x64_bf16 v[248:251], v[104:107], v[56:63], v109\n\t"
    "ds_read_b64_tr_b16 a[72:73], v14 offset:4608\n\t"
    "v_exp_f32 v51, v51\n\t"
    "v_exp_f32 v52, v52\n\t"
    "v_exp_f32 v53, v53\n\t"
    "v_exp_f32 v54, v54\n\t"
    "v_exp_f32 v55, v55\n\t"
    "v_cvt_pk_bf16_f32 v68, v48, v49\n\t"
    "v_cvt_pk_bf16_f32 v69, v50, v51\n\t"
    "v_cvt_pk_bf16_f32 v70, v52, v53\n\t"
    "v_cvt_pk_bf16_f32 v71, v54, v55\n\t"
    "v_mfma_f32_32x32x16_bf16 v[40:55], a[244:247], a[228:231], 0\n\t"
    "buffer_load_dwordx4 %[tv], %[sw2], s62 offen offset:1024 lds\n\t"
    "v_exp_f32 v24, v24\n\t"
    "v_exp_f32 v25, v25\n\t"
    "v_exp_f32 v26, v26\n\t"
    "v_exp_f32 v27, v27\n\t"
    "v_exp_f32 v28, v28\n\t"
    "v_exp_f32 v29, v29\n\t"
    "v_smfmac_f32_32x32x32_bf16 v[160:175], v[80:83], v[64:71], v108\n\t"
    "buffer_load_dwordx4 v[80:83], %[kv], %[sws], s61 offen offset:0 sc1\n\t"
    "v_exp_f32 v30, v30\n\t"
    "v_exp_f32 v31, v31\n\t"
    "v_exp_f32 v32, v32\n\t"
    "v_cvt_pk_bf16_f32 v56, v24, v25\n\t"
    "v_cvt_pk_bf16_f32 v57, v26, v27\n\t"
    "v_cvt_pk_bf16_f32 v58, v28, v29\n\t"
    "v_cvt_pk_bf16_f32 v59, v30, v31\n\t"
    "v_exp_f32 v33, v33\n\t"
    "v_exp_f32 v34, v34\n\t"
    "v_smfmac_f32_16x16x64_bf16 v[252:255], v[104:107], v[64:71], v109\n\t"
    "s_waitcnt vmcnt(20)\n\t"
    "ds_read_b64_tr_b16 a[76:77], v15 offset:4608\n\t"
    "v_exp_f32 v35, v35\n\t"
    "v_exp_f32 v36, v36\n\t"
    "v_exp_f32 v37, v37\n\t"
    "v_exp_f32 v38, v38\n\t"
    "v_exp_f32 v39, v39\n\t"
    "v_cvt_pk_bf16_f32 v60, v32, v33\n\t"
    "v_cvt_pk_bf16_f32 v61, v34, v35\n\t"
    "v_cvt_pk_bf16_f32 v62, v36, v37\n\t"
    "v_cvt_pk_bf16_f32 v63, v38, v39\n\t"
    "v_mfma_f32_32x32x16_bf16 v[24:39], a[244:247], a[232:235], 0\n\t"
    "buffer_load_dwordx4 %[tv], %[sw2], s62 offen offset:2048 lds\n\t"
    "v_exp_f32 v40, v40\n\t"
    "v_exp_f32 v41, v41\n\t"
    "v_exp_f32 v42, v42\n\t"
    "v_exp_f32 v43, v43\n\t"
    "v_exp_f32 v44, v44\n\t"
    "v_exp_f32 v45, v45\n\t"
    "v_smfmac_f32_32x32x32_bf16 v[112:127], v[84:87], v[56:63], v108\n\t"
    "buffer_load_dwordx4 a[240:243], %[kv], %[sws], s60 offen offset:0 sc1\n\t"
    "v_exp_f32 v46, v46\n\t"
    "v_exp_f32 v47, v47\n\t"
    "v_exp_f32 v48, v48\n\t"
    "v_cvt_pk_bf16_f32 v64, v40, v41\n\t"
    "v_cvt_pk_bf16_f32 v65, v42, v43\n\t"
    "v_cvt_pk_bf16_f32 v66, v44, v45\n\t"
    "v_cvt_pk_bf16_f32 v67, v46, v47\n\t"
    "v_exp_f32 v49, v49\n\t"
    "v_exp_f32 v50, v50\n\t"
    "v_smfmac_f32_16x16x64_bf16 v[240:243], v[104:107], v[56:63], v109\n\t"
    "ds_read_b64_tr_b16 a[74:75], v14 offset:4864\n\t"
    "v_exp_f32 v51, v51\n\t"
    "v_exp_f32 v52, v52\n\t"
    "v_exp_f32 v53, v53\n\t"
    "v_exp_f32 v54, v54\n\t"
    "v_exp_f32 v55, v55\n\t"
    "v_cvt_pk_bf16_f32 v68, v48, v49\n\t"
    "v_cvt_pk_bf16_f32 v69, v50, v51\n\t"
    "v_cvt_pk_bf16_f32 v70, v52, v53\n\t"
    "v_cvt_pk_bf16_f32 v71, v54, v55\n\t"
    "v_mfma_f32_32x32x16_bf16 v[40:55], a[244:247], a[236:239], 0\n\t"
    "ds_read_b64_tr_b16 a[78:79], v15 offset:4864\n\t"
    "v_exp_f32 v24, v24\n\t"
    "v_exp_f32 v25, v25\n\t"
    "v_exp_f32 v26, v26\n\t"
    "v_exp_f32 v27, v27\n\t"
    "v_exp_f32 v28, v28\n\t"
    "v_exp_f32 v29, v29\n\t"
    "v_smfmac_f32_32x32x32_bf16 v[128:143], v[84:87], v[64:71], v108\n\t"
    "buffer_load_dwordx4 %[tv], %[sw2], s62 offen offset:3072 lds\n\t"
    "v_exp_f32 v30, v30\n\t"
    "v_exp_f32 v31, v31\n\t"
    "v_exp_f32 v32, v32\n\t"
    "v_cvt_pk_bf16_f32 v56, v24, v25\n\t"
    "v_cvt_pk_bf16_f32 v57, v26, v27\n\t"
    "v_cvt_pk_bf16_f32 v58, v28, v29\n\t"
    "v_cvt_pk_bf16_f32 v59, v30, v31\n\t"
    "v_exp_f32 v33, v33\n\t"
    "v_exp_f32 v34, v34\n\t"
    "v_smfmac_f32_16x16x64_bf16 v[244:247], v[104:107], v[64:71], v109\n\t"
    "s_add_u32 m0, s91, 0x0\n\t"
    "ds_read_b64_tr_b16 a[80:81], v14 offset:8192\n\t"
    "v_exp_f32 v35, v35\n\t"
    "v_exp_f32 v36, v36\n\t"
    "v_exp_f32 v37, v37\n\t"
    "v_exp_f32 v38, v38\n\t"
    "v_exp_f32 v39, v39\n\t"
    "v_cvt_pk_bf16_f32 v60, v32, v33\n\t"
    "v_cvt_pk_bf16_f32 v61, v34, v35\n\t"
    "v_cvt_pk_bf16_f32 v62, v36, v37\n\t"
    "v_cvt_pk_bf16_f32 v63, v38, v39\n\t"
    "v_mfma_f32_32x32x16_bf16 v[24:39], a[248:251], a[224:227], 0\n\t"
    "s_add_u32 s62, s89, 0x58000\n\t"
    "ds_read_b64_tr_b16 a[84:85], v15 offset:8192\n\t"
    "v_exp_f32 v40, v40\n\t"
    "v_exp_f32 v41, v41\n\t"
    "v_exp_f32 v42, v42\n\t"
    "v_exp_f32 v43, v43\n\t"
    "v_exp_f32 v44, v44\n\t"
    "v_exp_f32 v45, v45\n\t"
    "v_smfmac_f32_32x32x32_bf16 v[144:159], v[84:87], v[56:63], v108\n\t"
    "buffer_load_dwordx4 %[tv], %[sw2], s62 offen offset:0 lds\n\t"
    "v_exp_f32 v46, v46\n\t"
    "v_exp_f32 v47, v47\n\t"
    "v_exp_f32 v48, v48\n\t"
    "v_cvt_pk_bf16_f32 v64, v40, v41\n\t"
    "v_cvt_pk_bf16_f32 v65, v42, v43\n\t"
    "v_cvt_pk_bf16_f32 v66, v44, v45\n\t"
    "v_cvt_pk_bf16_f32 v67, v46, v47\n\t"
    "v_exp_f32 v49, v49\n\t"
    "v_exp_f32 v50, v50\n\t"
    "v_smfmac_f32_16x16x64_bf16 v[248:251], v[104:107], v[56:63], v109\n\t"
    "ds_read_b64_tr_b16 a[82:83], v14 offset:8448\n\t"
    "v_exp_f32 v51, v51\n\t"
    "v_exp_f32 v52, v52\n\t"
    "v_exp_f32 v53, v53\n\t"
    "v_exp_f32 v54, v54\n\t"
    "v_exp_f32 v55, v55\n\t"
    "v_cvt_pk_bf16_f32 v68, v48, v49\n\t"
    "v_cvt_pk_bf16_f32 v69, v50, v51\n\t"
    "v_cvt_pk_bf16_f32 v70, v52, v53\n\t"
    "v_cvt_pk_bf16_f32 v71, v54, v55\n\t"
    "v_mfma_f32_32x32x16_bf16 v[40:55], a[248:251], a[228:231], 0\n\t"
    "buffer_load_dwordx4 %[tv], %[sw2], s62 offen offset:1024 lds\n\t"
    "v_exp_f32 v24, v24\n\t"
    "v_exp_f32 v25, v25\n\t"
    "v_exp_f32 v26, v26\n\t"
    "v_exp_f32 v27, v27\n\t"
    "v_exp_f32 v28, v28\n\t"
    "v_exp_f32 v29, v29\n\t"
    "v_smfmac_f32_32x32x32_bf16 v[160:175], v[84:87], v[64:71], v108\n\t"
    "buffer_load_dwordx4 v[84:87], %[kv], %[sws], s61 offen offset:1024 sc1\n\t"
    "v_exp_f32 v30, v30\n\t"
    "v_exp_f32 v31, v31\n\t"
    "v_exp_f32 v32, v32\n\t"
    "v_cvt_pk_bf16_f32 v56, v24, v25\n\t"
    "v_cvt_pk_bf16_f32 v57, v26, v27\n\t"
    "v_cvt_pk_bf16_f32 v58, v28, v29\n\t"
    "v_cvt_pk_bf16_f32 v59, v30, v31\n\t"
    "v_exp_f32 v33, v33\n\t"
    "v_exp_f32 v34, v34\n\t"
    "v_smfmac_f32_16x16x64_bf16 v[252:255], v[104:107], v[64:71], v109\n\t"
    "buffer_load_dword %[fl], %[cdv], s[92:95], 0 offen\n\t"
    "v_exp_f32 v35, v35\n\t"
    "v_exp_f32 v36, v36\n\t"
    "v_exp_f32 v37, v37\n\t"
    "v_exp_f32 v38, v38\n\t"
    "v_exp_f32 v39, v39\n\t"
    "v_cvt_pk_bf16_f32 v60, v32, v33\n\t"
    "v_cvt_pk_bf16_f32 v61, v34, v35\n\t"
    "v_cvt_pk_bf16_f32 v62, v36, v37\n\t"
    "v_cvt_pk_bf16_f32 v63, v38, v39\n\t"
    "v_mfma_f32_32x32x16_bf16 v[24:39], a[248:251], a[232:235], 0\n\t"
    "buffer_load_dwordx4 %[tv], %[sw2], s62 offen offset:2048 lds\n\t"
    "v_exp_f32 v40, v40\n\t"
    "v_exp_f32 v41, v41\n\t"
    "v_exp_f32 v42, v42\n\t"
    "v_exp_f32 v43, v43\n\t"
    "v_exp_f32 v44, v44\n\t"
    "v_exp_f32 v45, v45\n\t"
    "v_smfmac_f32_32x32x32_bf16 v[112:127], v[88:91], v[56:63], v108\n\t"
    "buffer_load_dwordx4 a[244:247], %[kv], %[sws], s60 offen offset:1024 sc1\n\t"
    "v_exp_f32 v46, v46\n\t"
    "v_exp_f32 v47, v47\n\t"
    "v_exp_f32 v48, v48\n\t"
    "v_cvt_pk_bf16_f32 v64, v40, v41\n\t"
    "v_cvt_pk_bf16_f32 v65, v42, v43\n\t"
    "v_cvt_pk_bf16_f32 v66, v44, v45\n\t"
    "v_cvt_pk_bf16_f32 v67, v46, v47\n\t"
    "v_exp_f32 v49, v49\n\t"
    "v_exp_f32 v50, v50\n\t"
    "v_smfmac_f32_16x16x64_bf16 v[240:243], v[104:107], v[56:63], v109\n\t"
    "s_waitcnt vmcnt(24)\n\t"
    "ds_read_b64_tr_b16 a[86:87], v15 offset:8448\n\t"
    "v_exp_f32 v51, v51\n\t"
    "v_exp_f32 v52, v52\n\t"
    "v_exp_f32 v53, v53\n\t"
    "v_exp_f32 v54, v54\n\t"
    "v_exp_f32 v55, v55\n\t"
    "v_cvt_pk_bf16_f32 v68, v48, v49\n\t"
    "v_cvt_pk_bf16_f32 v69, v50, v51\n\t"
    "v_cvt_pk_bf16_f32 v70, v52, v53\n\t"
    "v_cvt_pk_bf16_f32 v71, v54, v55\n\t"
    "v_mfma_f32_32x32x16_bf16 v[40:55], a[248:251], a[236:239], 0\n\t"
    "s_waitcnt lgkmcnt(4)\n\t"
    "ds_read_b64_tr_b16 a[88:89], v14 offset:8704\n\t"
    "v_exp_f32 v24, v24\n\t"
    "v_exp_f32 v25, v25\n\t"
    "v_exp_f32 v26, v26\n\t"
    "v_exp_f32 v27, v27\n\t"
    "v_exp_f32 v28, v28\n\t"
    "v_exp_f32 v29, v29\n\t"
    "v_smfmac_f32_32x32x32_bf16 v[128:143], v[88:91], v[64:71], v108\n\t"
    "buffer_load_dwordx4 %[tv], %[sw2], s62 offen offset:3072 lds\n\t"
    "v_exp_f32 v30, v30\n\t"
    "v_exp_f32 v31, v31\n\t"
    "v_exp_f32 v32, v32\n\t"
    "v_cvt_pk_bf16_f32 v56, v24, v25\n\t"
    "v_cvt_pk_bf16_f32 v57, v26, v27\n\t"
    "v_cvt_pk_bf16_f32 v58, v28, v29\n\t"
    "v_cvt_pk_bf16_f32 v59, v30, v31\n\t"
    "v_exp_f32 v33, v33\n\t"
    "v_exp_f32 v34, v34\n\t"
    "v_smfmac_f32_16x16x64_bf16 v[244:247], v[104:107], v[64:71], v109\n\t"
    "s_add_u32 m0, s91, 0x1000\n\t"
    "ds_read_b64_tr_b16 a[92:93], v15 offset:8704\n\t"
    "v_exp_f32 v35, v35\n\t"
    "v_exp_f32 v36, v36\n\t"
    "v_exp_f32 v37, v37\n\t"
    "v_exp_f32 v38, v38\n\t"
    "v_exp_f32 v39, v39\n\t"
    "v_cvt_pk_bf16_f32 v60, v32, v33\n\t"
    "v_cvt_pk_bf16_f32 v61, v34, v35\n\t"
    "v_cvt_pk_bf16_f32 v62, v36, v37\n\t"
    "v_cvt_pk_bf16_f32 v63, v38, v39\n\t"
    "v_mfma_f32_32x32x16_bf16 v[24:39], a[252:255], a[224:227], 0\n\t"
    "s_add_u32 s62, s89, 0x60000\n\t"
    "ds_read_b64_tr_b16 a[90:91], v14 offset:8960\n\t"
    "v_exp_f32 v40, v40\n\t"
    "v_exp_f32 v41, v41\n\t"
    "v_exp_f32 v42, v42\n\t"
    "v_exp_f32 v43, v43\n\t"
    "v_exp_f32 v44, v44\n\t"
    "v_exp_f32 v45, v45\n\t"
    "v_smfmac_f32_32x32x32_bf16 v[144:159], v[88:91], v[56:63], v108\n\t"
    "buffer_load_dwordx4 %[tv], %[sw2], s62 offen offset:0 lds\n\t"
    "v_exp_f32 v46, v46\n\t"
    "v_exp_f32 v47, v47\n\t"
    "v_exp_f32 v48, v48\n\t"
    "v_cvt_pk_bf16_f32 v64, v40, v41\n\t"
    "v_cvt_pk_bf16_f32 v65, v42, v43\n\t"
    "v_cvt_pk_bf16_f32 v66, v44, v45\n\t"
    "v_cvt_pk_bf16_f32 v67, v46, v47\n\t"
    "v_exp_f32 v49, v49\n\t"
    "v_exp_f32 v50, v50\n\t"
    "v_smfmac_f32_16x16x64_bf16 v[248:251], v[104:107], v[56:63], v109\n\t"
    "ds_read_b64_tr_b16 a[94:95], v15 offset:8960\n\t"
    "v_exp_f32 v51, v51\n\t"
    "v_exp_f32 v52, v52\n\t"
    "v_exp_f32 v53, v53\n\t"
    "v_exp_f32 v54, v54\n\t"
    "v_exp_f32 v55, v55\n\t"
    "v_cvt_pk_bf16_f32 v68, v48, v49\n\t"
    "v_cvt_pk_bf16_f32 v69, v50, v51\n\t"
    "v_cvt_pk_bf16_f32 v70, v52, v53\n\t"
    "v_cvt_pk_bf16_f32 v71, v54, v55\n\t"
    "v_mfma_f32_32x32x16_bf16 v[40:55], a[252:255], a[228:231], 0\n\t"
    "buffer_load_dwordx4 %[tv], %[sw2], s62 offen offset:1024 lds\n\t"
    "v_exp_f32 v24, v24\n\t"
    "v_exp_f32 v25, v25\n\t"
    "v_exp_f32 v26, v26\n\t"
    "v_exp_f32 v27, v27\n\t"
    "v_exp_f32 v28, v28\n\t"
    "v_exp_f32 v29, v29\n\t"
    "v_smfmac_f32_32x32x32_bf16 v[160:175], v[88:91], v[64:71], v108\n\t"
    "buffer_load_dwordx4 v[88:91], %[kv], %[sws], s61 offen offset:2048 sc1\n\t"
    "v_exp_f32 v30, v30\n\t"
    "v_exp_f32 v31, v31\n\t"
    "v_exp_f32 v32, v32\n\t"
    "v_cvt_pk_bf16_f32 v56, v24, v25\n\t"
    "v_cvt_pk_bf16_f32 v57, v26, v27\n\t"
    "v_cvt_pk_bf16_f32 v58, v28, v29\n\t"
    "v_cvt_pk_bf16_f32 v59, v30, v31\n\t"
    "v_exp_f32 v33, v33\n\t"
    "v_exp_f32 v34, v34\n\t"
    "v_smfmac_f32_16x16x64_bf16 v[252:255], v[104:107], v[64:71], v109\n\t"
    "s_waitcnt vmcnt(23)\n\t"
    "ds_read_b64_tr_b16 a[96:97], v12 offset:0\n\t"
    "v_exp_f32 v35, v35\n\t"
    "v_exp_f32 v36, v36\n\t"
    "v_exp_f32 v37, v37\n\t"
    "v_exp_f32 v38, v38\n\t"
    "v_exp_f32 v39, v39\n\t"
    "v_cvt_pk_bf16_f32 v60, v32, v33\n\t"
    "v_cvt_pk_bf16_f32 v61, v34, v35\n\t"
    "v_cvt_pk_bf16_f32 v62, v36, v37\n\t"
    "v_cvt_pk_bf16_f32 v63, v38, v39\n\t"
    "v_mfma_f32_32x32x16_bf16 v[24:39], a[252:255], a[232:235], 0\n\t"
    "buffer_load_dwordx4 %[tv], %[sw2], s62 offen offset:2048 lds\n\t"
    "v_exp_f32 v40, v40\n\t"
    "v_exp_f32 v41, v41\n\t"
    "v_exp_f32 v42, v42\n\t"
    "v_exp_f32 v43, v43\n\t"
    "v_exp_f32 v44, v44\n\t"
    "v_exp_f32 v45, v45\n\t"
    "v_smfmac_f32_32x32x32_bf16 v[112:127], v[92:95], v[56:63], v108\n\t"
    "buffer_load_dwordx4 a[248:251], %[kv], %[sws], s60 offen offset:2048 sc1\n\t"
    "v_exp_f32 v46, v46\n\t"
    "v_exp_f32 v47, v47\n\t"
    "v_exp_f32 v48, v48\n\t"
    "v_cvt_pk_bf16_f32 v64, v40, v41\n\t"
    "v_cvt_pk_bf16_f32 v65, v42, v43\n\t"
    "v_cvt_pk_bf16_f32 v66, v44, v45\n\t"
    "v_cvt_pk_bf16_f32 v67, v46, v47\n\t"
    "v_exp_f32 v49, v49\n\t"
    "v_exp_f32 v50, v50\n\t"
    "v_smfmac_f32_16x16x64_bf16 v[240:243], v[104:107], v[56:63], v109\n\t"
    "s_waitcnt lgkmcnt(1)\n\t"
    "ds_read_b64_tr_b16 a[100:101], v13 offset:0\n\t"
    "v_exp_f32 v51, v51\n\t"
    "v_exp_f32 v52, v52\n\t"
    "v_exp_f32 v53, v53\n\t"
    "v_exp_f32 v54, v54\n\t"
    "v_exp_f32 v55, v55\n\t"
    "v_cvt_pk_bf16_f32 v68, v48, v49\n\t"
    "v_cvt_pk_bf16_f32 v69, v50, v51\n\t"
    "v_cvt_pk_bf16_f32 v70, v52, v53\n\t"
    "v_cvt_pk_bf16_f32 v71, v54, v55\n\t"
    "v_mfma_f32_32x32x16_bf16 v[40:55], a[252:255], a[236:239], 0\n\t"
    "ds_read_b64_tr_b16 a[98:99], v12 offset:256\n\t"
    "v_exp_f32 v24, v24\n\t"
    "v_exp_f32 v25, v25\n\t"
    "v_exp_f32 v26, v26\n\t"
    "v_exp_f32 v27, v27\n\t"
    "v_exp_f32 v28, v28\n\t"
    "v_exp_f32 v29, v29\n\t"
    "v_smfmac_f32_32x32x32_bf16 v[128:143], v[92:95], v[64:71], v108\n\t"
    "buffer_load_dwordx4 %[tv], %[sw2], s62 offen offset:3072 lds\n\t"
    "v_exp_f32 v30, v30\n\t"
    "v_exp_f32 v31, v31\n\t"
    "v_exp_f32 v32, v32\n\t"
    "v_cvt_pk_bf16_f32 v56, v24, v25\n\t"
    "v_cvt_pk_bf16_f32 v57, v26, v27\n\t"
    "v_cvt_pk_bf16_f32 v58, v28, v29\n\t"
    "v_cvt_pk_bf16_f32 v59, v30, v31\n\t"
    "v_exp_f32 v33, v33\n\t"
    "v_exp_f32 v34, v34\n\t"
    "v_smfmac_f32_16x16x64_bf16 v[244:247], v[104:107], v[64:71], v109\n\t"
    "s_add_u32 m0, s91, 0x2000\n\t"
    "ds_read_b64_tr_b16 a[102:103], v13 offset:256\n\t"
    "v_exp_f32 v35, v35\n\t"
    "v_exp_f32 v36, v36\n\t"
    "v_exp_f32 v37, v37\n\t"
    "v_exp_f32 v38, v38\n\t"
    "v_exp_f32 v39, v39\n\t"
    "v_cvt_pk_bf16_f32 v60, v32, v33\n\t"
    "v_cvt_pk_bf16_f32 v61, v34, v35\n\t"
    "v_cvt_pk_bf16_f32 v62, v36, v37\n\t"
    "v_cvt_pk_bf16_f32 v63, v38, v39\n\t"
    "v_mfma_f32_32x32x16_bf16 v[24:39], v[72:75], a[224:227], 0\n\t"
    "s_add_u32 s62, s89, 0x68000\n\t"
    "ds_read_b64_tr_b16 a[104:105], v12 offset:512\n\t"
    "v_exp_f32 v40, v40\n\t"
    "v_exp_f32 v41, v41\n\t"
    "v_exp_f32 v42, v42\n\t"
    "v_exp_f32 v43, v43\n\t"
    "v_exp_f32 v44, v44\n\t"
    "v_exp_f32 v45, v45\n\t"
    "v_smfmac_f32_32x32x32_bf16 v[144:159], v[92:95], v[56:63], v108\n\t"
    "buffer_load_dwordx4 %[tv], %[sw2], s62 offen offset:0 lds\n\t"
    "v_exp_f32 v46, v46\n\t"
    "v_exp_f32 v47, v47\n\t"
    "v_exp_f32 v48, v48\n\t"
    "v_cvt_pk_bf16_f32 v64, v40, v41\n\t"
    "v_cvt_pk_bf16_f32 v65, v42, v43\n\t"
    "v_cvt_pk_bf16_f32 v66, v44, v45\n\t"
    "v_cvt_pk_bf16_f32 v67, v46, v47\n\t"
    "v_exp_f32 v49, v49\n\t"
    "v_exp_f32 v50, v50\n\t"
    "v_smfmac_f32_16x16x64_bf16 v[248:251], v[104:107], v[56:63], v109\n\t"
    "ds_read_b64_tr_b16 a[108:109], v13 offset:512\n\t"
    "v_exp_f32 v51, v51\n\t"
    "v_exp_f32 v52, v52\n\t"
    "v_exp_f32 v53, v53\n\t"
    "v_exp_f32 v54, v54\n\t"
    "v_exp_f32 v55, v55\n\t"
    "v_cvt_pk_bf16_f32 v68, v48, v49\n\t"
    "v_cvt_pk_bf16_f32 v69, v50, v51\n\t"
    "v_cvt_pk_bf16_f32 v70, v52, v53\n\t"
    "v_cvt_pk_bf16_f32 v71, v54, v55\n\t"
    "v_mfma_f32_32x32x16_bf16 v[40:55], v[72:75], a[228:231], 0\n\t"
    "buffer_load_dwordx4 %[tv], %[sw2], s62 offen offset:1024 lds\n\t"
    "v_exp_f32 v24, v24\n\t"
    "v_exp_f32 v25, v25\n\t"
    "v_exp_f32 v26, v26\n\t"
    "v_exp_f32 v27, v27\n\t"
    "v_exp_f32 v28, v28\n\t"
    "v_exp_f32 v29, v29\n\t"
    "v_smfmac_f32_32x32x32_bf16 v[160:175], v[92:95], v[64:71], v108\n\t"
    "buffer_load_dwordx4 v[92:95], %[kv], %[sws], s61 offen offset:3072 sc1\n\t"
    "v_exp_f32 v30, v30\n\t"
    "v_exp_f32 v31, v31\n\t"
    "v_exp_f32 v32, v32\n\t"
    "v_cvt_pk_bf16_f32 v56, v24, v25\n\t"
    "v_cvt_pk_bf16_f32 v57, v26, v27\n\t"
    "v_cvt_pk_bf16_f32 v58, v28, v29\n\t"
    "v_cvt_pk_bf16_f32 v59, v30, v31\n\t"
    "v_exp_f32 v33, v33\n\t"
    "v_exp_f32 v34, v34\n\t"
    "v_smfmac_f32_16x16x64_bf16 v[252:255], v[104:107], v[64:71], v109\n\t"
    "s_waitcnt vmcnt(22)\n\t"
    "ds_read_b64_tr_b16 a[106:107], v12 offset:768\n\t"
    "v_exp_f32 v35, v35\n\t"
    "v_exp_f32 v36, v36\n\t"
    "v_exp_f32 v37, v37\n\t"
    "v_exp_f32 v38, v38\n\t"
    "v_exp_f32 v39, v39\n\t"
    "v_cvt_pk_bf16_f32 v60, v32, v33\n\t"
    "v_cvt_pk_bf16_f32 v61, v34, v35\n\t"
    "v_cvt_pk_bf16_f32 v62, v36, v37\n\t"
    "v_cvt_pk_bf16_f32 v63, v38, v39\n\t"
    "v_mfma_f32_32x32x16_bf16 v[24:39], v[72:75], a[232:235], 0\n\t"
    "buffer_load_dwordx4 %[tv], %[sw2], s62 offen offset:2048 lds\n\t"
    "v_exp_f32 v40, v40\n\t"
    "v_exp_f32 v41, v41\n\t"
    "v_exp_f32 v42, v42\n\t"
    "v_exp_f32 v43, v43\n\t"
    "v_exp_f32 v44, v44\n\t"
    "v_exp_f32 v45, v45\n\t"
    "v_smfmac_f32_32x32x32_bf16 v[112:127], v[96:99], v[56:63], v108\n\t"
    "buffer_load_dwordx4 a[252:255], %[kv], %[sws], s60 offen offset:3072 sc1\n\t"
    "v_exp_f32 v46, v46\n\t"
    "v_exp_f32 v47, v47\n\t"
    "v_exp_f32 v48, v48\n\t"
    "v_cvt_pk_bf16_f32 v64, v40, v41\n\t"
    "v_cvt_pk_bf16_f32 v65, v42, v43\n\t"
    "v_cvt_pk_bf16_f32 v66, v44, v45\n\t"
    "v_cvt_pk_bf16_f32 v67, v46, v47\n\t"
    "v_exp_f32 v49, v49\n\t"
    "v_exp_f32 v50, v50\n\t"
    "v_smfmac_f32_16x16x64_bf16 v[240:243], v[104:107], v[56:63], v109\n\t"
    "ds_read_b64_tr_b16 a[110:111], v13 offset:768\n\t"
    "v_exp_f32 v51, v51\n\t"
    "v_exp_f32 v52, v52\n\t"
    "v_exp_f32 v53, v53\n\t"
    "v_exp_f32 v54, v54\n\t"
    "v_exp_f32 v55, v55\n\t"
    "v_cvt_pk_bf16_f32 v68, v48, v49\n\t"
    "v_cvt_pk_bf16_f32 v69, v50, v51\n\t"
    "v_cvt_pk_bf16_f32 v70, v52, v53\n\t"
    "v_cvt_pk_bf16_f32 v71, v54, v55\n\t"
    "v_mfma_f32_32x32x16_bf16 v[40:55], v[72:75], a[236:239], 0\n\t"
    "s_mov_b32 s61, 0x207000\n\t"
    "ds_read_b64_tr_b16 a[112:113], v12 offset:4096\n\t"
    "v_exp_f32 v24, v24\n\t"
    "v_exp_f32 v25, v25\n\t"
    "v_exp_f32 v26, v26\n\t"
    "v_exp_f32 v27, v27\n\t"
    "v_exp_f32 v28, v28\n\t"
    "v_exp_f32 v29, v29\n\t"
    "v_smfmac_f32_32x32x32_bf16 v[128:143], v[96:99], v[64:71], v108\n\t"
    "buffer_load_dwordx4 %[tv], %[sw2], s62 offen offset:3072 lds\n\t"
    "v_exp_f32 v30, v30\n\t"
    "v_exp_f32 v31, v31\n\t"
    "v_exp_f32 v32, v32\n\t"
    "v_cvt_pk_bf16_f32 v56, v24, v25\n\t"
    "v_cvt_pk_bf16_f32 v57, v26, v27\n\t"
    "v_cvt_pk_bf16_f32 v58, v28, v29\n\t"
    "v_cvt_pk_bf16_f32 v59, v30, v31\n\t"
    "v_exp_f32 v33, v33\n\t"
    "v_exp_f32 v34, v34\n\t"
    "v_smfmac_f32_16x16x64_bf16 v[244:247], v[104:107], v[64:71], v109\n\t"
    "s_mov_b32 s60, 0x7000\n\t"
    "ds_read_b64_tr_b16 a[116:117], v13 offset:4096\n\t"
    "v_exp_f32 v35, v35\n\t"
    "v_exp_f32 v36, v36\n\t"
    "v_exp_f32 v37, v37\n\t"
    "v_exp_f32 v38, v38\n\t"
    "v_exp_f32 v39, v39\n\t"
    "v_cvt_pk_bf16_f32 v60, v32, v33\n\t"
    "v_cvt_pk_bf16_f32 v61, v34, v35\n\t"
    "v_cvt_pk_bf16_f32 v62, v36, v37\n\t"
    "v_cvt_pk_bf16_f32 v63, v38, v39\n\t"
    "v_mfma_f32_32x32x16_bf16 v[24:39], v[76:79], a[224:227], 0\n\t"
    "s_waitcnt lgkmcnt(2)\n\t"
    "ds_read_b64_tr_b16 a[114:115], v12 offset:4352\n\t"
    "v_exp_f32 v40, v40\n\t"
    "v_exp_f32 v41, v41\n\t"
    "v_exp_f32 v42, v42\n\t"
    "v_exp_f32 v43, v43\n\t"
    "v_exp_f32 v44, v44\n\t"
    "v_exp_f32 v45, v45\n\t"
    "v_smfmac_f32_32x32x32_bf16 v[144:159], v[96:99], v[56:63], v108\n\t"
    "s_add_u32 m0, s90, 0x5400\n\t"
    "s_mov_b32 s62, 0x54000\n\t"
    "v_exp_f32 v46, v46\n\t"
    "v_exp_f32 v47, v47\n\t"
    "v_exp_f32 v48, v48\n\t"
    "v_cvt_pk_bf16_f32 v64, v40, v41\n\t"
    "v_cvt_pk_bf16_f32 v65, v42, v43\n\t"
    "v_cvt_pk_bf16_f32 v66, v44, v45\n\t"
    "v_cvt_pk_bf16_f32 v67, v46, v47\n\t"
    "v_exp_f32 v49, v49\n\t"
    "v_exp_f32 v50, v50\n\t"
    "v_smfmac_f32_16x16x64_bf16 v[248:251], v[104:107], v[56:63], v109\n\t"
    "ds_read_b64_tr_b16 a[118:119], v13 offset:4352\n\t"
    "v_exp_f32 v51, v51\n\t"
    "v_exp_f32 v52, v52\n\t"
    "v_exp_f32 v53, v53\n\t"
    "v_exp_f32 v54, v54\n\t"
    "v_exp_f32 v55, v55\n\t"
    "v_cvt_pk_bf16_f32 v68, v48, v49\n\t"
    "v_cvt_pk_bf16_f32 v69, v50, v51\n\t"
    "v_cvt_pk_bf16_f32 v70, v52, v53\n\t"
    "v_cvt_pk_bf16_f32 v71, v54, v55\n\t"
    "v_mfma_f32_32x32x16_bf16 v[40:55], v[76:79], a[228:231], 0\n\t"
    "buffer_load_dwordx4 v19, %[sw1], s62 offen offset:3072 lds\n\t"
    "v_exp_f32 v24, v24\n\t"
    "v_exp_f32 v25, v25\n\t"
    "v_exp_f32 v26, v26\n\t"
    "v_exp_f32 v27, v27\n\t"
    "v_exp_f32 v28, v28\n\t"
    "v_exp_f32 v29, v29\n\t"
    "v_smfmac_f32_32x32x32_bf16 v[160:175], v[96:99], v[64:71], v108\n\t"
    "buffer_load_dwordx4 v[96:99], %[kv], %[sws], s61 offen offset:0 sc1\n\t"
    "v_exp_f32 v30, v30\n\t"
    "v_exp_f32 v31, v31\n\t"
    "v_exp_f32 v32, v32\n\t"
    "v_cvt_pk_bf16_f32 v56, v24, v25\n\t"
    "v_cvt_pk_bf16_f32 v57, v26, v27\n\t"
    "v_cvt_pk_bf16_f32 v58, v28, v29\n\t"
    "v_cvt_pk_bf16_f32 v59, v30, v31\n\t"
    "v_exp_f32 v33, v33\n\t"
    "v_exp_f32 v34, v34\n\t"
    "v_smfmac_f32_16x16x64_bf16 v[252:255], v[104:107], v[64:71], v109\n\t"
    "s_waitcnt vmcnt(22)\n\t"
    "ds_read_b64_tr_b16 a[120:121], v12 offset:4608\n\t"
    "v_exp_f32 v35, v35\n\t"
    "v_exp_f32 v36, v36\n\t"
    "v_exp_f32 v37, v37\n\t"
    "v_exp_f32 v38, v38\n\t"
    "v_exp_f32 v39, v39\n\t"
    "v_cvt_pk_bf16_f32 v60, v32, v33\n\t"
    "v_cvt_pk_bf16_f32 v61, v34, v35\n\t"
    "v_cvt_pk_bf16_f32 v62, v36, v37\n\t"
    "v_cvt_pk_bf16_f32 v63, v38, v39\n\t"
    "v_mfma_f32_32x32x16_bf16 v[24:39], v[76:79], a[232:235], 0\n\t"
    "s_add_u32 m0, s90, 0x6400\n\t"
    "ds_read_b64_tr_b16 a[124:125], v13 offset:4608\n\t"
    "v_exp_f32 v40, v40\n\t"
    "v_exp_f32 v41, v41\n\t"
    "v_exp_f32 v42, v42\n\t"
    "v_exp_f32 v43, v43\n\t"
    "v_exp_f32 v44, v44\n\t"
    "v_exp_f32 v45, v45\n\t"
    "v_smfmac_f32_32x32x32_bf16 v[112:127], v[100:103], v[56:63], v108\n\t"
    "buffer_load_dwordx4 v[72:75], %[kv], %[sws], s60 offen offset:0 sc1\n\t"
    "v_exp_f32 v46, v46\n\t"
    "v_exp_f32 v47, v47\n\t"
    "v_exp_f32 v48, v48\n\t"
    "v_cvt_pk_bf16_f32 v64, v40, v41\n\t"
    "v_cvt_pk_bf16_f32 v65, v42, v43\n\t"
    "v_cvt_pk_bf16_f32 v66, v44, v45\n\t"
    "v_cvt_pk_bf16_f32 v67, v46, v47\n\t"
    "v_exp_f32 v49, v49\n\t"
    "v_exp_f32 v50, v50\n\t"
    "v_smfmac_f32_16x16x64_bf16 v[240:243], v[104:107], v[56:63], v109\n\t"
    "s_mov_b32 s62, 0x54000\n\t"
    "ds_read_b64_tr_b16 a[122:123], v12 offset:4864\n\t"
    "v_exp_f32 v51, v51\n\t"
    "v_exp_f32 v52, v52\n\t"
    "v_exp_f32 v53, v53\n\t"
    "v_exp_f32 v54, v54\n\t"
    "v_exp_f32 v55, v55\n\t"
    "v_cvt_pk_bf16_f32 v68, v48, v49\n\t"
    "v_cvt_pk_bf16_f32 v69, v50, v51\n\t"
    "v_cvt_pk_bf16_f32 v70, v52, v53\n\t"
    "v_cvt_pk_bf16_f32 v71, v54, v55\n\t"
    "v_mfma_f32_32x32x16_bf16 v[40:55], v[76:79], a[236:239], 0\n\t"
    "ds_read_b64_tr_b16 a[126:127], v13 offset:4864\n\t"
    "v_exp_f32 v24, v24\n\t"
    "v_exp_f32 v25, v25\n\t"
    "v_exp_f32 v26, v26\n\t"
    "v_exp_f32 v27, v27\n\t"
    "v_exp_f32 v28, v28\n\t"
    "v_exp_f32 v29, v29\n\t"
    "v_smfmac_f32_32x32x32_bf16 v[128:143], v[100:103], v[64:71], v108\n\t"
    "buffer_load_dwordx4 v20, %[sw1], s62 offen offset:0 lds\n\t"
    "v_exp_f32 v30, v30\n\t"
    "v_exp_f32 v31, v31\n\t"
    "v_exp_f32 v32, v32\n\t"
    "v_cvt_pk_bf16_f32 v56, v24, v25\n\t"
    "v_cvt_pk_bf16_f32 v57, v26, v27\n\t"
    "v_cvt_pk_bf16_f32 v58, v28, v29\n\t"
    "v_cvt_pk_bf16_f32 v59, v30, v31\n\t"
    "v_exp_f32 v33, v33\n\t"
    "v_exp_f32 v34, v34\n\t"
    "v_smfmac_f32_16x16x64_bf16 v[244:247], v[104:107], v[64:71], v109\n\t"
    "ds_read_b64_tr_b16 a[128:129], v12 offset:8192\n\t"
    "v_exp_f32 v35, v35\n\t"
    "v_exp_f32 v36, v36\n\t"
    "v_exp_f32 v37, v37\n\t"
    "v_exp_f32 v38, v38\n\t"
    "v_exp_f32 v39, v39\n\t"
    "v_cvt_pk_bf16_f32 v60, v32, v33\n\t"
    "v_cvt_pk_bf16_f32 v61, v34, v35\n\t"
    "v_cvt_pk_bf16_f32 v62, v36, v37\n\t"
    "v_cvt_pk_bf16_f32 v63, v38, v39\n\t"
    "v_mfma_f32_32x32x16_bf16 v[24:39], a[240:243], a[224:227], 0\n\t"
    "ds_read_b64_tr_b16 a[132:133], v13 offset:8192\n\t"
    "v_exp_f32 v40, v40\n\t"
    "v_exp_f32 v41, v41\n\t"
    "v_exp_f32 v42, v42\n\t"
    "v_exp_f32 v43, v43\n\t"
    "v_exp_f32 v44, v44\n\t"
    "v_exp_f32 v45, v45\n\t"
    "v_smfmac_f32_32x32x32_bf16 v[144:159], v[100:103], v[56:63], v108\n\t"
    "buffer_load_dwordx4 v21, %[sw1], s62 offen offset:1024 lds\n\t"
    "v_exp_f32 v46, v46\n\t"
    "v_exp_f32 v47, v47\n\t"
    "v_exp_f32 v48, v48\n\t"
    "v_cvt_pk_bf16_f32 v64, v40, v41\n\t"
    "v_cvt_pk_bf16_f32 v65, v42, v43\n\t"
    "v_cvt_pk_bf16_f32 v66, v44, v45\n\t"
    "v_cvt_pk_bf16_f32 v67, v46, v47\n\t"
    "v_exp_f32 v49, v49\n\t"
    "v_exp_f32 v50, v50\n\t"
    "v_smfmac_f32_16x16x64_bf16 v[248:251], v[104:107], v[56:63], v109\n\t"
    "ds_read_b64_tr_b16 a[130:131], v12 offset:8448\n\t"
    "v_exp_f32 v51, v51\n\t"
    "v_exp_f32 v52, v52\n\t"
    "v_exp_f32 v53, v53\n\t"
    "v_exp_f32 v54, v54\n\t"
    "v_exp_f32 v55, v55\n\t"
    "v_cvt_pk_bf16_f32 v68, v48, v49\n\t"
    "v_cvt_pk_bf16_f32 v69, v50, v51\n\t"
    "v_cvt_pk_bf16_f32 v70, v52, v53\n\t"
    "v_cvt_pk_bf16_f32 v71, v54, v55\n\t"
    "v_mfma_f32_32x32x16_bf16 v[40:55], a[240:243], a[228:231], 0\n\t"
    "buffer_load_dwordx4 v22, %[sw1], s62 offen offset:2048 lds\n\t"
    "v_exp_f32 v24, v24\n\t"
    "v_exp_f32 v25, v25\n\t"
    "v_exp_f32 v26, v26\n\t"
    "v_exp_f32 v27, v27\n\t"
    "v_exp_f32 v28, v28\n\t"
    "v_exp_f32 v29, v29\n\t"
    "v_smfmac_f32_32x32x32_bf16 v[160:175], v[100:103], v[64:71], v108\n\t"
    "buffer_load_dwordx4 v[100:103], %[kv], %[sws], s61 offen offset:1024 sc1\n\t"
    "v_exp_f32 v30, v30\n\t"
    "v_exp_f32 v31, v31\n\t"
    "v_exp_f32 v32, v32\n\t"
    "v_cvt_pk_bf16_f32 v56, v24, v25\n\t"
    "v_cvt_pk_bf16_f32 v57, v26, v27\n\t"
    "v_cvt_pk_bf16_f32 v58, v28, v29\n\t"
    "v_cvt_pk_bf16_f32 v59, v30, v31\n\t"
    "v_exp_f32 v33, v33\n\t"
    "v_exp_f32 v34, v34\n\t"
    "v_smfmac_f32_16x16x64_bf16 v[252:255], v[104:107], v[64:71], v109\n\t"
    "s_waitcnt vmcnt(20)\n\t"
    "ds_read_b64_tr_b16 a[134:135], v13 offset:8448\n\t"
    "v_exp_f32 v35, v35\n\t"
    "v_exp_f32 v36, v36\n\t"
    "v_exp_f32 v37, v37\n\t"
    "v_exp_f32 v38, v38\n\t"
    "v_exp_f32 v39, v39\n\t"
    "v_cvt_pk_bf16_f32 v60, v32, v33\n\t"
    "v_cvt_pk_bf16_f32 v61, v34, v35\n\t"
    "v_cvt_pk_bf16_f32 v62, v36, v37\n\t"
    "v_cvt_pk_bf16_f32 v63, v38, v39\n\t"
    "v_mfma_f32_32x32x16_bf16 v[24:39], a[240:243], a[232:235], 0\n\t"
    "s_waitcnt lgkmcnt(4)\n\t"
    "ds_read_b64_tr_b16 a[136:137], v12 offset:8704\n\t"
    "v_exp_f32 v40, v40\n\t"
    "v_exp_f32 v41, v41\n\t"
    "v_exp_f32 v42, v42\n\t"
    "v_exp_f32 v43, v43\n\t"
    "v_exp_f32 v44, v44\n\t"
    "v_exp_f32 v45, v45\n\t"
    "v_smfmac_f32_32x32x32_bf16 v[112:127], v[80:83], v[56:63], v108\n\t"
    "buffer_load_dwordx4 v[76:79], %[kv], %[sws], s60 offen offset:1024 sc1\n\t"
    "v_exp_f32 v46, v46\n\t"
    "v_exp_f32 v47, v47\n\t"
    "v_exp_f32 v48, v48\n\t"
    "v_cvt_pk_bf16_f32 v64, v40, v41\n\t"
    "v_cvt_pk_bf16_f32 v65, v42, v43\n\t"
    "v_cvt_pk_bf16_f32 v66, v44, v45\n\t"
    "v_cvt_pk_bf16_f32 v67, v46, v47\n\t"
    "v_exp_f32 v49, v49\n\t"
    "v_exp_f32 v50, v50\n\t"
    "v_smfmac_f32_16x16x64_bf16 v[240:243], v[104:107], v[56:63], v109\n\t"
    "s_add_u32 m0, s90, 0x4800\n\t"
    "ds_read_b64_tr_b16 a[140:141], v13 offset:8704\n\t"
    "v_exp_f32 v51, v51\n\t"
    "v_exp_f32 v52, v52\n\t"
    "v_exp_f32 v53, v53\n\t"
    "v_exp_f32 v54, v54\n\t"
    "v_exp_f32 v55, v55\n\t"
    "v_cvt_pk_bf16_f32 v68, v48, v49\n\t"
    "v_cvt_pk_bf16_f32 v69, v50, v51\n\t"
    "v_cvt_pk_bf16_f32 v70, v52, v53\n\t"
    "v_cvt_pk_bf16_f32 v71, v54, v55\n\t"
    "v_mfma_f32_32x32x16_bf16 v[40:55], a[240:243], a[236:239], 0\n\t"
    "s_mov_b32 s62, 0x38000\n\t"
    "ds_read_b64_tr_b16 a[138:139], v12 offset:8960\n\t"
    "v_exp_f32 v24, v24\n\t"
    "v_exp_f32 v25, v25\n\t"
    "v_exp_f32 v26, v26\n\t"
    "v_exp_f32 v27, v27\n\t"
    "v_exp_f32 v28, v28\n\t"
    "v_exp_f32 v29, v29\n\t"
    "v_smfmac_f32_32x32x32_bf16 v[128:143], v[80:83], v[64:71], v108\n\t"
    "buffer_load_dwordx4 v22, %[sw1], s62 offen offset:2048 lds\n\t"
    "v_exp_f32 v30, v30\n\t"
    "v_exp_f32 v31, v31\n\t"
    "v_exp_f32 v32, v32\n\t"
    "v_cvt_pk_bf16_f32 v56, v24, v25\n\t"
    "v_cvt_pk_bf16_f32 v57, v26, v27\n\t"
    "v_cvt_pk_bf16_f32 v58, v28, v29\n\t"
    "v_cvt_pk_bf16_f32 v59, v30, v31\n\t"
    "v_exp_f32 v33, v33\n\t"
    "v_exp_f32 v34, v34\n\t"
    "v_smfmac_f32_16x16x64_bf16 v[244:247], v[104:107], v[64:71], v109\n\t"
    "s_add_u32 m0, s90, 0x5400\n\t"
    "ds_read_b64_tr_b16 a[142:143], v13 offset:8960\n\t"
    "v_exp_f32 v35, v35\n\t"
    "v_exp_f32 v36, v36\n\t"
    "v_exp_f32 v37, v37\n\t"
    "v_exp_f32 v38, v38\n\t"
    "v_exp_f32 v39, v39\n\t"
    "v_cvt_pk_bf16_f32 v60, v32, v33\n\t"
    "v_cvt_pk_bf16_f32 v61, v34, v35\n\t"
    "v_cvt_pk_bf16_f32 v62, v36, v37\n\t"
    "v_cvt_pk_bf16_f32 v63, v38, v39\n\t"
    "v_mfma_f32_32x32x16_bf16 v[24:39], a[244:247], a[224:227], 0\n\t"
    "s_mov_b32 s62, 0x54000\n\t"
    "ds_read_b64_tr_b16 a[144:145], v12 offset:12288\n\t"
    "v_exp_f32 v40, v40\n\t"
    "v_exp_f32 v41, v41\n\t"
    "v_exp_f32 v42, v42\n\t"
    "v_exp_f32 v43, v43\n\t"
    "v_exp_f32 v44, v44\n\t"
    "v_exp_f32 v45, v45\n\t"
    "v_smfmac_f32_32x32x32_bf16 v[144:159], v[80:83], v[56:63], v108\n\t"
    "buffer_load_dwordx4 v16, %[sw1], s62 offen offset:0 lds\n\t"
    "v_exp_f32 v46, v46\n\t"
    "v_exp_f32 v47, v47\n\t"
    "v_exp_f32 v48, v48\n\t"
    "v_cvt_pk_bf16_f32 v64, v40, v41\n\t"
    "v_cvt_pk_bf16_f32 v65, v42, v43\n\t"
    "v_cvt_pk_bf16_f32 v66, v44, v45\n\t"
    "v_cvt_pk_bf16_f32 v67, v46, v47\n\t"
    "v_exp_f32 v49, v49\n\t"
    "v_exp_f32 v50, v50\n\t"
    "v_smfmac_f32_16x16x64_bf16 v[248:251], v[104:107], v[56:63], v109\n\t"
    "ds_read_b64_tr_b16 a[148:149], v13 offset:12288\n\t"
    "v_exp_f32 v51, v51\n\t"
    "v_exp_f32 v52, v52\n\t"
    "v_exp_f32 v53, v53\n\t"
    "v_exp_f32 v54, v54\n\t"
    "v_exp_f32 v55, v55\n\t"
    "v_cvt_pk_bf16_f32 v68, v48, v49\n\t"
    "v_cvt_pk_bf16_f32 v69, v50, v51\n\t"
    "v_cvt_pk_bf16_f32 v70, v52, v53\n\t"
    "v_cvt_pk_bf16_f32 v71, v54, v55\n\t"
    "v_mfma_f32_32x32x16_bf16 v[40:55], a[244:247], a[228:231], 0\n\t"
    "buffer_load_dwordx4 v17, %[sw1], s62 offen offset:1024 lds\n\t"
    "v_exp_f32 v24, v24\n\t"
    "v_exp_f32 v25, v25\n\t"
    "v_exp_f32 v26, v26\n\t"
    "v_exp_f32 v27, v27\n\t"
    "v_exp_f32 v28, v28\n\t"
    "v_exp_f32 v29, v29\n\t"
    "v_smfmac_f32_32x32x32_bf16 v[160:175], v[80:83], v[64:71], v108\n\t"
    "buffer_load_dwordx4 v[80:83], %[kv], %[sws], s61 offen offset:2048 sc1\n\t"
    "v_exp_f32 v30, v30\n\t"
    "v_exp_f32 v31, v31\n\t"
    "v_exp_f32 v32, v32\n\t"
    "v_cvt_pk_bf16_f32 v56, v24, v25\n\t"
    "v_cvt_pk_bf16_f32 v57, v26, v27\n\t"
    "v_cvt_pk_bf16_f32 v58, v28, v29\n\t"
    "v_cvt_pk_bf16_f32 v59, v30, v31\n\t"
    "v_exp_f32 v33, v33\n\t"
    "v_exp_f32 v34, v34\n\t"
    "v_smfmac_f32_16x16x64_bf16 v[252:255], v[104:107], v[64:71], v109\n\t"
    "s_waitcnt vmcnt(19)\n\t"
    "ds_read_b64_tr_b16 a[146:147], v12 offset:12544\n\t"
    "v_exp_f32 v35, v35\n\t"
    "v_exp_f32 v36, v36\n\t"
    "v_exp_f32 v37, v37\n\t"
    "v_exp_f32 v38, v38\n\t"
    "v_exp_f32 v39, v39\n\t"
    "v_cvt_pk_bf16_f32 v60, v32, v33\n\t"
    "v_cvt_pk_bf16_f32 v61, v34, v35\n\t"
    "v_cvt_pk_bf16_f32 v62, v36, v37\n\t"
    "v_cvt_pk_bf16_f32 v63, v38, v39\n\t"
    "v_mfma_f32_32x32x16_bf16 v[24:39], a[244:247], a[232:235], 0\n\t"
    "buffer_load_dwordx4 v18, %[sw1], s62 offen offset:2048 lds\n\t"
    "v_exp_f32 v40, v40\n\t"
    "v_exp_f32 v41, v41\n\t"
    "v_exp_f32 v42, v42\n\t"
    "v_exp_f32 v43, v43\n\t"
    "v_exp_f32 v44, v44\n\t"
    "v_exp_f32 v45, v45\n\t"
    "v_smfmac_f32_32x32x32_bf16 v[112:127], v[84:87], v[56:63], v108\n\t"
    "buffer_load_dwordx4 a[240:243], %[kv], %[sws], s60 offen offset:2048 sc1\n\t"
    "v_exp_f32 v46, v46\n\t"
    "v_exp_f32 v47, v47\n\t"
    "v_exp_f32 v48, v48\n\t"
    "v_cvt_pk_bf16_f32 v64, v40, v41\n\t"
    "v_cvt_pk_bf16_f32 v65, v42, v43\n\t"
    "v_cvt_pk_bf16_f32 v66, v44, v45\n\t"
    "v_cvt_pk_bf16_f32 v67, v46, v47\n\t"
    "v_exp_f32 v49, v49\n\t"
    "v_exp_f32 v50, v50\n\t"
    "v_smfmac_f32_16x16x64_bf16 v[240:243], v[104:107], v[56:63], v109\n\t"
    "s_waitcnt lgkmcnt(3)\n\t"
    "ds_read_b64_tr_b16 a[150:151], v13 offset:12544\n\t"
    "v_exp_f32 v51, v51\n\t"
    "v_exp_f32 v52, v52\n\t"
    "v_exp_f32 v53, v53\n\t"
    "v_exp_f32 v54, v54\n\t"
    "v_exp_f32 v55, v55\n\t"
    "v_cvt_pk_bf16_f32 v68, v48, v49\n\t"
    "v_cvt_pk_bf16_f32 v69, v50, v51\n\t"
    "v_cvt_pk_bf16_f32 v70, v52, v53\n\t"
    "v_cvt_pk_bf16_f32 v71, v54, v55\n\t"
    "v_mfma_f32_32x32x16_bf16 v[40:55], a[244:247], a[236:239], 0\n\t"
    "s_add_u32 m0, s90, 0x0\n\t"
    "ds_read_b64_tr_b16 a[152:153], v12 offset:12800\n\t"
    "v_exp_f32 v24, v24\n\t"
    "v_exp_f32 v25, v25\n\t"
    "v_exp_f32 v26, v26\n\t"
    "v_exp_f32 v27, v27\n\t"
    "v_exp_f32 v28, v28\n\t"
    "v_exp_f32 v29, v29\n\t"
    "v_smfmac_f32_32x32x32_bf16 v[128:143], v[84:87], v[64:71], v108\n\t"
    "s_mov_b32 s62, 0x0\n\t"
    "v_exp_f32 v30, v30\n\t"
    "v_exp_f32 v31, v31\n\t"
    "v_exp_f32 v32, v32\n\t"
    "v_cvt_pk_bf16_f32 v56, v24, v25\n\t"
    "v_cvt_pk_bf16_f32 v57, v26, v27\n\t"
    "v_cvt_pk_bf16_f32 v58, v28, v29\n\t"
    "v_cvt_pk_bf16_f32 v59, v30, v31\n\t"
    "v_exp_f32 v33, v33\n\t"
    "v_exp_f32 v34, v34\n\t"
    "v_smfmac_f32_16x16x64_bf16 v[244:247], v[104:107], v[64:71], v109\n\t"
    "ds_read_b64_tr_b16 a[156:157], v13 offset:12800\n\t"
    "v_exp_f32 v35, v35\n\t"
    "v_exp_f32 v36, v36\n\t"
    "v_exp_f32 v37, v37\n\t"
    "v_exp_f32 v38, v38\n\t"
    "v_exp_f32 v39, v39\n\t"
    "v_cvt_pk_bf16_f32 v60, v32, v33\n\t"
    "v_cvt_pk_bf16_f32 v61, v34, v35\n\t"
    "v_cvt_pk_bf16_f32 v62, v36, v37\n\t"
    "v_cvt_pk_bf16_f32 v63, v38, v39\n\t"
    "v_mfma_f32_32x32x16_bf16 v[24:39], a[248:251], a[224:227], 0\n\t"
    "ds_read_b64_tr_b16 a[154:155], v12 offset:13056\n\t"
    "v_exp_f32 v40, v40\n\t"
    "v_exp_f32 v41, v41\n\t"
    "v_exp_f32 v42, v42\n\t"
    "v_exp_f32 v43, v43\n\t"
    "v_exp_f32 v44, v44\n\t"
    "v_exp_f32 v45, v45\n\t"
    "v_smfmac_f32_32x32x32_bf16 v[144:159], v[84:87], v[56:63], v108\n\t"
    "buffer_load_dwordx4 v16, %[sw1], s62 offen offset:0 lds\n\t"
    "v_exp_f32 v46, v46\n\t"
    "v_exp_f32 v47, v47\n\t"
    "v_exp_f32 v48, v48\n\t"
    "v_cvt_pk_bf16_f32 v64, v40, v41\n\t"
    "v_cvt_pk_bf16_f32 v65, v42, v43\n\t"
    "v_cvt_pk_bf16_f32 v66, v44, v45\n\t"
    "v_cvt_pk_bf16_f32 v67, v46, v47\n\t"
    "v_exp_f32 v49, v49\n\t"
    "v_exp_f32 v50, v50\n\t"
    "v_smfmac_f32_16x16x64_bf16 v[248:251], v[104:107], v[56:63], v109\n\t"
    "ds_read_b64_tr_b16 a[158:159], v13 offset:13056\n\t"
    "v_exp_f32 v51, v51\n\t"
    "v_exp_f32 v52, v52\n\t"
    "v_exp_f32 v53, v53\n\t"
    "v_exp_f32 v54, v54\n\t"
    "v_exp_f32 v55, v55\n\t"
    "v_cvt_pk_bf16_f32 v68, v48, v49\n\t"
    "v_cvt_pk_bf16_f32 v69, v50, v51\n\t"
    "v_cvt_pk_bf16_f32 v70, v52, v53\n\t"
    "v_cvt_pk_bf16_f32 v71, v54, v55\n\t"
    "v_mfma_f32_32x32x16_bf16 v[40:55], a[248:251], a[228:231], 0\n\t"
    "buffer_load_dwordx4 v17, %[sw1], s62 offen offset:1024 lds\n\t"
    "v_exp_f32 v24, v24\n\t"
    "v_exp_f32 v25, v25\n\t"
    "v_exp_f32 v26, v26\n\t"
    "v_exp_f32 v27, v27\n\t"
    "v_exp_f32 v28, v28\n\t"
    "v_exp_f32 v29, v29\n\t"
    "v_smfmac_f32_32x32x32_bf16 v[160:175], v[84:87], v[64:71], v108\n\t"
    "buffer_load_dwordx4 v[84:87], %[kv], %[sws], s61 offen offset:3072 sc1\n\t"
    "v_exp_f32 v30, v30\n\t"
    "v_exp_f32 v31, v31\n\t"
    "v_exp_f32 v32, v32\n\t"
    "v_cvt_pk_bf16_f32 v56, v24, v25\n\t"
    "v_cvt_pk_bf16_f32 v57, v26, v27\n\t"
    "v_cvt_pk_bf16_f32 v58, v28, v29\n\t"
    "v_cvt_pk_bf16_f32 v59, v30, v31\n\t"
    "v_exp_f32 v33, v33\n\t"
    "v_exp_f32 v34, v34\n\t"
    "v_smfmac_f32_16x16x64_bf16 v[252:255], v[104:107], v[64:71], v109\n\t"
    "buffer_load_dword %[fl], %[cdv], s[92:95], 0 offen\n\t"
    "v_exp_f32 v35, v35\n\t"
    "v_exp_f32 v36, v36\n\t"
    "v_exp_f32 v37, v37\n\t"
    "v_exp_f32 v38, v38\n\t"
    "v_exp_f32 v39, v39\n\t"
    "v_cvt_pk_bf16_f32 v60, v32, v33\n\t"
    "v_cvt_pk_bf16_f32 v61, v34, v35\n\t"
    "v_cvt_pk_bf16_f32 v62, v36, v37\n\t"
    "v_cvt_pk_bf16_f32 v63, v38, v39\n\t"
    "v_mfma_f32_32x32x16_bf16 v[24:39], a[248:251], a[232:235], 0\n\t"
    "buffer_load_dwordx4 v18, %[sw1], s62 offen offset:2048 lds\n\t"
    "v_exp_f32 v40, v40\n\t"
    "v_exp_f32 v41, v41\n\t"
    "v_exp_f32 v42, v42\n\t"
    "v_exp_f32 v43, v43\n\t"
    "v_exp_f32 v44, v44\n\t"
    "v_exp_f32 v45, v45\n\t"
    "v_smfmac_f32_32x32x32_bf16 v[112:127], v[88:91], v[56:63], v108\n\t"
    "buffer_load_dwordx4 a[244:247], %[kv], %[sws], s60 offen offset:3072 sc1\n\t"
    "v_exp_f32 v46, v46\n\t"
    "v_exp_f32 v47, v47\n\t"
    "v_exp_f32 v48, v48\n\t"
    "v_cvt_pk_bf16_f32 v64, v40, v41\n\t"
    "v_cvt_pk_bf16_f32 v65, v42, v43\n\t"
    "v_cvt_pk_bf16_f32 v66, v44, v45\n\t"
    "v_cvt_pk_bf16_f32 v67, v46, v47\n\t"
    "v_exp_f32 v49, v49\n\t"
    "v_exp_f32 v50, v50\n\t"
    "v_smfmac_f32_16x16x64_bf16 v[240:243], v[104:107], v[56:63], v109\n\t"
    "s_waitcnt vmcnt(21)\n\t"
    "ds_read_b64_tr_b16 a[160:161], v12 offset:16384\n\t"
    "v_exp_f32 v51, v51\n\t"
    "v_exp_f32 v52, v52\n\t"
    "v_exp_f32 v53, v53\n\t"
    "v_exp_f32 v54, v54\n\t"
    "v_exp_f32 v55, v55\n\t"
    "v_cvt_pk_bf16_f32 v68, v48, v49\n\t"
    "v_cvt_pk_bf16_f32 v69, v50, v51\n\t"
    "v_cvt_pk_bf16_f32 v70, v52, v53\n\t"
    "v_cvt_pk_bf16_f32 v71, v54, v55\n\t"
    "v_mfma_f32_32x32x16_bf16 v[40:55], a[248:251], a[236:239], 0\n\t"
    "s_waitcnt lgkmcnt(1)\n\t"
    "ds_read_b64_tr_b16 a[164:165], v13 offset:16384\n\t"
    "v_exp_f32 v24, v24\n\t"
    "v_exp_f32 v25, v25\n\t"
    "v_exp_f32 v26, v26\n\t"
    "v_exp_f32 v27, v27\n\t"
    "v_exp_f32 v28, v28\n\t"
    "v_exp_f32 v29, v29\n\t"
    "v_smfmac_f32_32x32x32_bf16 v[128:143], v[88:91], v[64:71], v108\n\t"
    "buffer_load_dwordx4 v19, %[sw1], s62 offen offset:3072 lds\n\t"
    "v_exp_f32 v30, v30\n\t"
    "v_exp_f32 v31, v31\n\t"
    "v_exp_f32 v32, v32\n\t"
    "v_cvt_pk_bf16_f32 v56, v24, v25\n\t"
    "v_cvt_pk_bf16_f32 v57, v26, v27\n\t"
    "v_cvt_pk_bf16_f32 v58, v28, v29\n\t"
    "v_cvt_pk_bf16_f32 v59, v30, v31\n\t"
    "v_exp_f32 v33, v33\n\t"
    "v_exp_f32 v34, v34\n\t"
    "v_smfmac_f32_16x16x64_bf16 v[244:247], v[104:107], v[64:71], v109\n\t"
    "s_add_u32 m0, s90, 0x1000\n\t"
    "ds_read_b64_tr_b16 a[162:163], v12 offset:16640\n\t"
    "v_exp_f32 v35, v35\n\t"
    "v_exp_f32 v36, v36\n\t"
    "v_exp_f32 v37, v37\n\t"
    "v_exp_f32 v38, v38\n\t"
    "v_exp_f32 v39, v39\n\t"
    "v_cvt_pk_bf16_f32 v60, v32, v33\n\t"
    "v_cvt_pk_bf16_f32 v61, v34, v35\n\t"
    "v_cvt_pk_bf16_f32 v62, v36, v37\n\t"
    "v_cvt_pk_bf16_f32 v63, v38, v39\n\t"
    "v_mfma_f32_32x32x16_bf16 v[24:39], a[252:255], a[224:227], 0\n\t"
    "s_mov_b32 s62, 0x0\n\t"
    "ds_read_b64_tr_b16 a[166:167], v13 offset:16640\n\t"
    "v_exp_f32 v40, v40\n\t"
    "v_exp_f32 v41, v41\n\t"
    "v_exp_f32 v42, v42\n\t"
    "v_exp_f32 v43, v43\n\t"
    "v_exp_f32 v44, v44\n\t"
    "v_exp_f32 v45, v45\n\t"
    "v_smfmac_f32_32x32x32_bf16 v[144:159], v[88:91], v[56:63], v108\n\t"
    "buffer_load_dwordx4 v20, %[sw1], s62 offen offset:0 lds\n\t"
    "v_exp_f32 v46, v46\n\t"
    "v_exp_f32 v47, v47\n\t"
    "v_exp_f32 v48, v48\n\t"
    "v_cvt_pk_bf16_f32 v64, v40, v41\n\t"
    "v_cvt_pk_bf16_f32 v65, v42, v43\n\t"
    "v_cvt_pk_bf16_f32 v66, v44, v45\n\t"
    "v_cvt_pk_bf16_f32 v67, v46, v47\n\t"
    "v_exp_f32 v49, v49\n\t"
    "v_exp_f32 v50, v50\n\t"
    "v_smfmac_f32_16x16x64_bf16 v[248:251], v[104:107], v[56:63], v109\n\t"
    "ds_read_b64_tr_b16 a[168:169], v12 offset:16896\n\t"
    "v_exp_f32 v51, v51\n\t"
    "v_exp_f32 v52, v52\n\t"
    "v_exp_f32 v53, v53\n\t"
    "v_exp_f32 v54, v54\n\t"
    "v_exp_f32 v55, v55\n\t"
    "v_cvt_pk_bf16_f32 v68, v48, v49\n\t"
    "v_cvt_pk_bf16_f32 v69, v50, v51\n\t"
    "v_cvt_pk_bf16_f32 v70, v52, v53\n\t"
    "v_cvt_pk_bf16_f32 v71, v54, v55\n\t"
    "v_mfma_f32_32x32x16_bf16 v[40:55], a[252:255], a[228:231], 0\n\t"
    "buffer_load_dwordx4 v21, %[sw1], s62 offen offset:1024 lds\n\t"
    "v_exp_f32 v24, v24\n\t"
    "v_exp_f32 v25, v25\n\t"
    "v_exp_f32 v26, v26\n\t"
    "v_exp_f32 v27, v27\n\t"
    "v_exp_f32 v28, v28\n\t"
    "v_exp_f32 v29, v29\n\t"
    "v_smfmac_f32_32x32x32_bf16 v[160:175], v[88:91], v[64:71], v108\n\t"
    "buffer_load_dwordx4 v22, %[sw1], s62 offen offset:2048 lds\n\t"
    "v_exp_f32 v30, v30\n\t"
    "v_exp_f32 v31, v31\n\t"
    "v_exp_f32 v32, v32\n\t"
    "v_cvt_pk_bf16_f32 v56, v24, v25\n\t"
    "v_cvt_pk_bf16_f32 v57, v26, v27\n\t"
    "v_cvt_pk_bf16_f32 v58, v28, v29\n\t"
    "v_cvt_pk_bf16_f32 v59, v30, v31\n\t"
    "v_exp_f32 v33, v33\n\t"
    "v_exp_f32 v34, v34\n\t"
    "v_smfmac_f32_16x16x64_bf16 v[252:255], v[104:107], v[64:71], v109\n\t"
    "s_waitcnt vmcnt(21)\n\t"
    "ds_read_b64_tr_b16 a[172:173], v13 offset:16896\n\t"
    "v_exp_f32 v35, v35\n\t"
    "v_exp_f32 v36, v36\n\t"
    "v_exp_f32 v37, v37\n\t"
    "v_exp_f32 v38, v38\n\t"
    "v_exp_f32 v39, v39\n\t"
    "v_cvt_pk_bf16_f32 v60, v32, v33\n\t"
    "v_cvt_pk_bf16_f32 v61, v34, v35\n\t"
    "v_cvt_pk_bf16_f32 v62, v36, v37\n\t"
    "v_cvt_pk_bf16_f32 v63, v38, v39\n\t"
    "v_mfma_f32_32x32x16_bf16 v[24:39], a[252:255], a[232:235], 0\n\t"
    "s_add_u32 m0, s90, 0x1c00\n\t"
    "ds_read_b64_tr_b16 a[170:171], v12 offset:17152\n\t"
    "v_exp_f32 v40, v40\n\t"
    "v_exp_f32 v41, v41\n\t"
    "v_exp_f32 v42, v42\n\t"
    "v_exp_f32 v43, v43\n\t"
    "v_exp_f32 v44, v44\n\t"
    "v_exp_f32 v45, v45\n\t"
    "v_smfmac_f32_32x32x32_bf16 v[112:127], v[92:95], v[56:63], v108\n\t"
    "s_mov_b32 s62, 0x1c000\n\t"
    "v_exp_f32 v46, v46\n\t"
    "v_exp_f32 v47, v47\n\t"
    "v_exp_f32 v48, v48\n\t"
    "v_cvt_pk_bf16_f32 v64, v40, v41\n\t"
    "v_cvt_pk_bf16_f32 v65, v42, v43\n\t"
    "v_cvt_pk_bf16_f32 v66, v44, v45\n\t"
    "v_cvt_pk_bf16_f32 v67, v46, v47\n\t"
    "v_exp_f32 v49, v49\n\t"
    "v_exp_f32 v50, v50\n\t"
    "v_smfmac_f32_16x16x64_bf16 v[240:243], v[104:107], v[56:63], v109\n\t"
    "ds_read_b64_tr_b16 a[174:175], v13 offset:17152\n\t"
    "v_exp_f32 v51, v51\n\t"
    "v_exp_f32 v52, v52\n\t"
    "v_exp_f32 v53, v53\n\t"
    "v_exp_f32 v54, v54\n\t"
    "v_exp_f32 v55, v55\n\t"
    "v_cvt_pk_bf16_f32 v68, v48, v49\n\t"
    "v_cvt_pk_bf16_f32 v69, v50, v51\n\t"
    "v_cvt_pk_bf16_f32 v70, v52, v53\n\t"
    "v_cvt_pk_bf16_f32 v71, v54, v55\n\t"
    "v_mfma_f32_32x32x16_bf16 v[40:55], a[252:255], a[236:239], 0\n\t"
    "ds_read_b64_tr_b16 a[176:177], v14 offset:0\n\t"
    "v_exp_f32 v24, v24\n\t"
    "v_exp_f32 v25, v25\n\t"
    "v_exp_f32 v26, v26\n\t"
    "v_exp_f32 v27, v27\n\t"
    "v_exp_f32 v28, v28\n\t"
    "v_exp_f32 v29, v29\n\t"
    "v_smfmac_f32_32x32x32_bf16 v[128:143], v[92:95], v[64:71], v108\n\t"
    "buffer_load_dwordx4 v16, %[sw1], s62 offen offset:0 lds\n\t"
    "v_exp_f32 v30, v30\n\t"
    "v_exp_f32 v31, v31\n\t"
    "v_exp_f32 v32, v32\n\t"
    "v_cvt_pk_bf16_f32 v56, v24, v25\n\t"
    "v_cvt_pk_bf16_f32 v57, v26, v27\n\t"
    "v_cvt_pk_bf16_f32 v58, v28, v29\n\t"
    "v_cvt_pk_bf16_f32 v59, v30, v31\n\t"
    "v_exp_f32 v33, v33\n\t"
    "v_exp_f32 v34, v34\n\t"
    "v_smfmac_f32_16x16x64_bf16 v[244:247], v[104:107], v[64:71], v109\n\t"
    "ds_read_b64_tr_b16 a[180:181], v15 offset:0\n\t"
    "v_exp_f32 v35, v35\n\t"
    "v_exp_f32 v36, v36\n\t"
    "v_exp_f32 v37, v37\n\t"
    "v_exp_f32 v38, v38\n\t"
    "v_exp_f32 v39, v39\n\t"
    "v_cvt_pk_bf16_f32 v60, v32, v33\n\t"
    "v_cvt_pk_bf16_f32 v61, v34, v35\n\t"
    "v_cvt_pk_bf16_f32 v62, v36, v37\n\t"
    "v_cvt_pk_bf16_f32 v63, v38, v39\n\t"
    "v_mfma_f32_32x32x16_bf16 v[24:39], v[72:75], a[224:227], 0\n\t"
    "s_waitcnt lgkmcnt(2)\n\t"
    "ds_read_b64_tr_b16 a[178:179], v14 offset:256\n\t"
    "v_exp_f32 v40, v40\n\t"
    "v_exp_f32 v41, v41\n\t"
    "v_exp_f32 v42, v42\n\t"
    "v_exp_f32 v43, v43\n\t"
    "v_exp_f32 v44, v44\n\t"
    "v_exp_f32 v45, v45\n\t"
    "v_smfmac_f32_32x32x32_bf16 v[144:159], v[92:95], v[56:63], v108\n\t"
    "buffer_load_dwordx4 v17, %[sw1], s62 offen offset:1024 lds\n\t"
    "v_exp_f32 v46, v46\n\t"
    "v_exp_f32 v47, v47\n\t"
    "v_exp_f32 v48, v48\n\t"
    "v_cvt_pk_bf16_f32 v64, v40, v41\n\t"
    "v_cvt_pk_bf16_f32 v65, v42, v43\n\t"
    "v_cvt_pk_bf16_f32 v66, v44, v45\n\t"
    "v_cvt_pk_bf16_f32 v67, v46, v47\n\t"
    "v_exp_f32 v49, v49\n\t"
    "v_exp_f32 v50, v50\n\t"
    "v_smfmac_f32_16x16x64_bf16 v[248:251], v[104:107], v[56:63], v109\n\t"
    "ds_read_b64_tr_b16 a[182:183], v15 offset:256\n\t"
    "v_exp_f32 v51, v51\n\t"
    "v_exp_f32 v52, v52\n\t"
    "v_exp_f32 v53, v53\n\t"
    "v_exp_f32 v54, v54\n\t"
    "v_exp_f32 v55, v55\n\t"
    "v_cvt_pk_bf16_f32 v68, v48, v49\n\t"
    "v_cvt_pk_bf16_f32 v69, v50, v51\n\t"
    "v_cvt_pk_bf16_f32 v70, v52, v53\n\t"
    "v_cvt_pk_bf16_f32 v71, v54, v55\n\t"
    "v_mfma_f32_32x32x16_bf16 v[40:55], v[72:75], a[228:231], 0\n\t"
    "buffer_load_dwordx4 v18, %[sw1], s62 offen offset:2048 lds\n\t"
    "v_exp_f32 v24, v24\n\t"
    "v_exp_f32 v25, v25\n\t"
    "v_exp_f32 v26, v26\n\t"
    "v_exp_f32 v27, v27\n\t"
    "v_exp_f32 v28, v28\n\t"
    "v_exp_f32 v29, v29\n\t"
    "v_smfmac_f32_32x32x32_bf16 v[160:175], v[92:95], v[64:71], v108\n\t"
    "buffer_load_dwordx4 v19, %[sw1], s62 offen offset:3072 lds\n\t"
    "v_exp_f32 v30, v30\n\t"
    "v_exp_f32 v31, v31\n\t"
    "v_exp_f32 v32, v32\n\t"
    "v_cvt_pk_bf16_f32 v56, v24, v25\n\t"
    "v_cvt_pk_bf16_f32 v57, v26, v27\n\t"
    "v_cvt_pk_bf16_f32 v58, v28, v29\n\t"
    "v_cvt_pk_bf16_f32 v59, v30, v31\n\t"
    "v_exp_f32 v33, v33\n\t"
    "v_exp_f32 v34, v34\n\t"
    "v_smfmac_f32_16x16x64_bf16 v[252:255], v[104:107], v[64:71], v109\n\t"
    "s_waitcnt vmcnt(20)\n\t"
    "ds_read_b64_tr_b16 a[184:185], v14 offset:512\n\t"
    "v_exp_f32 v35, v35\n\t"
    "v_exp_f32 v36, v36\n\t"
    "v_exp_f32 v37, v37\n\t"
    "v_exp_f32 v38, v38\n\t"
    "v_exp_f32 v39, v39\n\t"
    "v_cvt_pk_bf16_f32 v60, v32, v33\n\t"
    "v_cvt_pk_bf16_f32 v61, v34, v35\n\t"
    "v_cvt_pk_bf16_f32 v62, v36, v37\n\t"
    "v_cvt_pk_bf16_f32 v63, v38, v39\n\t"
    "v_mfma_f32_32x32x16_bf16 v[24:39], v[72:75], a[232:235], 0\n\t"
    "s_add_u32 m0, s90, 0x2c00\n\t"
    "ds_read_b64_tr_b16 a[188:189], v15 offset:512\n\t"
    "v_exp_f32 v40, v40\n\t"
    "v_exp_f32 v41, v41\n\t"
    "v_exp_f32 v42, v42\n\t"
    "v_exp_f32 v43, v43\n\t"
    "v_exp_f32 v44, v44\n\t"
    "v_exp_f32 v45, v45\n\t"
    "v_smfmac_f32_32x32x32_bf16 v[112:127], v[96:99], v[56:63], v108\n\t"
    "s_mov_b32 s62, 0x1c000\n\t"
    "v_exp_f32 v46, v46\n\t"
    "v_exp_f32 v47, v47\n\t"
    "v_exp_f32 v48, v48\n\t"
    "v_cvt_pk_bf16_f32 v64, v40, v41\n\t"
    "v_cvt_pk_bf16_f32 v65, v42, v43\n\t"
    "v_cvt_pk_bf16_f32 v66, v44, v45\n\t"
    "v_cvt_pk_bf16_f32 v67, v46, v47\n\t"
    "v_exp_f32 v49, v49\n\t"
    "v_exp_f32 v50, v50\n\t"
    "v_smfmac_f32_16x16x64_bf16 v[240:243], v[104:107], v[56:63], v109\n\t"
    "ds_read_b64_tr_b16 a[186:187], v14 offset:768\n\t"
    "v_exp_f32 v51, v51\n\t"
    "v_exp_f32 v52, v52\n\t"
    "v_exp_f32 v53, v53\n\t"
    "v_exp_f32 v54, v54\n\t"
    "v_exp_f32 v55, v55\n\t"
    "v_cvt_pk_bf16_f32 v68, v48, v49\n\t"
    "v_cvt_pk_bf16_f32 v69, v50, v51\n\t"
    "v_cvt_pk_bf16_f32 v70, v52, v53\n\t"
    "v_cvt_pk_bf16_f32 v71, v54, v55\n\t"
    "v_mfma_f32_32x32x16_bf16 v[40:55], v[72:75], a[236:239], 0\n\t"
    "ds_read_b64_tr_b16 a[190:191], v15 offset:768\n\t"
    "v_exp_f32 v24, v24\n\t"
    "v_exp_f32 v25, v25\n\t"
    "v_exp_f32 v26, v26\n\t"
    "v_exp_f32 v27, v27\n\t"
    "v_exp_f32 v28, v28\n\t"
    "v_exp_f32 v29, v29\n\t"
    "v_smfmac_f32_32x32x32_bf16 v[128:143], v[96:99], v[64:71], v108\n\t"
    "buffer_load_dwordx4 v20, %[sw1], s62 offen offset:0 lds\n\t"
    "v_exp_f32 v30, v30\n\t"
    "v_exp_f32 v31, v31\n\t"
    "v_exp_f32 v32, v32\n\t"
    "v_cvt_pk_bf16_f32 v56, v24, v25\n\t"
    "v_cvt_pk_bf16_f32 v57, v26, v27\n\t"
    "v_cvt_pk_bf16_f32 v58, v28, v29\n\t"
    "v_cvt_pk_bf16_f32 v59, v30, v31\n\t"
    "v_exp_f32 v33, v33\n\t"
    "v_exp_f32 v34, v34\n\t"
    "v_smfmac_f32_16x16x64_bf16 v[244:247], v[104:107], v[64:71], v109\n\t"
    "ds_read_b64_tr_b16 a[192:193], v14 offset:4096\n\t"
    "v_exp_f32 v35, v35\n\t"
    "v_exp_f32 v36, v36\n\t"
    "v_exp_f32 v37, v37\n\t"
    "v_exp_f32 v38, v38\n\t"
    "v_exp_f32 v39, v39\n\t"
    "v_cvt_pk_bf16_f32 v60, v32, v33\n\t"
    "v_cvt_pk_bf16_f32 v61, v34, v35\n\t"
    "v_cvt_pk_bf16_f32 v62, v36, v37\n\t"
    "v_cvt_pk_bf16_f32 v63, v38, v39\n\t"
    "v_mfma_f32_32x32x16_bf16 v[24:39], v[76:79], a[224:227], 0\n\t"
    "ds_read_b64_tr_b16 a[196:197], v15 offset:4096\n\t"
    "v_exp_f32 v40, v40\n\t"
    "v_exp_f32 v41, v41\n\t"
    "v_exp_f32 v42, v42\n\t"
    "v_exp_f32 v43, v43\n\t"
    "v_exp_f32 v44, v44\n\t"
    "v_exp_f32 v45, v45\n\t"
    "v_smfmac_f32_32x32x32_bf16 v[144:159], v[96:99], v[56:63], v108\n\t"
    "buffer_load_dwordx4 v21, %[sw1], s62 offen offset:1024 lds\n\t"
    "v_exp_f32 v46, v46\n\t"
    "v_exp_f32 v47, v47\n\t"
    "v_exp_f32 v48, v48\n\t"
    "v_cvt_pk_bf16_f32 v64, v40, v41\n\t"
    "v_cvt_pk_bf16_f32 v65, v42, v43\n\t"
    "v_cvt_pk_bf16_f32 v66, v44, v45\n\t"
    "v_cvt_pk_bf16_f32 v67, v46, v47\n\t"
    "v_exp_f32 v49, v49\n\t"
    "v_exp_f32 v50, v50\n\t"
    "v_smfmac_f32_16x16x64_bf16 v[248:251], v[104:107], v[56:63], v109\n\t"
    "ds_read_b64_tr_b16 a[194:195], v14 offset:4352\n\t"
    "v_exp_f32 v51, v51\n\t"
    "v_exp_f32 v52, v52\n\t"
    "v_exp_f32 v53, v53\n\t"
    "v_exp_f32 v54, v54\n\t"
    "v_exp_f32 v55, v55\n\t"
    "v_cvt_pk_bf16_f32 v68, v48, v49\n\t"
    "v_cvt_pk_bf16_f32 v69, v50, v51\n\t"
    "v_cvt_pk_bf16_f32 v70, v52, v53\n\t"
    "v_cvt_pk_bf16_f32 v71, v54, v55\n\t"
    "v_mfma_f32_32x32x16_bf16 v[40:55], v[76:79], a[228:231], 0\n\t"
    "buffer_load_dwordx4 v22, %[sw1], s62 offen offset:2048 lds\n\t"
    "v_exp_f32 v24, v24\n\t"
    "v_exp_f32 v25, v25\n\t"
    "v_exp_f32 v26, v26\n\t"
    "v_exp_f32 v27, v27\n\t"
    "v_exp_f32 v28, v28\n\t"
    "v_exp_f32 v29, v29\n\t"
    "v_smfmac_f32_32x32x32_bf16 v[160:175], v[96:99], v[64:71], v108\n\t"
    "s_waitcnt vmcnt(17)\n\t"
    "s_waitcnt lgkmcnt(3)\n\t"
    "v_exp_f32 v30, v30\n\t"
    "v_exp_f32 v31, v31\n\t"
    "v_exp_f32 v32, v32\n\t"
    "v_cvt_pk_bf16_f32 v56, v24, v25\n\t"
    "v_cvt_pk_bf16_f32 v57, v26, v27\n\t"
    "v_cvt_pk_bf16_f32 v58, v28, v29\n\t"
    "v_cvt_pk_bf16_f32 v59, v30, v31\n\t"
    "v_exp_f32 v33, v33\n\t"
    "v_exp_f32 v34, v34\n\t"
    "v_smfmac_f32_16x16x64_bf16 v[252:255], v[104:107], v[64:71], v109\n\t"
    "s_add_u32 m0, s90, 0x3800\n\t"
    "ds_read_b64_tr_b16 a[198:199], v15 offset:4352\n\t"
    "v_exp_f32 v35, v35\n\t"
    "v_exp_f32 v36, v36\n\t"
    "v_exp_f32 v37, v37\n\t"
    "v_exp_f32 v38, v38\n\t"
    "v_exp_f32 v39, v39\n\t"
    "v_cvt_pk_bf16_f32 v60, v32, v33\n\t"
    "v_cvt_pk_bf16_f32 v61, v34, v35\n\t"
    "v_cvt_pk_bf16_f32 v62, v36, v37\n\t"
    "v_cvt_pk_bf16_f32 v63, v38, v39\n\t"
    "v_mfma_f32_32x32x16_bf16 v[24:39], v[76:79], a[232:235], 0\n\t"
    "s_mov_b32 s62, 0x38000\n\t"
    "ds_read_b64_tr_b16 a[200:201], v14 offset:4608\n\t"
    "v_exp_f32 v40, v40\n\t"
    "v_exp_f32 v41, v41\n\t"
    "v_exp_f32 v42, v42\n\t"
    "v_exp_f32 v43, v43\n\t"
    "v_exp_f32 v44, v44\n\t"
    "v_exp_f32 v45, v45\n\t"
    "v_smfmac_f32_32x32x32_bf16 v[112:127], v[100:103], v[56:63], v108\n\t"
    "buffer_load_dwordx4 v16, %[sw1], s62 offen offset:0 lds\n\t"
    "v_exp_f32 v46, v46\n\t"
    "v_exp_f32 v47, v47\n\t"
    "v_exp_f32 v48, v48\n\t"
    "v_cvt_pk_bf16_f32 v64, v40, v41\n\t"
    "v_cvt_pk_bf16_f32 v65, v42, v43\n\t"
    "v_cvt_pk_bf16_f32 v66, v44, v45\n\t"
    "v_cvt_pk_bf16_f32 v67, v46, v47\n\t"
    "v_exp_f32 v49, v49\n\t"
    "v_exp_f32 v50, v50\n\t"
    "v_smfmac_f32_16x16x64_bf16 v[240:243], v[104:107], v[56:63], v109\n\t"
    "ds_read_b64_tr_b16 a[204:205], v15 offset:4608\n\t"
    "v_exp_f32 v51, v51\n\t"
    "v_exp_f32 v52, v52\n\t"
    "v_exp_f32 v53, v53\n\t"
    "v_exp_f32 v54, v54\n\t"
    "v_exp_f32 v55, v55\n\t"
    "v_cvt_pk_bf16_f32 v68, v48, v49\n\t"
    "v_cvt_pk_bf16_f32 v69, v50, v51\n\t"
    "v_cvt_pk_bf16_f32 v70, v52, v53\n\t"
    "v_cvt_pk_bf16_f32 v71, v54, v55\n\t"
    "v_mfma_f32_32x32x16_bf16 v[40:55], v[76:79], a[236:239], 0\n\t"
    "ds_read_b64_tr_b16 a[202:203], v14 offset:4864\n\t"
    "v_exp_f32 v24, v24\n\t"
    "v_exp_f32 v25, v25\n\t"
    "v_exp_f32 v26, v26\n\t"
    "v_exp_f32 v27, v27\n\t"
    "v_exp_f32 v28, v28\n\t"
    "v_exp_f32 v29, v29\n\t"
    "v_smfmac_f32_32x32x32_bf16 v[128:143], v[100:103], v[64:71], v108\n\t"
    "buffer_load_dwordx4 v17, %[sw1], s62 offen offset:1024 lds\n\t"
    "v_exp_f32 v30, v30\n\t"
    "v_exp_f32 v31, v31\n\t"
    "v_exp_f32 v32, v32\n\t"
    "v_cvt_pk_bf16_f32 v56, v24, v25\n\t"
    "v_cvt_pk_bf16_f32 v57, v26, v27\n\t"
    "v_cvt_pk_bf16_f32 v58, v28, v29\n\t"
    "v_cvt_pk_bf16_f32 v59, v30, v31\n\t"
    "v_exp_f32 v33, v33\n\t"
    "v_exp_f32 v34, v34\n\t"
    "v_smfmac_f32_16x16x64_bf16 v[244:247], v[104:107], v[64:71], v109\n\t"
    "ds_read_b64_tr_b16 a[206:207], v15 offset:4864\n\t"
    "v_exp_f32 v35, v35\n\t"
    "v_exp_f32 v36, v36\n\t"
    "v_exp_f32 v37, v37\n\t"
    "v_exp_f32 v38, v38\n\t"
    "v_exp_f32 v39, v39\n\t"
    "v_cvt_pk_bf16_f32 v60, v32, v33\n\t"
    "v_cvt_pk_bf16_f32 v61, v34, v35\n\t"
    "v_cvt_pk_bf16_f32 v62, v36, v37\n\t"
    "v_cvt_pk_bf16_f32 v63, v38, v39\n\t"
    "v_mfma_f32_32x32x16_bf16 v[24:39], a[240:243], a[224:227], 0\n\t"
    "ds_read_b64_tr_b16 a[208:209], v14 offset:8192\n\t"
    "v_exp_f32 v40, v40\n\t"
    "v_exp_f32 v41, v41\n\t"
    "v_exp_f32 v42, v42\n\t"
    "v_exp_f32 v43, v43\n\t"
    "v_exp_f32 v44, v44\n\t"
    "v_exp_f32 v45, v45\n\t"
    "v_smfmac_f32_32x32x32_bf16 v[144:159], v[100:103], v[56:63], v108\n\t"
    "buffer_load_dwordx4 v18, %[sw1], s62 offen offset:2048 lds\n\t"
    "v_exp_f32 v46, v46\n\t"
    "v_exp_f32 v47, v47\n\t"
    "v_exp_f32 v48, v48\n\t"
    "v_cvt_pk_bf16_f32 v64, v40, v41\n\t"
    "v_cvt_pk_bf16_f32 v65, v42, v43\n\t"
    "v_cvt_pk_bf16_f32 v66, v44, v45\n\t"
    "v_cvt_pk_bf16_f32 v67, v46, v47\n\t"
    "v_exp_f32 v49, v49\n\t"
    "v_exp_f32 v50, v50\n\t"
    "v_smfmac_f32_16x16x64_bf16 v[248:251], v[104:107], v[56:63], v109\n\t"
    "ds_read_b64_tr_b16 a[212:213], v15 offset:8192\n\t"
    "v_exp_f32 v51, v51\n\t"
    "v_exp_f32 v52, v52\n\t"
    "v_exp_f32 v53, v53\n\t"
    "v_exp_f32 v54, v54\n\t"
    "v_exp_f32 v55, v55\n\t"
    "v_cvt_pk_bf16_f32 v68, v48, v49\n\t"
    "v_cvt_pk_bf16_f32 v69, v50, v51\n\t"
    "v_cvt_pk_bf16_f32 v70, v52, v53\n\t"
    "v_cvt_pk_bf16_f32 v71, v54, v55\n\t"
    "v_mfma_f32_32x32x16_bf16 v[40:55], a[240:243], a[228:231], 0\n\t"
    "buffer_load_dwordx4 v19, %[sw1], s62 offen offset:3072 lds\n\t"
    "v_exp_f32 v24, v24\n\t"
    "v_exp_f32 v25, v25\n\t"
    "v_exp_f32 v26, v26\n\t"
    "v_exp_f32 v27, v27\n\t"
    "v_exp_f32 v28, v28\n\t"
    "v_exp_f32 v29, v29\n\t"
    "v_smfmac_f32_32x32x32_bf16 v[160:175], v[100:103], v[64:71], v108\n\t"
    "buffer_load_dwordx2 v[72:73], %[xv], %[sx], 0 offen offset:0\n\t"
    "v_exp_f32 v30, v30\n\t"
    "v_exp_f32 v31, v31\n\t"
    "v_exp_f32 v32, v32\n\t"
    "v_cvt_pk_bf16_f32 v56, v24, v25\n\t"
    "v_cvt_pk_bf16_f32 v57, v26, v27\n\t"
    "v_cvt_pk_bf16_f32 v58, v28, v29\n\t"
    "v_cvt_pk_bf16_f32 v59, v30, v31\n\t"
    "v_exp_f32 v33, v33\n\t"
    "v_exp_f32 v34, v34\n\t"
    "v_smfmac_f32_16x16x64_bf16 v[252:255], v[104:107], v[64:71], v109\n\t"
    "s_waitcnt vmcnt(16)\n\t"
    "ds_read_b64_tr_b16 a[210:211], v14 offset:8448\n\t"
    "v_exp_f32 v35, v35\n\t"
    "v_exp_f32 v36, v36\n\t"
    "v_exp_f32 v37, v37\n\t"
    "v_exp_f32 v38, v38\n\t"
    "v_exp_f32 v39, v39\n\t"
    "v_cvt_pk_bf16_f32 v60, v32, v33\n\t"
    "v_cvt_pk_bf16_f32 v61, v34, v35\n\t"
    "v_cvt_pk_bf16_f32 v62, v36, v37\n\t"
    "v_cvt_pk_bf16_f32 v63, v38, v39\n\t"
    "v_mfma_f32_32x32x16_bf16 v[24:39], a[240:243], a[232:235], 0\n\t"
    "s_waitcnt lgkmcnt(3)\n\t"
    "ds_read_b64_tr_b16 a[214:215], v15 offset:8448\n\t"
    "v_exp_f32 v40, v40\n\t"
    "v_exp_f32 v41, v41\n\t"
    "v_exp_f32 v42, v42\n\t"
    "v_exp_f32 v43, v43\n\t"
    "v_exp_f32 v44, v44\n\t"
    "v_exp_f32 v45, v45\n\t"
    "v_smfmac_f32_32x32x32_bf16 v[112:127], v[80:83], v[56:63], v108\n\t"
    "buffer_load_dwordx2 v[74:75], %[xv], %[sx], 0 offen offset:16\n\t"
    "v_exp_f32 v46, v46\n\t"
    "v_exp_f32 v47, v47\n\t"
    "v_exp_f32 v48, v48\n\t"
    "v_cvt_pk_bf16_f32 v64, v40, v41\n\t"
    "v_cvt_pk_bf16_f32 v65, v42, v43\n\t"
    "v_cvt_pk_bf16_f32 v66, v44, v45\n\t"
    "v_cvt_pk_bf16_f32 v67, v46, v47\n\t"
    "v_exp_f32 v49, v49\n\t"
    "v_exp_f32 v50, v50\n\t"
    "v_smfmac_f32_16x16x64_bf16 v[240:243], v[104:107], v[56:63], v109\n\t"
    "s_add_u32 m0, s90, 0x4800\n\t"
    "ds_read_b64_tr_b16 a[216:217], v14 offset:8704\n\t"
    "v_exp_f32 v51, v51\n\t"
    "v_exp_f32 v52, v52\n\t"
    "v_exp_f32 v53, v53\n\t"
    "v_exp_f32 v54, v54\n\t"
    "v_exp_f32 v55, v55\n\t"
    "v_cvt_pk_bf16_f32 v68, v48, v49\n\t"
    "v_cvt_pk_bf16_f32 v69, v50, v51\n\t"
    "v_cvt_pk_bf16_f32 v70, v52, v53\n\t"
    "v_cvt_pk_bf16_f32 v71, v54, v55\n\t"
    "v_mfma_f32_32x32x16_bf16 v[40:55], a[240:243], a[236:239], 0\n\t"
    "s_mov_b32 s62, 0x38000\n\t"
    "ds_read_b64_tr_b16 a[220:221], v15 offset:8704\n\t"
    "v_exp_f32 v24, v24\n\t"
    "v_exp_f32 v25, v25\n\t"
    "v_exp_f32 v26, v26\n\t"
    "v_exp_f32 v27, v27\n\t"
    "v_exp_f32 v28, v28\n\t"
    "v_exp_f32 v29, v29\n\t"
    "v_smfmac_f32_32x32x32_bf16 v[128:143], v[80:83], v[64:71], v108\n\t"
    "buffer_load_dwordx2 v[76:77], %[xv], %[sx], 0 offen offset:32\n\t"
    "v_exp_f32 v30, v30\n\t"
    "v_exp_f32 v31, v31\n\t"
    "v_exp_f32 v32, v32\n\t"
    "v_cvt_pk_bf16_f32 v56, v24, v25\n\t"
    "v_cvt_pk_bf16_f32 v57, v26, v27\n\t"
    "v_cvt_pk_bf16_f32 v58, v28, v29\n\t"
    "v_cvt_pk_bf16_f32 v59, v30, v31\n\t"
    "v_exp_f32 v33, v33\n\t"
    "v_exp_f32 v34, v34\n\t"
    "v_smfmac_f32_16x16x64_bf16 v[244:247], v[104:107], v[64:71], v109\n\t"
    "ds_read_b64_tr_b16 a[218:219], v14 offset:8960\n\t"
    "v_exp_f32 v35, v35\n\t"
    "v_exp_f32 v36, v36\n\t"
    "v_exp_f32 v37, v37\n\t"
    "v_exp_f32 v38, v38\n\t"
    "v_exp_f32 v39, v39\n\t"
    "v_cvt_pk_bf16_f32 v60, v32, v33\n\t"
    "v_cvt_pk_bf16_f32 v61, v34, v35\n\t"
    "v_cvt_pk_bf16_f32 v62, v36, v37\n\t"
    "v_cvt_pk_bf16_f32 v63, v38, v39\n\t"
    "v_mfma_f32_32x32x16_bf16 v[24:39], a[244:247], a[224:227], 0\n\t"
    "ds_read_b64_tr_b16 a[222:223], v15 offset:8960\n\t"
    "v_exp_f32 v40, v40\n\t"
    "v_exp_f32 v41, v41\n\t"
    "v_exp_f32 v42, v42\n\t"
    "v_exp_f32 v43, v43\n\t"
    "v_exp_f32 v44, v44\n\t"
    "v_exp_f32 v45, v45\n\t"
    "v_smfmac_f32_32x32x32_bf16 v[144:159], v[80:83], v[56:63], v108\n\t"
    "buffer_load_dwordx2 v[78:79], %[xv], %[sx], 0 offen offset:48\n\t"
    "v_exp_f32 v46, v46\n\t"
    "v_exp_f32 v47, v47\n\t"
    "v_exp_f32 v48, v48\n\t"
    "v_cvt_pk_bf16_f32 v64, v40, v41\n\t"
    "v_cvt_pk_bf16_f32 v65, v42, v43\n\t"
    "v_cvt_pk_bf16_f32 v66, v44, v45\n\t"
    "v_cvt_pk_bf16_f32 v67, v46, v47\n\t"
    "v_exp_f32 v49, v49\n\t"
    "v_exp_f32 v50, v50\n\t"
    "v_smfmac_f32_16x16x64_bf16 v[248:251], v[104:107], v[56:63], v109\n\t"
    "v_exp_f32 v51, v51\n\t"
    "v_exp_f32 v52, v52\n\t"
    "v_exp_f32 v53, v53\n\t"
    "v_exp_f32 v54, v54\n\t"
    "v_exp_f32 v55, v55\n\t"
    "v_cvt_pk_bf16_f32 v68, v48, v49\n\t"
    "v_cvt_pk_bf16_f32 v69, v50, v51\n\t"
    "v_cvt_pk_bf16_f32 v70, v52, v53\n\t"
    "v_cvt_pk_bf16_f32 v71, v54, v55\n\t"
    "v_mfma_f32_32x32x16_bf16 v[40:55], a[244:247], a[228:231], 0\n\t"
    "buffer_load_dwordx4 v20, %[sw1], s62 offen offset:0 lds\n\t"
    "v_exp_f32 v24, v24\n\t"
    "v_exp_f32 v25, v25\n\t"
    "v_exp_f32 v26, v26\n\t"
    "v_exp_f32 v27, v27\n\t"
    "v_exp_f32 v28, v28\n\t"
    "v_exp_f32 v29, v29\n\t"
    "v_smfmac_f32_32x32x32_bf16 v[160:175], v[80:83], v[64:71], v108\n\t"
    "buffer_load_dwordx4 v21, %[sw1], s62 offen offset:1024 lds\n\t"
    "v_exp_f32 v30, v30\n\t"
    "v_exp_f32 v31, v31\n\t"
    "v_exp_f32 v32, v32\n\t"
    "v_cvt_pk_bf16_f32 v56, v24, v25\n\t"
    "v_cvt_pk_bf16_f32 v57, v26, v27\n\t"
    "v_cvt_pk_bf16_f32 v58, v28, v29\n\t"
    "v_cvt_pk_bf16_f32 v59, v30, v31\n\t"
    "v_exp_f32 v33, v33\n\t"
    "v_exp_f32 v34, v34\n\t"
    "v_smfmac_f32_16x16x64_bf16 v[252:255], v[104:107], v[64:71], v109\n\t"
    "buffer_load_dword %[fl], %[cdv], s[92:95], 0 offen\n\t"
    "v_exp_f32 v35, v35\n\t"
    "v_exp_f32 v36, v36\n\t"
    "v_exp_f32 v37, v37\n\t"
    "v_exp_f32 v38, v38\n\t"
    "v_exp_f32 v39, v39\n\t"
    "v_cvt_pk_bf16_f32 v60, v32, v33\n\t"
    "v_cvt_pk_bf16_f32 v61, v34, v35\n\t"
    "v_cvt_pk_bf16_f32 v62, v36, v37\n\t"
    "v_cvt_pk_bf16_f32 v63, v38, v39\n\t"
    "v_mfma_f32_32x32x16_bf16 v[24:39], a[244:247], a[232:235], 0\n\t"
    "s_waitcnt lgkmcnt(0)\n\t"
    "v_exp_f32 v40, v40\n\t"
    "v_exp_f32 v41, v41\n\t"
    "v_exp_f32 v42, v42\n\t"
    "v_exp_f32 v43, v43\n\t"
    "v_exp_f32 v44, v44\n\t"
    "v_exp_f32 v45, v45\n\t"
    "v_smfmac_f32_32x32x32_bf16 v[112:127], v[84:87], v[56:63], v108\n\t"
    "v_exp_f32 v46, v46\n\t"
    "v_exp_f32 v47, v47\n\t"
    "v_exp_f32 v48, v48\n\t"
    "v_cvt_pk_bf16_f32 v64, v40, v41\n\t"
    "v_cvt_pk_bf16_f32 v65, v42, v43\n\t"
    "v_cvt_pk_bf16_f32 v66, v44, v45\n\t"
    "v_cvt_pk_bf16_f32 v67, v46, v47\n\t"
    "v_exp_f32 v49, v49\n\t"
    "v_exp_f32 v50, v50\n\t"
    "v_smfmac_f32_16x16x64_bf16 v[240:243], v[104:107], v[56:63], v109\n\t"
    "v_exp_f32 v51, v51\n\t"
    "v_exp_f32 v52, v52\n\t"
    "v_exp_f32 v53, v53\n\t"
    "v_exp_f32 v54, v54\n\t"
    "v_exp_f32 v55, v55\n\t"
    "v_cvt_pk_bf16_f32 v68, v48, v49\n\t"
    "v_cvt_pk_bf16_f32 v69, v50, v51\n\t"
    "v_cvt_pk_bf16_f32 v70, v52, v53\n\t"
    "v_cvt_pk_bf16_f32 v71, v54, v55\n\t"
    "v_mfma_f32_32x32x16_bf16 v[40:55], a[244:247], a[236:239], 0\n\t"
    "v_exp_f32 v24, v24\n\t"
    "v_exp_f32 v25, v25\n\t"
    "v_exp_f32 v26, v26\n\t"
    "v_exp_f32 v27, v27\n\t"
    "v_exp_f32 v28, v28\n\t"
    "v_exp_f32 v29, v29\n\t"
    "v_smfmac_f32_32x32x32_bf16 v[128:143], v[84:87], v[64:71], v108\n\t"
    "v_exp_f32 v30, v30\n\t"
    "v_exp_f32 v31, v31\n\t"
    "v_exp_f32 v32, v32\n\t"
    "v_cvt_pk_bf16_f32 v56, v24, v25\n\t"
    "v_cvt_pk_bf16_f32 v57, v26, v27\n\t"
    "v_cvt_pk_bf16_f32 v58, v28, v29\n\t"
    "v_cvt_pk_bf16_f32 v59, v30, v31\n\t"
    "v_exp_f32 v33, v33\n\t"
    "v_exp_f32 v34, v34\n\t"
    "v_smfmac_f32_16x16x64_bf16 v[244:247], v[104:107], v[64:71], v109\n\t"
    "v_exp_f32 v35, v35\n\t"
    "v_exp_f32 v36, v36\n\t"
    "v_exp_f32 v37, v37\n\t"
    "v_exp_f32 v38, v38\n\t"
    "v_exp_f32 v39, v39\n\t"
    "v_cvt_pk_bf16_f32 v60, v32, v33\n\t"
    "v_cvt_pk_bf16_f32 v61, v34, v35\n\t"
    "v_cvt_pk_bf16_f32 v62, v36, v37\n\t"
    "v_cvt_pk_bf16_f32 v63, v38, v39\n\t"
    "v_exp_f32 v40, v40\n\t"
    "v_exp_f32 v41, v41\n\t"
    "v_exp_f32 v42, v42\n\t"
    "v_exp_f32 v43, v43\n\t"
    "v_exp_f32 v44, v44\n\t"
    "v_exp_f32 v45, v45\n\t"
    "v_smfmac_f32_32x32x32_bf16 v[144:159], v[84:87], v[56:63], v108\n\t"
    "v_exp_f32 v46, v46\n\t"
    "v_exp_f32 v47, v47\n\t"
    "v_exp_f32 v48, v48\n\t"
    "v_cvt_pk_bf16_f32 v64, v40, v41\n\t"
    "v_cvt_pk_bf16_f32 v65, v42, v43\n\t"
    "v_cvt_pk_bf16_f32 v66, v44, v45\n\t"
    "v_cvt_pk_bf16_f32 v67, v46, v47\n\t"
    "v_exp_f32 v49, v49\n\t"
    "v_exp_f32 v50, v50\n\t"
    "v_smfmac_f32_16x16x64_bf16 v[248:251], v[104:107], v[56:63], v109\n\t"
    "v_exp_f32 v51, v51\n\t"
    "v_exp_f32 v52, v52\n\t"
    "v_exp_f32 v53, v53\n\t"
    "v_exp_f32 v54, v54\n\t"
    "v_exp_f32 v55, v55\n\t"
    "v_cvt_pk_bf16_f32 v68, v48, v49\n\t"
    "v_cvt_pk_bf16_f32 v69, v50, v51\n\t"
    "v_cvt_pk_bf16_f32 v70, v52, v53\n\t"
    "v_cvt_pk_bf16_f32 v71, v54, v55\n\t"
    "v_pk_add_f32 v[112:113], v[112:113], v[120:121]\n\t"
    "v_pk_add_f32 v[114:115], v[114:115], v[122:123]\n\t"
    "v_pk_add_f32 v[116:117], v[116:117], v[124:125]\n\t"
    "v_pk_add_f32 v[118:119], v[118:119], v[126:127]\n\t"
    "v_smfmac_f32_32x32x32_bf16 v[160:175], v[84:87], v[64:71], v108\n\t"
    "v_smfmac_f32_16x16x64_bf16 v[252:255], v[104:107], v[64:71], v109\n\t"
    ASTAMP(13)
    "v_pk_add_f32 v[144:145], v[144:145], v[152:153]\n\t"
    "v_pk_add_f32 v[146:147], v[146:147], v[154:155]\n\t"
    "v_pk_add_f32 v[148:149], v[148:149], v[156:157]\n\t"
    "v_pk_add_f32 v[150:151], v[150:151], v[158:159]\n\t"
    "ds_write_b128 %[xw], v[144:147]\n\t"
    "ds_write_b128 %[xw], v[148:151] offset:1024\n\t"
    "v_pk_add_f32 v[128:129], v[128:129], v[136:137]\n\t"
    "v_pk_add_f32 v[130:131], v[130:131], v[138:139]\n\t"
    "v_pk_add_f32 v[132:133], v[132:133], v[140:141]\n\t"
    "v_pk_add_f32 v[134:135], v[134:135], v[142:143]\n\t"
    "v_add_f32 v240, v240, v241\n\t"
    "v_add_f32 v241, v244, v245\n\t"
    "v_add_f32 v248, v248, v249\n\t"
    "v_add_f32 v249, v252, v253\n\t"
    "v_pk_add_f32 v[160:161], v[160:161], v[168:169]\n\t"
    "v_pk_add_f32 v[162:163], v[162:163], v[170:171]\n\t"
    "v_pk_add_f32 v[164:165], v[164:165], v[172:173]\n\t"
    "v_pk_add_f32 v[166:167], v[166:167], v[174:175]\n\t"
    "ds_write_b64 %[xw], v[248:249] offset:4096\n\t"
    "ds_write_b128 %[xw], v[160:163] offset:2048\n\t"
    "ds_write_b128 %[xw], v[164:167] offset:3072\n\t"
    "s_waitcnt vmcnt(0) lgkmcnt(0)\n\t"
    "s_barrier\n\t"
    "ds_read_b64 v[248:249], %[xr] offset:4096\n\t"
    "ds_read_b128 v[144:147], %[xr] offset:0\n\t"
    "ds_read_b128 v[148:151], %[xr] offset:1024\n\t"
    "ds_read_b128 v[152:155], %[xr] offset:2048\n\t"
    "ds_read_b128 v[156:159], %[xr] offset:3072\n\t"
    "s_waitcnt lgkmcnt(4)\n\t"
    "v_pk_add_f32 v[240:241], v[240:241], v[248:249]\n\t"
    "v_rcp_f32 v248, v240\n\t"
    "v_rcp_f32 v252, v241\n\t"
    "s_waitcnt lgkmcnt(2)\n\t"
    "v_pk_add_f32 v[112:113], v[112:113], v[144:145]\n\t"
    "v_pk_add_f32 v[114:115], v[114:115], v[146:147]\n\t"
    "v_pk_add_f32 v[116:117], v[116:117], v[148:149]\n\t"
    "v_pk_add_f32 v[118:119], v[118:119], v[150:151]\n\t"
    "v_pk_mul_f32 v[120:121], v[248:249], v[112:113] op_sel_hi:[0,1]\n\t"
    "v_pk_mul_f32 v[122:123], v[248:249], v[114:115] op_sel_hi:[0,1]\n\t"
    "v_pk_mul_f32 v[124:125], v[248:249], v[116:117] op_sel_hi:[0,1]\n\t"
    "v_pk_mul_f32 v[126:127], v[248:249], v[118:119] op_sel_hi:[0,1]\n\t"
    "v_cvt_pk_bf16_f32 v24, v120, v121\n\t"
    "v_cvt_pk_bf16_f32 v25, v122, v123\n\t"
    "v_cvt_pk_bf16_f32 v26, v124, v125\n\t"
    "v_cvt_pk_bf16_f32 v27, v126, v127\n\t"
    "buffer_store_dwordx4 v[24:27], %[ov], %[sws], 0 offen sc1\n\t"
    "s_mov_b32 s60, 0x8000\n\t"
    "s_waitcnt lgkmcnt(0)\n\t"
    "v_pk_add_f32 v[128:129], v[128:129], v[152:153]\n\t"
    "v_pk_add_f32 v[130:131], v[130:131], v[154:155]\n\t"
    "v_pk_add_f32 v[132:133], v[132:133], v[156:157]\n\t"
    "v_pk_add_f32 v[134:135], v[134:135], v[158:159]\n\t"
    "v_pk_mul_f32 v[136:137], v[252:253], v[128:129] op_sel_hi:[0,1]\n\t"
    "v_pk_mul_f32 v[138:139], v[252:253], v[130:131] op_sel_hi:[0,1]\n\t"
    "v_pk_mul_f32 v[140:141], v[252:253], v[132:133] op_sel_hi:[0,1]\n\t"
    "v_pk_mul_f32 v[142:143], v[252:253], v[134:135] op_sel_hi:[0,1]\n\t"
    "v_cvt_pk_bf16_f32 v28, v136, v137\n\t"
    "v_cvt_pk_bf16_f32 v29, v138, v139\n\t"
    "v_cvt_pk_bf16_f32 v30, v140, v141\n\t"
    "v_cvt_pk_bf16_f32 v31, v142, v143\n\t"
    "buffer_store_dwordx4 v[28:31], %[ov], %[sws], s60 offen sc1\n\t"
    "v_pk_add_f32 v[112:113], v[112:113], v[128:129]\n\t"
    "v_pk_add_f32 v[114:115], v[114:115], v[130:131]\n\t"
    "v_pk_add_f32 v[116:117], v[116:117], v[132:133]\n\t"
    "v_pk_add_f32 v[118:119], v[118:119], v[134:135]\n\t"
    "v_pk_add_f32 v[112:113], v[112:113], v[114:115]\n\t"
    "v_pk_add_f32 v[116:117], v[116:117], v[118:119]\n\t"
    "v_pk_add_f32 v[112:113], v[112:113], v[116:117]\n\t"
    "v_add_f32 v112, v112, v113\n\t"
    "s_mov_b32 s60, 0x2b800000\n\t"
    "s_mov_b32 s61, 0x5d800000\n\t"
    "s_mov_b32 s62, 0x7b800000\n\t"
    "v_cmp_nlt_f32 %[bad], |v112|, s62\n\t"
    "v_cmp_nlt_f32 vcc, s60, v240\n\t"
    "s_or_b64 %[bad], %[bad], vcc\n\t"
    "v_cmp_ngt_f32 vcc, s61, v240\n\t"
    "s_or_b64 %[bad], %[bad], vcc\n\t"
    "v_cmp_nlt_f32 vcc, s60, v241\n\t"
    "s_or_b64 %[bad], %[bad], vcc\n\t"
    "v_cmp_ngt_f32 vcc, s61, v241\n\t"
    "s_or_b64 %[bad], %[bad], vcc\n\t"
            : "=&{v[176:207]}"(woA), "=&{v[208:239]}"(woB), "=&{v[72:79]}"(xres), [bad] "=&s"(bad), [fl] "+v"(fl) ASTAMP_OPS
            : [kv] "v"(kv), [q0] "v"(q0), [q1] "v"(q0 ^ 2048), [tv] "v"(tv), [xv] "v"(xv), [ov] "v"(ov), [xw] "v"(xw),
              [xr] "v"(xr), "{v[16:23]}"(w1v), "{v[12:15]}"(tr2), "{v[104:107]}"(la), "{v[108:109]}"(ix),
              [tag] "s"(tag), [sws] "s"(sws), [sx] "s"(sx), [swo] "s"(swo), [sw1] "s"(sw1), [sw2] "s"(sw2), [twv] "v"(twv), [cdv] "v"(cdv), "{s[92:95]}"(scode),
              "{s[88:91]}"((u32x4){wv * 16384u, wv * 448u * 1024u, mA, mB})
            : "v24", "v25", "v26", "v27", "v28", "v29", "v30", "v31", "v32", "v33", "v34", "v35", "v36", "v37", "v38", "v39", "v40", "v41", "v42", "v43", "v44", "v45", "v46", "v47", "v48", "v49", "v50", "v51", "v52", "v53", "v54", "v55", "v56", "v57", "v58", "v59", "v60", "v61", "v62", "v63", "v64", "v65", "v66", "v67", "v68", "v69", "v70", "v71", "v80", "v81", "v82", "v83", "v84", "v85", "v86", "v87", "v88", "v89", "v90", "v91", "v92", "v93", "v94", "v95", "v96", "v97", "v98", "v99", "v100", "v101", "v102", "v103", "v112", "v113", "v114", "v115", "v116", "v117", "v118", "v119", "v120", "v121", "v122", "v123", "v124", "v125", "v126", "v127", "v128", "v129", "v130", "v131", "v132", "v133", "v134", "v135", "v136", "v137", "v138", "v139", "v140", "v141", "v142", "v143", "v144", "v145", "v146", "v147", "v148", "v149", "v150", "v151", "v152", "v153", "v154", "v155", "v156", "v157", "v158", "v159", "v160", "v161", "v162", "v163", "v164", "v165", "v166", "v167", "v168", "v169", "v170", "v171", "v172", "v173", "v174", "v175", "v240", "v241", "v242", "v243", "v244", "v245", "v246", "v247", "v248", "v249", "v250", "v251", "v252", "v253", "v254", "v255", "a0", "a1", "a2", "a3", "a4", "a5", "a6", "a7", "a8", "a9", "a10", "a11", "a12", "a13", "a14", "a15", "a16", "a17", "a18", "a19", "a20", "a21", "a22", "a23", "a24", "a25", "a26", "a27", "a28", "a29", "a30", "a31", "a32", "a33", "a34", "a35", "a36", "a37", "a38", "a39", "a40", "a41", "a42", "a43", "a44", "a45", "a46", "a47", "a48", "a49", "a50", "a51", "a52", "a53", "a54", "a55", "a56", "a57", "a58", "a59", "a60", "a61", "a62", "a63", "a64", "a65", "a66", "a67", "a68", "a69", "a70", "a71", "a72", "a73", "a74", "a75", "a76", "a77", "a78", "a79", "a80", "a81", "a82", "a83", "a84", "a85", "a86", "a87", "a88", "a89", "a90", "a91", "a92", "a93", "a94", "a95", "a96", "a97", "a98", "a99", "a100", "a101", "a102", "a103", "a104", "a105", "a106", "a107", "a108", "a109", "a110", "a111", "a112", "a113", "a114", "a115", "a116", "a117", "a118", "a119", "a120", "a121", "a122", "a123", "a124", "a125", "a126", "a127", "a128", "a129", "a130", "a131", "a132", "a133", "a134", "a135", "a136", "a137", "a138", "a139", "a140", "a141", "a142", "a143", "a144", "a145", "a146", "a147", "a148", "a149", "a150", "a151", "a152", "a153", "a154", "a155", "a156", "a157", "a158", "a159", "a160", "a161", "a162", "a163", "a164", "a165", "a166", "a167", "a168", "a169", "a170", "a171", "a172", "a173", "a174", "a175", "a176", "a177", "a178", "a179", "a180", "a181", "a182", "a183", "a184", "a185", "a186", "a187", "a188", "a189", "a190", "a191", "a192", "a193", "a194", "a195", "a196", "a197", "a198", "a199", "a200", "a201", "a202", "a203", "a204", "a205", "a206", "a207", "a208", "a209", "a210", "a211", "a212", "a213", "a214", "a215", "a216", "a217", "a218", "a219", "a220", "a221", "a222", "a223", "a224", "a225", "a226", "a227", "a228", "a229", "a230", "a231", "a232", "a233", "a234", "a235", "a236", "a237", "a238", "a239", "a240", "a241", "a242", "a243", "a244", "a245", "a246", "a247", "a248", "a249", "a250", "a251", "a252", "a253", "a254", "a255", "s60", "s61", "s62", "s63", "vcc", "scc", "memory" ASTAMP_CLOB);
        if (__builtin_expect(bad != 0, 0)) {
            const int ln = lane_id();
#pragma unroll 1
            for (int q = 0; q < 2; ++q) {
                const u32x4 qf = __builtin_amdgcn_raw_buffer_load_b128(rws, WS_Q + head * 65536 + (qt + 2 * kh + q) * 1024 + ln * 16, 0, 16);
                float m = -INFINITY;
                f32x16 o = f32x16{};
                f32x4 l4 = f32x4{};
#pragma unroll 1
                for (int t = 0; t < 64; ++t) {
                    const u32x4 kf = __builtin_amdgcn_raw_buffer_load_b128(rws, WS_K + head * 65536 + t * 1024 + ln * 16, 0, 16);
                    const u32x4 vf = __builtin_amdgcn_raw_buffer_load_b128(rws, WS_V + head * 65536 + t * 1024 + ln * 16, 0, 16);
                    f32x16 s;
                    asm volatile("v_mfma_f32_32x32x16_bf16 %0, %1, %2, 0\n\ts_nop 15" : "=&v"(s) : "v"(kf), "v"(qf));
                    float tm = s[0];
#pragma unroll
                    for (int i = 1; i < 16; ++i) tm = fmaxf(tm, s[i]);
                    const float mn = fmaxf(m, xhalf_max(tm));
                    const float al = __builtin_amdgcn_exp2f(m - mn);
                    m = mn;
                    u32x8 p;
#pragma unroll
                    for (int i = 0; i < 8; ++i) p[i] = cvtpk(__builtin_amdgcn_exp2f(s[2 * i] - mn), __builtin_amdgcn_exp2f(s[2 * i + 1] - mn));
                    o *= al; l4 *= al;
                    asm volatile("s_nop 1\n\tv_smfmac_f32_32x32x32_bf16 %0, %2, %3, %5\n\tv_smfmac_f32_16x16x64_bf16 %1, %4, %3, %6\n\ts_nop 15"
                                 : "+v"(o), "+v"(l4) : "v"(vf), "v"(p), "v"(la), "v"(ix[0]), "v"(ix[1]));
                }
                const float rl = __builtin_amdgcn_rcpf(l4[0] + l4[1]);
                u32x4 pk;
#pragma unroll
                for (int i = 0; i < 4; ++i) pk[i] = cvtpk((o[2 * i] + o[8 + 2 * i]) * rl, (o[2 * i + 1] + o[9 + 2 * i]) * rl);
                st16(rws, WS_O + ((qt + 2 * kh + q) * 32 + opos) * 1024 + ln * 16, pk);
            }
        }
    }
    {
        const int lane = lane_id(), r32 = lane & 31, hh = lane >> 5, q4 = (lane & 15) >> 2;
        const unsigned tf = lds0 + hh * 896 + q4 * 64 + ((lane >> 4) & 1) * 32 + (lane & 3) * 8, th = lds0 + hh * 896 + 768 + q4 * 32 + (lane & 3) * 8;
        const unsigned c0 = wv == 3 ? th : tf + wv * 256, scr = lds0 + LDS_SCR;
        const u32x4 tr = {tf, tf + 57344, th, th + 57344};
        const u32x8 va = {lane * 16u, lane * 4u, (unsigned)(tt5 * 32 + r32) * 1024 + nt5 * 64 + hh * 16, c0, c0 + 57344,
                          scr + wv * 12288 + lane * 16, scr + hh * 16, scr + wv * 12288 + r32 * 4};
        const u32x4 sout = mksrd(out, 2048 * 1024);
        STAMP(26);
        asm volatile(
    "v_mov_b32 v64, 0\n\t"
    "v_mov_b32 v65, 0\n\t"
    "v_mov_b32 v66, 0\n\t"
    "v_mov_b32 v67, 0\n\t"
    "v_mov_b32 v10, %[tagl]\n\t"
    "v_mov_b32 v11, %[tagh]\n\t"
    "ds_read_b64_tr_b16 v[24:25], v19 offset:0\n\t"
    "ds_read_b64_tr_b16 v[26:27], v19 offset:1792\n\t"
    "ds_read_b64_tr_b16 v[28:29], v19 offset:3584\n\t"
    "ds_read_b64_tr_b16 v[30:31], v19 offset:5376\n\t"
    "ds_read_b64_tr_b16 v[32:33], v19 offset:7168\n\t"
    "ds_read_b64_tr_b16 v[34:35], v19 offset:8960\n\t"
    "ds_read_b64_tr_b16 v[36:37], v19 offset:10752\n\t"
    "ds_read_b64_tr_b16 v[38:39], v19 offset:12544\n\t"
    "ds_read_b64_tr_b16 v[40:41], v19 offset:14336\n\t"
    "ds_read_b64_tr_b16 v[42:43], v19 offset:16128\n\t"
    "ds_read_b64_tr_b16 v[44:45], v19 offset:17920\n\t"
    "ds_read_b64_tr_b16 v[46:47], v19 offset:19712\n\t"
    "s_waitcnt vmcnt(0)\n\t"
    "s_mov_b64 exec, 1\n\t"
    "buffer_store_dwordx2 v[10:11], off, %[sws], s68 sc1\n\t"
    "s_mov_b64 exec, -1\n\t"
    ASTAMP(14)
    "ds_read_b64_tr_b16 v[48:49], v19 offset:21504\n\t"
    "ds_read_b64_tr_b16 v[50:51], v19 offset:23296\n\t"
    "ds_read_b64_tr_b16 v[52:53], v19 offset:25088\n\t"
    "ds_read_b64_tr_b16 v[54:55], v19 offset:26880\n\t"
    "s_waitcnt lgkmcnt(12)\n\t"
    "v_smfmac_f32_16x16x64_bf16 v[64:67], v[104:107], v[24:31], v109\n\t"
    "ds_read_b64_tr_b16 v[56:57], v19 offset:28672\n\t"
    "ds_read_b64_tr_b16 v[58:59], v19 offset:30464\n\t"
    "ds_read_b64_tr_b16 v[60:61], v19 offset:32256\n\t"
    "ds_read_b64_tr_b16 v[62:63], v19 offset:34048\n\t"
    "s_waitcnt lgkmcnt(12)\n\t"
    "v_smfmac_f32_16x16x64_bf16 v[64:67], v[104:107], v[32:39], v109\n\t"
    "ds_read_b64_tr_b16 v[24:25], v19 offset:35840\n\t"
    "ds_read_b64_tr_b16 v[26:27], v19 offset:37632\n\t"
    "ds_read_b64_tr_b16 v[28:29], v19 offset:39424\n\t"
    "ds_read_b64_tr_b16 v[30:31], v19 offset:41216\n\t"
    "s_waitcnt lgkmcnt(12)\n\t"
    "v_smfmac_f32_16x16x64_bf16 v[64:67], v[104:107], v[40:47], v109\n\t"
    "ds_read_b64_tr_b16 v[32:33], v19 offset:43008\n\t"
    "ds_read_b64_tr_b16 v[34:35], v19 offset:44800\n\t"
    "ds_read_b64_tr_b16 v[36:37], v19 offset:46592\n\t"
    "ds_read_b64_tr_b16 v[38:39], v19 offset:48384\n\t"
    "s_waitcnt lgkmcnt(12)\n\t"
    "v_smfmac_f32_16x16x64_bf16 v[64:67], v[104:107], v[48:55], v109\n\t"
    "ds_read_b64_tr_b16 v[40:41], v19 offset:50176\n\t"
    "ds_read_b64_tr_b16 v[42:43], v19 offset:51968\n\t"
    "ds_read_b64_tr_b16 v[44:45], v19 offset:53760\n\t"
    "ds_read_b64_tr_b16 v[46:47], v19 offset:55552\n\t"
    "s_waitcnt lgkmcnt(12)\n\t"
    "v_smfmac_f32_16x16x64_bf16 v[64:67], v[104:107], v[56:63], v109\n\t"
    "ds_read_b64_tr_b16 v[48:49], v20 offset:0\n\t"
    "ds_read_b64_tr_b16 v[50:51], v20 offset:1792\n\t"
    "ds_read_b64_tr_b16 v[52:53], v20 offset:3584\n\t"
    "ds_read_b64_tr_b16 v[54:55], v20 offset:5376\n\t"
    "s_waitcnt lgkmcnt(12)\n\t"
    "v_smfmac_f32_16x16x64_bf16 v[64:67], v[104:107], v[24:31], v109\n\t"
    "ds_read_b64_tr_b16 v[56:57], v20 offset:7168\n\t"
    "ds_read_b64_tr_b16 v[58:59], v20 offset:8960\n\t"
    "ds_read_b64_tr_b16 v[60:61], v20 offset:10752\n\t"
    "ds_read_b64_tr_b16 v[62:63], v20 offset:12544\n\t"
    "s_waitcnt lgkmcnt(12)\n\t"
    "v_smfmac_f32_16x16x64_bf16 v[64:67], v[104:107], v[32:39], v109\n\t"
    "ds_read_b64_tr_b16 v[24:25], v20 offset:14336\n\t"
    "ds_read_b64_tr_b16 v[26:27], v20 offset:16128\n\t"
    "ds_read_b64_tr_b16 v[28:29], v20 offset:17920\n\t"
    "ds_read_b64_tr_b16 v[30:31], v20 offset:19712\n\t"
    "s_waitcnt lgkmcnt(12)\n\t"
    "v_smfmac_f32_16x16x64_bf16 v[64:67], v[104:107], v[40:47], v109\n\t"
    "ds_read_b64_tr_b16 v[32:33], v20 offset:21504\n\t"
    "ds_read_b64_tr_b16 v[34:35], v20 offset:23296\n\t"
    "ds_read_b64_tr_b16 v[36:37], v20 offset:25088\n\t"
    "ds_read_b64_tr_b16 v[38:39], v20 offset:26880\n\t"
    "s_waitcnt lgkmcnt(12)\n\t"
    "v_smfmac_f32_16x16x64_bf16 v[64:67], v[104:107], v[48:55], v109\n\t"
    "ds_read_b64_tr_b16 v[40:41], v20 offset:28672\n\t"
    "ds_read_b64_tr_b16 v[42:43], v20 offset:30464\n\t"
    "ds_read_b64_tr_b16 v[44:45], v20 offset:32256\n\t"
    "ds_read_b64_tr_b16 v[46:47], v20 offset:34048\n\t"
    "s_waitcnt lgkmcnt(12)\n\t"
    "v_smfmac_f32_16x16x64_bf16 v[64:67], v[104:107], v[56:63], v109\n\t"
    "ds_read_b64_tr_b16 v[48:49], v20 offset:35840\n\t"
    "ds_read_b64_tr_b16 v[50:51], v20 offset:37632\n\t"
    "ds_read_b64_tr_b16 v[52:53], v20 offset:39424\n\t"
    "ds_read_b64_tr_b16 v[54:55], v20 offset:41216\n\t"
    "s_waitcnt lgkmcnt(12)\n\t"
    "v_smfmac_f32_16x16x64_bf16 v[64:67], v[104:107], v[24:31], v109\n\t"
    "ds_read_b64_tr_b16 v[56:57], v20 offset:43008\n\t"
    "ds_read_b64_tr_b16 v[58:59], v20 offset:44800\n\t"
    "ds_read_b64_tr_b16 v[60:61], v20 offset:46592\n\t"
    "ds_read_b64_tr_b16 v[62:63], v20 offset:48384\n\t"
    "s_waitcnt lgkmcnt(12)\n\t"
    "v_smfmac_f32_16x16x64_bf16 v[64:67], v[104:107], v[32:39], v109\n\t"
    "ds_read_b64_tr_b16 v[24:25], v20 offset:50176\n\t"
    "ds_read_b64_tr_b16 v[26:27], v20 offset:51968\n\t"
    "ds_read_b64_tr_b16 v[28:29], v20 offset:53760\n\t"
    "ds_read_b64_tr_b16 v[30:31], v20 offset:55552\n\t"
    "s_waitcnt lgkmcnt(12)\n\t"
    "v_smfmac_f32_16x16x64_bf16 v[64:67], v[104:107], v[40:47], v109\n\t"
    "s_waitcnt lgkmcnt(8)\n\t"
    "v_smfmac_f32_16x16x64_bf16 v[64:67], v[104:107], v[48:55], v109\n\t"
    "s_waitcnt lgkmcnt(4)\n\t"
    "v_smfmac_f32_16x16x64_bf16 v[64:67], v[104:107], v[56:63], v109\n\t"
    "s_waitcnt lgkmcnt(0)\n\t"
    "v_smfmac_f32_16x16x64_bf16 v[64:67], v[104:107], v[24:31], v109\n\t"
    "s_barrier\n\t"
    "v_lshlrev_b32 v112, 16, v72\n\t"
    "v_and_b32 v113, 0xffff0000, v72\n\t"
    "v_lshlrev_b32 v114, 16, v73\n\t"
    "v_and_b32 v115, 0xffff0000, v73\n\t"
    "v_lshlrev_b32 v116, 16, v74\n\t"
    "v_and_b32 v117, 0xffff0000, v74\n\t"
    "v_lshlrev_b32 v118, 16, v75\n\t"
    "v_and_b32 v119, 0xffff0000, v75\n\t"
    "v_lshlrev_b32 v120, 16, v76\n\t"
    "v_and_b32 v121, 0xffff0000, v76\n\t"
    "v_lshlrev_b32 v122, 16, v77\n\t"
    "v_and_b32 v123, 0xffff0000, v77\n\t"
    "v_lshlrev_b32 v124, 16, v78\n\t"
    "v_and_b32 v125, 0xffff0000, v78\n\t"
    "v_lshlrev_b32 v126, 16, v79\n\t"
    "v_and_b32 v127, 0xffff0000, v79\n\t"
    "v_add_u32 v97, s84, v16\n\t"
    "v_add_u32 v98, s85, v16\n\t"
    "v_add_u32 v99, s86, v16\n\t"
    "v_and_b32 v96, 0x70, v16\n\t"
    "v_add_f32 v100, v64, v65\n\t"
    "s_add_u32 s60, s70, 0x1000\n\t"
    "s_add_u32 s61, s71, 0x1000\n\t"
    "v_add_u32 v101, %[ct1k], v16\n\t"
    "s_mov_b32 s63, 0x40000\n\t"
    "proj_poll:\n\t"
    "buffer_load_dwordx2 v[248:249], v96, %[sws], s69 offen sc1\n\t"
    "s_waitcnt vmcnt(0)\n\t"
    "v_cmp_ne_u64 vcc, %[tag], v[248:249]\n\t"
    "s_cbranch_vccz proj_go\n\t"
    "s_sub_u32 s63, s63, 1\n\t"
    "s_cbranch_scc0 proj_poll\n\t"
    "proj_go:\n\t"
    "s_barrier\n\t"
    ASTAMP(15)
    "buffer_load_dwordx4 v[24:27], v16, %[sws], s71 offen offset:0 sc1\n\t"
    "s_nop 11\n\t"
    "buffer_load_dwordx4 v[28:31], v16, %[sws], s71 offen offset:1024 sc1\n\t"
    "s_nop 11\n\t"
    "buffer_load_dwordx4 v[32:35], v16, %[sws], s71 offen offset:2048 sc1\n\t"
    "s_nop 11\n\t"
    "buffer_load_dwordx4 v[36:39], v16, %[sws], s71 offen offset:3072 sc1\n\t"
    "s_nop 11\n\t"
    "buffer_load_dwordx4 v[40:43], v16, %[sws], s61 offen offset:0 sc1\n\t"
    "s_nop 11\n\t"
    "buffer_load_dwordx4 v[44:47], v16, %[sws], s61 offen offset:1024 sc1\n\t"
    "s_nop 11\n\t"
    "buffer_load_dwordx4 v[48:51], v16, %[sws], s61 offen offset:2048 sc1\n\t"
    "s_nop 11\n\t"
    "buffer_load_dwordx4 v[52:55], v16, %[sws], s61 offen offset:3072 sc1\n\t"
    "s_nop 11\n\t"
    "s_waitcnt vmcnt(7)\n\t"
    "v_mfma_f32_32x32x16_bf16 v[144:159], v[176:179], v[24:27], 0\n\t"
    "buffer_load_dwordx4 v[56:59], v16, %[sws], s70 offen offset:0 sc1\n\t"
    "v_mfma_f32_32x32x16_bf16 v[160:175], v[180:183], v[24:27], 0\n\t"
    "buffer_load_dwordx4 v[60:63], v16, %[sws], s70 offen offset:1024 sc1\n\t"
    "s_waitcnt vmcnt(8)\n\t"
    "v_mfma_f32_32x32x16_bf16 v[144:159], v[184:187], v[28:31], v[144:159]\n\t"
    "buffer_load_dwordx4 v[64:67], v16, %[sws], s70 offen offset:2048 sc1\n\t"
    "v_mfma_f32_32x32x16_bf16 v[160:175], v[188:191], v[28:31], v[160:175]\n\t"
    "buffer_load_dwordx4 v[68:71], v16, %[sws], s70 offen offset:3072 sc1\n\t"
    "s_waitcnt vmcnt(9)\n\t"
    "v_mfma_f32_32x32x16_bf16 v[144:159], v[192:195], v[32:35], v[144:159]\n\t"
    "buffer_load_dwordx4 v[72:75], v16, %[sws], s60 offen offset:0 sc1\n\t"
    "v_mfma_f32_32x32x16_bf16 v[160:175], v[196:199], v[32:35], v[160:175]\n\t"
    "buffer_load_dwordx4 v[76:79], v16, %[sws], s60 offen offset:1024 sc1\n\t"
    "s_waitcnt vmcnt(10)\n\t"
    "v_mfma_f32_32x32x16_bf16 v[144:159], v[200:203], v[36:39], v[144:159]\n\t"
    "buffer_load_dwordx4 v[80:83], v16, %[sws], s60 offen offset:2048 sc1\n\t"
    "v_mfma_f32_32x32x16_bf16 v[160:175], v[204:207], v[36:39], v[160:175]\n\t"
    "buffer_load_dwordx4 v[84:87], v16, %[sws], s60 offen offset:3072 sc1\n\t"
    "s_waitcnt vmcnt(11)\n\t"
    "v_mfma_f32_32x32x16_bf16 v[144:159], v[208:211], v[40:43], v[144:159]\n\t"
    "v_mfma_f32_32x32x16_bf16 v[160:175], v[212:215], v[40:43], v[160:175]\n\t"
    "s_waitcnt vmcnt(10)\n\t"
    "v_mfma_f32_32x32x16_bf16 v[144:159], v[216:219], v[44:47], v[144:159]\n\t"
    "v_mfma_f32_32x32x16_bf16 v[160:175], v[220:223], v[44:47], v[160:175]\n\t"
    "s_waitcnt vmcnt(9)\n\t"
    "v_mfma_f32_32x32x16_bf16 v[144:159], v[224:227], v[48:51], v[144:159]\n\t"
    "v_mfma_f32_32x32x16_bf16 v[160:175], v[228:231], v[48:51], v[160:175]\n\t"
    "s_waitcnt vmcnt(8)\n\t"
    "v_mfma_f32_32x32x16_bf16 v[144:159], v[232:235], v[52:55], v[144:159]\n\t"
    "v_mfma_f32_32x32x16_bf16 v[160:175], v[236:239], v[52:55], v[160:175]\n\t"
    "s_waitcnt vmcnt(7)\n\t"
    "v_mfma_f32_32x32x16_bf16 v[128:143], v[180:183], v[56:59], 0\n\t"
    "s_waitcnt vmcnt(6)\n\t"
    "v_mfma_f32_32x32x16_bf16 v[128:143], v[188:191], v[60:63], v[128:143]\n\t"
    "s_nop 7\n\t"
    "s_waitcnt vmcnt(5)\n\t"
    "v_mfma_f32_32x32x16_bf16 v[128:143], v[196:199], v[64:67], v[128:143]\n\t"
    "ds_write_b128 v98, v[144:147] offset:4096\n\t"
    "ds_write_b128 v98, v[148:151] offset:5120\n\t"
    "s_waitcnt vmcnt(4)\n\t"
    "v_mfma_f32_32x32x16_bf16 v[128:143], v[204:207], v[68:71], v[128:143]\n\t"
    "ds_write_b128 v98, v[152:155] offset:6144\n\t"
    "ds_write_b128 v98, v[156:159] offset:7168\n\t"
    "s_waitcnt vmcnt(3)\n\t"
    "v_mfma_f32_32x32x16_bf16 v[128:143], v[212:215], v[72:75], v[128:143]\n\t"
    "ds_write_b128 v99, v[160:163] offset:8192\n\t"
    "s_waitcnt vmcnt(2)\n\t"
    "v_mfma_f32_32x32x16_bf16 v[128:143], v[220:223], v[76:79], v[128:143]\n\t"
    "ds_write_b128 v99, v[164:167] offset:9216\n\t"
    "s_waitcnt vmcnt(1)\n\t"
    "v_mfma_f32_32x32x16_bf16 v[128:143], v[228:231], v[80:83], v[128:143]\n\t"
    "ds_write_b128 v99, v[168:171] offset:10240\n\t"
    "s_waitcnt vmcnt(0)\n\t"
    "v_mfma_f32_32x32x16_bf16 v[128:143], v[236:239], v[84:87], v[128:143]\n\t"
    "ds_write_b128 v99, v[172:175] offset:11264\n\t"
    "v_mfma_f32_32x32x16_bf16 v[112:127], v[176:179], v[56:59], v[112:127]\n\t"
    "v_mfma_f32_32x32x16_bf16 v[112:127], v[184:187], v[60:63], v[112:127]\n\t"
    "s_nop 9\n\t"
    "ds_write_b128 v97, v[128:131] offset:0\n\t"
    "v_mfma_f32_32x32x16_bf16 v[112:127], v[192:195], v[64:67], v[112:127]\n\t"
    "ds_write_b128 v97, v[132:135] offset:1024\n\t"
    "v_mfma_f32_32x32x16_bf16 v[112:127], v[200:203], v[68:71], v[112:127]\n\t"
    "ds_write_b128 v97, v[136:139] offset:2048\n\t"
    "v_mfma_f32_32x32x16_bf16 v[112:127], v[208:211], v[72:75], v[112:127]\n\t"
    "ds_write_b128 v97, v[140:143] offset:3072\n\t"
    "v_mfma_f32_32x32x16_bf16 v[112:127], v[216:219], v[76:79], v[112:127]\n\t"
    "v_mfma_f32_32x32x16_bf16 v[112:127], v[224:227], v[80:83], v[112:127]\n\t"
    "v_mfma_f32_32x32x16_bf16 v[112:127], v[232:235], v[84:87], v[112:127]\n\t"
    ASTAMP(16)
    "s_waitcnt lgkmcnt(0)\n\t"
    "s_barrier\n\t"
    "ds_read_b128 v[24:27], v21 offset:0\n\t"
    "ds_read_b128 v[28:31], v21 offset:4096\n\t"
    "ds_read_b128 v[32:35], v21 offset:8192\n\t"
    "ds_read_b128 v[36:39], v21 offset:1024\n\t"
    "ds_read_b128 v[40:43], v21 offset:5120\n\t"
    "ds_read_b128 v[44:47], v21 offset:9216\n\t"
    "ds_read_b128 v[48:51], v21 offset:2048\n\t"
    "ds_read_b128 v[52:55], v21 offset:6144\n\t"
    "ds_read_b128 v[56:59], v21 offset:10240\n\t"
    "ds_read_b128 v[60:63], v21 offset:3072\n\t"
    "ds_read_b128 v[64:67], v21 offset:7168\n\t"
    "ds_read_b128 v[68:71], v21 offset:11264\n\t"
    "s_waitcnt lgkmcnt(9)\n\t"
    "v_pk_add_f32 v[24:25], v[24:25], v[28:29]\n\t"
    "v_pk_add_f32 v[112:113], v[112:113], v[32:33]\n\t"
    "v_pk_add_f32 v[26:27], v[26:27], v[30:31]\n\t"
    "v_pk_add_f32 v[114:115], v[114:115], v[34:35]\n\t"
    "v_pk_add_f32 v[112:113], v[112:113], v[24:25]\n\t"
    "v_pk_add_f32 v[114:115], v[114:115], v[26:27]\n\t"
    "v_cvt_pk_bf16_f32 v240, v112, v113\n\t"
    "v_cvt_pk_bf16_f32 v241, v114, v115\n\t"
    "s_waitcnt lgkmcnt(6)\n\t"
    "v_pk_add_f32 v[36:37], v[36:37], v[40:41]\n\t"
    "v_pk_add_f32 v[116:117], v[116:117], v[44:45]\n\t"
    "v_pk_add_f32 v[38:39], v[38:39], v[42:43]\n\t"
    "v_pk_add_f32 v[118:119], v[118:119], v[46:47]\n\t"
    "v_pk_add_f32 v[116:117], v[116:117], v[36:37]\n\t"
    "v_pk_add_f32 v[118:119], v[118:119], v[38:39]\n\t"
    "v_cvt_pk_bf16_f32 v242, v116, v117\n\t"
    "v_cvt_pk_bf16_f32 v243, v118, v119\n\t"
    "buffer_store_dwordx4 v[240:243], v16, %[sws], s72 offen offset:0 sc1\n\t"
    "s_waitcnt lgkmcnt(3)\n\t"
    "v_pk_add_f32 v[48:49], v[48:49], v[52:53]\n\t"
    "v_pk_add_f32 v[120:121], v[120:121], v[56:57]\n\t"
    "v_pk_add_f32 v[50:51], v[50:51], v[54:55]\n\t"
    "v_pk_add_f32 v[122:123], v[122:123], v[58:59]\n\t"
    "v_pk_add_f32 v[120:121], v[120:121], v[48:49]\n\t"
    "v_pk_add_f32 v[122:123], v[122:123], v[50:51]\n\t"
    "v_cvt_pk_bf16_f32 v244, v120, v121\n\t"
    "v_cvt_pk_bf16_f32 v245, v122, v123\n\t"
    "s_waitcnt lgkmcnt(0)\n\t"
    "v_pk_add_f32 v[60:61], v[60:61], v[64:65]\n\t"
    "v_pk_add_f32 v[124:125], v[124:125], v[68:69]\n\t"
    "v_pk_add_f32 v[62:63], v[62:63], v[66:67]\n\t"
    "v_pk_add_f32 v[126:127], v[126:127], v[70:71]\n\t"
    "v_pk_add_f32 v[124:125], v[124:125], v[60:61]\n\t"
    "v_pk_add_f32 v[126:127], v[126:127], v[62:63]\n\t"
    "v_cvt_pk_bf16_f32 v246, v124, v125\n\t"
    "v_cvt_pk_bf16_f32 v247, v126, v127\n\t"
    "buffer_store_dwordx4 v[244:247], v16, %[sws], s72 offen offset:1024 sc1\n\t"
    "v_pk_mul_f32 v[24:25], v[112:113], v[112:113]\n\t"
    "v_pk_mul_f32 v[26:27], v[114:115], v[114:115]\n\t"
    "v_pk_fma_f32 v[24:25], v[116:117], v[116:117], v[24:25]\n\t"
    "v_pk_fma_f32 v[26:27], v[118:119], v[118:119], v[26:27]\n\t"
    "v_pk_fma_f32 v[24:25], v[120:121], v[120:121], v[24:25]\n\t"
    "v_pk_fma_f32 v[26:27], v[122:123], v[122:123], v[26:27]\n\t"
    "v_pk_fma_f32 v[24:25], v[124:125], v[124:125], v[24:25]\n\t"
    "v_pk_fma_f32 v[26:27], v[126:127], v[126:127], v[26:27]\n\t"
    "v_pk_add_f32 v[112:113], v[112:113], v[120:121]\n\t"
    "v_pk_add_f32 v[114:115], v[114:115], v[122:123]\n\t"
    "v_pk_add_f32 v[116:117], v[116:117], v[124:125]\n\t"
    "v_pk_add_f32 v[118:119], v[118:119], v[126:127]\n\t"
    "v_pk_add_f32 v[24:25], v[24:25], v[26:27]\n\t"
    "v_pk_add_f32 v[112:113], v[112:113], v[114:115]\n\t"
    "v_pk_add_f32 v[116:117], v[116:117], v[118:119]\n\t"
    "v_pk_add_f32 v[112:113], v[112:113], v[116:117]\n\t"
    "v_add_f32 v25, v24, v25\n\t"
    "v_add_f32 v24, v112, v113\n\t"
    "ds_write_b32 v23, v100\n\t"
    "s_nop 0\n\t"
    "v_permlane32_swap_b32 v24, v25\n\t"
    "v_add_f32 v24, v24, v25\n\t"
    "buffer_store_dword v24, v17, %[sws], s73 offen sc1\n\t"
    ASTAMP(17)
    "v_lshlrev_b32 v96, 3, v96\n\t"
    "s_waitcnt vmcnt(0) lgkmcnt(0)\n\t"
    "s_barrier\n\t"
    "s_cmp_lg_u32 s83, 0\n\t"
    "s_cbranch_scc1 hflag_done\n\t"
    "s_mov_b64 exec, 1\n\t"
    "buffer_store_dwordx2 v[10:11], off, %[sws], s74 sc1\n\t"
    "s_mov_b64 exec, -1\n\t"
    "hflag_done:\n\t"
    ASTAMP(18)
    "ds_read_b64_tr_b16 v[88:89], v12 offset:0\n\t"
    "ds_read_b64_tr_b16 v[90:91], v12 offset:1792\n\t"
    "ds_read_b64_tr_b16 v[92:93], v12 offset:256\n\t"
    "ds_read_b64_tr_b16 v[94:95], v12 offset:2048\n\t"
    "ds_read_b64_tr_b16 v[2:3], v12 offset:3584\n\t"
    "ds_read_b64_tr_b16 v[4:5], v12 offset:5376\n\t"
    "ds_read_b64_tr_b16 v[6:7], v12 offset:3840\n\t"
    "ds_read_b64_tr_b16 v[8:9], v12 offset:5632\n\t"
    "v_add_u32 v101, %[ct42k], v16\n\t"
    "s_mov_b32 s63, 0x40000\n\t"
    "ffn1_poll:\n\t"
    "buffer_load_dwordx2 v[248:249], v96, %[sws], s75 offen sc1\n\t"
    "s_waitcnt vmcnt(0)\n\t"
    "v_cmp_ne_u64 vcc, %[tag], v[248:249]\n\t"
    "s_cbranch_vccz ffn1_go\n\t"
    "s_sub_u32 s63, s63, 1\n\t"
    "s_cbranch_scc0 ffn1_poll\n\t"
    "ffn1_go:\n\t"
    ASTAMP(19)
    "s_add_u32 s60, s76, 0x0\n\t"
    "buffer_load_dwordx4 v[24:27], v16, %[sws], s60 offen offset:0 sc1\n\t"
    "s_nop 11\n\t"
    "buffer_load_dwordx4 v[28:31], v16, %[sws], s60 offen offset:1024 sc1\n\t"
    "s_nop 11\n\t"
    "buffer_load_dwordx4 v[32:35], v16, %[sws], s60 offen offset:2048 sc1\n\t"
    "s_nop 11\n\t"
    "buffer_load_dwordx4 v[36:39], v16, %[sws], s60 offen offset:3072 sc1\n\t"
    "s_nop 11\n\t"
    "s_add_u32 s60, s76, 0x1000\n\t"
    "buffer_load_dwordx4 v[40:43], v16, %[sws], s60 offen offset:0 sc1\n\t"
    "s_nop 11\n\t"
    "buffer_load_dwordx4 v[44:47], v16, %[sws], s60 offen offset:1024 sc1\n\t"
    "s_nop 11\n\t"
    "buffer_load_dwordx4 v[48:51], v16, %[sws], s60 offen offset:2048 sc1\n\t"
    "s_nop 11\n\t"
    "buffer_load_dwordx4 v[52:55], v16, %[sws], s60 offen offset:3072 sc1\n\t"
    "s_nop 11\n\t"
    "s_waitcnt vmcnt(7)\n\t"
    "s_waitcnt lgkmcnt(6)\n\t"
    "v_mfma_f32_32x32x16_bf16 v[112:127], v[88:91], v[24:27], 0\n\t"
    "ds_read_b64_tr_b16 v[96:97], v12 offset:7168\n\t"
    "ds_read_b64_tr_b16 v[98:99], v12 offset:8960\n\t"
    "s_add_u32 s60, s76, 0x2000\n\t"
    "buffer_load_dwordx4 v[56:59], v16, %[sws], s60 offen offset:0 sc1\n\t"
    "s_waitcnt lgkmcnt(6)\n\t"
    "v_mfma_f32_32x32x16_bf16 v[128:143], v[92:95], v[24:27], 0\n\t"
    "ds_read_b64_tr_b16 v[100:101], v12 offset:7424\n\t"
    "ds_read_b64_tr_b16 v[102:103], v12 offset:9216\n\t"
    "buffer_load_dwordx4 v[60:63], v16, %[sws], s60 offen offset:1024 sc1\n\t"
    "s_waitcnt vmcnt(8)\n\t"
    "s_waitcnt lgkmcnt(6)\n\t"
    "v_mfma_f32_32x32x16_bf16 v[112:127], v[2:5], v[28:31], v[112:127]\n\t"
    "ds_read_b64_tr_b16 v[88:89], v12 offset:10752\n\t"
    "ds_read_b64_tr_b16 v[90:91], v12 offset:12544\n\t"
    "buffer_load_dwordx4 v[64:67], v16, %[sws], s60 offen offset:2048 sc1\n\t"
    "s_waitcnt lgkmcnt(6)\n\t"
    "v_mfma_f32_32x32x16_bf16 v[128:143], v[6:9], v[28:31], v[128:143]\n\t"
    "ds_read_b64_tr_b16 v[92:93], v12 offset:11008\n\t"
    "ds_read_b64_tr_b16 v[94:95], v12 offset:12800\n\t"
    "buffer_load_dwordx4 v[68:71], v16, %[sws], s60 offen offset:3072 sc1\n\t"
    "s_waitcnt vmcnt(9)\n\t"
    "s_waitcnt lgkmcnt(6)\n\t"
    "v_mfma_f32_32x32x16_bf16 v[112:127], v[96:99], v[32:35], v[112:127]\n\t"
    "ds_read_b64_tr_b16 v[2:3], v12 offset:14336\n\t"
    "ds_read_b64_tr_b16 v[4:5], v12 offset:16128\n\t"
    "s_add_u32 s60, s76, 0x3000\n\t"
    "buffer_load_dwordx4 v[72:75], v16, %[sws], s60 offen offset:0 sc1\n\t"
    "s_waitcnt lgkmcnt(6)\n\t"
    "v_mfma_f32_32x32x16_bf16 v[128:143], v[100:103], v[32:35], v[128:143]\n\t"
    "ds_read_b64_tr_b16 v[6:7], v12 offset:14592\n\t"
    "ds_read_b64_tr_b16 v[8:9], v12 offset:16384\n\t"
    "buffer_load_dwordx4 v[76:79], v16, %[sws], s60 offen offset:1024 sc1\n\t"
    "s_waitcnt vmcnt(10)\n\t"
    "s_waitcnt lgkmcnt(6)\n\t"
    "v_mfma_f32_32x32x16_bf16 v[112:127], v[88:91], v[36:39], v[112:127]\n\t"
    "ds_read_b64_tr_b16 v[96:97], v12 offset:17920\n\t"
    "ds_read_b64_tr_b16 v[98:99], v12 offset:19712\n\t"
    "buffer_load_dwordx4 v[80:83], v16, %[sws], s60 offen offset:2048 sc1\n\t"
    "s_waitcnt lgkmcnt(6)\n\t"
    "v_mfma_f32_32x32x16_bf16 v[128:143], v[92:95], v[36:39], v[128:143]\n\t"
    "ds_read_b64_tr_b16 v[100:101], v12 offset:18176\n\t"
    "ds_read_b64_tr_b16 v[102:103], v12 offset:19968\n\t"
    "buffer_load_dwordx4 v[84:87], v16, %[sws], s60 offen offset:3072 sc1\n\t"
    "s_waitcnt vmcnt(11)\n\t"
    "s_waitcnt lgkmcnt(6)\n\t"
    "v_mfma_f32_32x32x16_bf16 v[112:127], v[2:5], v[40:43], v[112:127]\n\t"
    "ds_read_b64_tr_b16 v[88:89], v12 offset:21504\n\t"
    "ds_read_b64_tr_b16 v[90:91], v12 offset:23296\n\t"
    "s_add_u32 s60, s76, 0x4000\n\t"
    "buffer_load_dwordx4 v[176:179], v16, %[sws], s60 offen offset:0 sc1\n\t"
    "s_waitcnt lgkmcnt(6)\n\t"
    "v_mfma_f32_32x32x16_bf16 v[128:143], v[6:9], v[40:43], v[128:143]\n\t"
    "ds_read_b64_tr_b16 v[92:93], v12 offset:21760\n\t"
    "ds_read_b64_tr_b16 v[94:95], v12 offset:23552\n\t"
    "s_waitcnt vmcnt(11)\n\t"
    "s_waitcnt lgkmcnt(6)\n\t"
    "v_mfma_f32_32x32x16_bf16 v[112:127], v[96:99], v[44:47], v[112:127]\n\t"
    "ds_read_b64_tr_b16 v[2:3], v12 offset:25088\n\t"
    "ds_read_b64_tr_b16 v[4:5], v12 offset:26880\n\t"
    "buffer_load_dwordx4 v[180:183], v16, %[sws], s60 offen offset:1024 sc1\n\t"
    "s_waitcnt lgkmcnt(6)\n\t"
    "v_mfma_f32_32x32x16_bf16 v[128:143], v[100:103], v[44:47], v[128:143]\n\t"
    "ds_read_b64_tr_b16 v[6:7], v12 offset:25344\n\t"
    "ds_read_b64_tr_b16 v[8:9], v12 offset:27136\n\t"
    "s_waitcnt vmcnt(11)\n\t"
    "s_waitcnt lgkmcnt(6)\n\t"
    "v_mfma_f32_32x32x16_bf16 v[112:127], v[88:91], v[48:51], v[112:127]\n\t"
    "ds_read_b64_tr_b16 v[96:97], v12 offset:28672\n\t"
    "ds_read_b64_tr_b16 v[98:99], v12 offset:30464\n\t"
    "buffer_load_dwordx4 v[184:187], v16, %[sws], s60 offen offset:2048 sc1\n\t"
    "s_waitcnt lgkmcnt(6)\n\t"
    "v_mfma_f32_32x32x16_bf16 v[128:143], v[92:95], v[48:51], v[128:143]\n\t"
    "ds_read_b64_tr_b16 v[100:101], v12 offset:28928\n\t"
    "ds_read_b64_tr_b16 v[102:103], v12 offset:30720\n\t"
    "s_waitcnt vmcnt(11)\n\t"
    "s_waitcnt lgkmcnt(6)\n\t"
    "v_mfma_f32_32x32x16_bf16 v[112:127], v[2:5], v[52:55], v[112:127]\n\t"
    "ds_read_b64_tr_b16 v[88:89], v12 offset:32256\n\t"
    "ds_read_b64_tr_b16 v[90:91], v12 offset:34048\n\t"
    "buffer_load_dwordx4 v[188:191], v16, %[sws], s60 offen offset:3072 sc1\n\t"
    "s_waitcnt lgkmcnt(6)\n\t"
    "v_mfma_f32_32x32x16_bf16 v[128:143], v[6:9], v[52:55], v[128:143]\n\t"
    "ds_read_b64_tr_b16 v[92:93], v12 offset:32512\n\t"
    "ds_read_b64_tr_b16 v[94:95], v12 offset:34304\n\t"
    "s_waitcnt vmcnt(11)\n\t"
    "s_waitcnt lgkmcnt(6)\n\t"
    "v_mfma_f32_32x32x16_bf16 v[112:127], v[96:99], v[56:59], v[112:127]\n\t"
    "ds_read_b64_tr_b16 v[2:3], v12 offset:35840\n\t"
    "ds_read_b64_tr_b16 v[4:5], v12 offset:37632\n\t"
    "s_add_u32 s60, s76, 0x5000\n\t"
    "buffer_load_dwordx4 v[192:195], v16, %[sws], s60 offen offset:0 sc1\n\t"
    "s_waitcnt lgkmcnt(6)\n\t"
    "v_mfma_f32_32x32x16_bf16 v[128:143], v[100:103], v[56:59], v[128:143]\n\t"
    "ds_read_b64_tr_b16 v[6:7], v12 offset:36096\n\t"
    "ds_read_b64_tr_b16 v[8:9], v12 offset:37888\n\t"
    "s_waitcnt vmcnt(11)\n\t"
    "s_waitcnt lgkmcnt(6)\n\t"
    "v_mfma_f32_32x32x16_bf16 v[112:127], v[88:91], v[60:63], v[112:127]\n\t"
    "ds_read_b64_tr_b16 v[96:97], v12 offset:39424\n\t"
    "ds_read_b64_tr_b16 v[98:99], v12 offset:41216\n\t"
    "buffer_load_dwordx4 v[196:199], v16, %[sws], s60 offen offset:1024 sc1\n\t"
    "s_waitcnt lgkmcnt(6)\n\t"
    "v_mfma_f32_32x32x16_bf16 v[128:143], v[92:95], v[60:63], v[128:143]\n\t"
    "ds_read_b64_tr_b16 v[100:101], v12 offset:39680\n\t"
    "ds_read_b64_tr_b16 v[102:103], v12 offset:41472\n\t"
    "s_waitcnt vmcnt(11)\n\t"
    "s_waitcnt lgkmcnt(6)\n\t"
    "v_mfma_f32_32x32x16_bf16 v[112:127], v[2:5], v[64:67], v[112:127]\n\t"
    "ds_read_b64_tr_b16 v[88:89], v12 offset:43008\n\t"
    "ds_read_b64_tr_b16 v[90:91], v12 offset:44800\n\t"
    "buffer_load_dwordx4 v[200:203], v16, %[sws], s60 offen offset:2048 sc1\n\t"
    "s_waitcnt lgkmcnt(6)\n\t"
    "v_mfma_f32_32x32x16_bf16 v[128:143], v[6:9], v[64:67], v[128:143]\n\t"
    "ds_read_b64_tr_b16 v[92:93], v12 offset:43264\n\t"
    "ds_read_b64_tr_b16 v[94:95], v12 offset:45056\n\t"
    "s_waitcnt vmcnt(11)\n\t"
    "s_waitcnt lgkmcnt(6)\n\t"
    "v_mfma_f32_32x32x16_bf16 v[112:127], v[96:99], v[68:71], v[112:127]\n\t"
    "ds_read_b64_tr_b16 v[2:3], v12 offset:46592\n\t"
    "ds_read_b64_tr_b16 v[4:5], v12 offset:48384\n\t"
    "buffer_load_dwordx4 v[204:207], v16, %[sws], s60 offen offset:3072 sc1\n\t"
    "s_waitcnt lgkmcnt(6)\n\t"
    "v_mfma_f32_32x32x16_bf16 v[128:143], v[100:103], v[68:71], v[128:143]\n\t"
    "ds_read_b64_tr_b16 v[6:7], v12 offset:46848\n\t"
    "ds_read_b64_tr_b16 v[8:9], v12 offset:48640\n\t"
    "s_waitcnt vmcnt(11)\n\t"
    "s_waitcnt lgkmcnt(6)\n\t"
    "v_mfma_f32_32x32x16_bf16 v[112:127], v[88:91], v[72:75], v[112:127]\n\t"
    "ds_read_b64_tr_b16 v[96:97], v12 offset:50176\n\t"
    "ds_read_b64_tr_b16 v[98:99], v12 offset:51968\n\t"
    "s_add_u32 s60, s76, 0x6000\n\t"
    "buffer_load_dwordx4 v[208:211], v16, %[sws], s60 offen offset:0 sc1\n\t"
    "s_waitcnt lgkmcnt(6)\n\t"
    "v_mfma_f32_32x32x16_bf16 v[128:143], v[92:95], v[72:75], v[128:143]\n\t"
    "ds_read_b64_tr_b16 v[100:101], v12 offset:50432\n\t"
    "ds_read_b64_tr_b16 v[102:103], v12 offset:52224\n\t"
    "s_waitcnt vmcnt(11)\n\t"
    "s_waitcnt lgkmcnt(6)\n\t"
    "v_mfma_f32_32x32x16_bf16 v[112:127], v[2:5], v[76:79], v[112:127]\n\t"
    "ds_read_b64_tr_b16 v[88:89], v12 offset:53760\n\t"
    "ds_read_b64_tr_b16 v[90:91], v12 offset:55552\n\t"
    "buffer_load_dwordx4 v[212:215], v16, %[sws], s60 offen offset:1024 sc1\n\t"
    "s_waitcnt lgkmcnt(6)\n\t"
    "v_mfma_f32_32x32x16_bf16 v[128:143], v[6:9], v[76:79], v[128:143]\n\t"
    "ds_read_b64_tr_b16 v[92:93], v12 offset:54016\n\t"
    "ds_read_b64_tr_b16 v[94:95], v12 offset:55808\n\t"
    "s_waitcnt vmcnt(11)\n\t"
    "s_waitcnt lgkmcnt(6)\n\t"
    "v_mfma_f32_32x32x16_bf16 v[112:127], v[96:99], v[80:83], v[112:127]\n\t"
    "ds_read_b64_tr_b16 v[2:3], v13 offset:0\n\t"
    "ds_read_b64_tr_b16 v[4:5], v13 offset:1792\n\t"
    "buffer_load_dwordx4 v[216:219], v16, %[sws], s60 offen offset:2048 sc1\n\t"
    "s_waitcnt lgkmcnt(6)\n\t"
    "v_mfma_f32_32x32x16_bf16 v[128:143], v[100:103], v[80:83], v[128:143]\n\t"
    "ds_read_b64_tr_b16 v[6:7], v13 offset:256\n\t"
    "ds_read_b64_tr_b16 v[8:9], v13 offset:2048\n\t"
    "s_waitcnt vmcnt(11)\n\t"
    "s_waitcnt lgkmcnt(6)\n\t"
    "v_mfma_f32_32x32x16_bf16 v[112:127], v[88:91], v[84:87], v[112:127]\n\t"
    "ds_read_b64_tr_b16 v[96:97], v13 offset:3584\n\t"
    "ds_read_b64_tr_b16 v[98:99], v13 offset:5376\n\t"
    "buffer_load_dwordx4 v[220:223], v16, %[sws], s60 offen offset:3072 sc1\n\t"
    "s_waitcnt lgkmcnt(6)\n\t"
    "v_mfma_f32_32x32x16_bf16 v[128:143], v[92:95], v[84:87], v[128:143]\n\t"
    "ds_read_b64_tr_b16 v[100:101], v13 offset:3840\n\t"
    "ds_read_b64_tr_b16 v[102:103], v13 offset:5632\n\t"
    "s_waitcnt vmcnt(11)\n\t"
    "s_waitcnt lgkmcnt(6)\n\t"
    "v_mfma_f32_32x32x16_bf16 v[112:127], v[2:5], v[176:179], v[112:127]\n\t"
    "ds_read_b64_tr_b16 v[88:89], v13 offset:7168\n\t"
    "ds_read_b64_tr_b16 v[90:91], v13 offset:8960\n\t"
    "s_add_u32 s60, s76, 0x7000\n\t"
    "buffer_load_dwordx4 v[224:227], v16, %[sws], s60 offen offset:0 sc1\n\t"
    "s_waitcnt lgkmcnt(6)\n\t"
    "v_mfma_f32_32x32x16_bf16 v[128:143], v[6:9], v[176:179], v[128:143]\n\t"
    "ds_read_b64_tr_b16 v[92:93], v13 offset:7424\n\t"
    "ds_read_b64_tr_b16 v[94:95], v13 offset:9216\n\t"
    "s_waitcnt vmcnt(11)\n\t"
    "s_waitcnt lgkmcnt(6)\n\t"
    "v_mfma_f32_32x32x16_bf16 v[112:127], v[96:99], v[180:183], v[112:127]\n\t"
    "ds_read_b64_tr_b16 v[2:3], v13 offset:10752\n\t"
    "ds_read_b64_tr_b16 v[4:5], v13 offset:12544\n\t"
    "buffer_load_dwordx4 v[228:231], v16, %[sws], s60 offen offset:1024 sc1\n\t"
    "s_waitcnt lgkmcnt(6)\n\t"
    "v_mfma_f32_32x32x16_bf16 v[128:143], v[100:103], v[180:183], v[128:143]\n\t"
    "ds_read_b64_tr_b16 v[6:7], v13 offset:11008\n\t"
    "ds_read_b64_tr_b16 v[8:9], v13 offset:12800\n\t"
    "s_waitcnt vmcnt(11)\n\t"
    "s_waitcnt lgkmcnt(6)\n\t"
    "v_mfma_f32_32x32x16_bf16 v[112:127], v[88:91], v[184:187], v[112:127]\n\t"
    "ds_read_b64_tr_b16 v[96:97], v13 offset:14336\n\t"
    "ds_read_b64_tr_b16 v[98:99], v13 offset:16128\n\t"
    "buffer_load_dwordx4 v[232:235], v16, %[sws], s60 offen offset:2048 sc1\n\t"
    "s_waitcnt lgkmcnt(6)\n\t"
    "v_mfma_f32_32x32x16_bf16 v[128:143], v[92:95], v[184:187], v[128:143]\n\t"
    "ds_read_b64_tr_b16 v[100:101], v13 offset:14592\n\t"
    "ds_read_b64_tr_b16 v[102:103], v13 offset:16384\n\t"
    "s_waitcnt vmcnt(11)\n\t"
    "s_waitcnt lgkmcnt(6)\n\t"
    "v_mfma_f32_32x32x16_bf16 v[112:127], v[2:5], v[188:191], v[112:127]\n\t"
    "ds_read_b64_tr_b16 v[88:89], v13 offset:17920\n\t"
    "ds_read_b64_tr_b16 v[90:91], v13 offset:19712\n\t"
    "buffer_load_dwordx4 v[236:239], v16, %[sws], s60 offen offset:3072 sc1\n\t"
    "s_waitcnt lgkmcnt(6)\n\t"
    "v_mfma_f32_32x32x16_bf16 v[128:143], v[6:9], v[188:191], v[128:143]\n\t"
    "ds_read_b64_tr_b16 v[92:93], v13 offset:18176\n\t"
    "ds_read_b64_tr_b16 v[94:95], v13 offset:19968\n\t"
    "buffer_load_dword v144, v17, %[sws], s77 offen offset:0 sc1\n\t"
    "buffer_load_dword v145, v17, %[sws], s77 offen offset:256 sc1\n\t"
    "buffer_load_dword v146, v17, %[sws], s77 offen offset:512 sc1\n\t"
    "buffer_load_dword v147, v17, %[sws], s77 offen offset:768 sc1\n\t"
    "s_waitcnt vmcnt(15)\n\t"
    "s_waitcnt lgkmcnt(6)\n\t"
    "v_mfma_f32_32x32x16_bf16 v[112:127], v[96:99], v[192:195], v[112:127]\n\t"
    "ds_read_b64_tr_b16 v[2:3], v13 offset:21504\n\t"
    "ds_read_b64_tr_b16 v[4:5], v13 offset:23296\n\t"
    "ds_read_b128 v[160:163], v22 offset:0\n\t"
    "ds_read_b128 v[164:167], v22 offset:32\n\t"
    "ds_read_b128 v[168:171], v22 offset:64\n\t"
    "ds_read_b128 v[172:175], v22 offset:96\n\t"
    "s_waitcnt lgkmcnt(10)\n\t"
    "v_mfma_f32_32x32x16_bf16 v[128:143], v[100:103], v[192:195], v[128:143]\n\t"
    "ds_read_b64_tr_b16 v[6:7], v13 offset:21760\n\t"
    "ds_read_b64_tr_b16 v[8:9], v13 offset:23552\n\t"
    "buffer_load_dword v148, v17, %[sws], s77 offen offset:1024 sc1\n\t"
    "buffer_load_dword v149, v17, %[sws], s77 offen offset:1280 sc1\n\t"
    "buffer_load_dword v150, v17, %[sws], s77 offen offset:1536 sc1\n\t"
    "buffer_load_dword v151, v17, %[sws], s77 offen offset:1792 sc1\n\t"
    "s_waitcnt vmcnt(18)\n\t"
    "s_waitcnt lgkmcnt(10)\n\t"
    "v_mfma_f32_32x32x16_bf16 v[112:127], v[88:91], v[196:199], v[112:127]\n\t"
    "ds_read_b64_tr_b16 v[96:97], v13 offset:25088\n\t"
    "ds_read_b64_tr_b16 v[98:99], v13 offset:26880\n\t"
    "s_waitcnt lgkmcnt(10)\n\t"
    "v_mfma_f32_32x32x16_bf16 v[128:143], v[92:95], v[196:199], v[128:143]\n\t"
    "ds_read_b64_tr_b16 v[100:101], v13 offset:25344\n\t"
    "ds_read_b64_tr_b16 v[102:103], v13 offset:27136\n\t"
    "buffer_load_dword v152, v17, %[sws], s77 offen offset:2048 sc1\n\t"
    "buffer_load_dword v153, v17, %[sws], s77 offen offset:2304 sc1\n\t"
    "buffer_load_dword v154, v17, %[sws], s77 offen offset:2560 sc1\n\t"
    "buffer_load_dword v155, v17, %[sws], s77 offen offset:2816 sc1\n\t"
    "s_waitcnt vmcnt(21)\n\t"
    "s_waitcnt lgkmcnt(10)\n\t"
    "v_mfma_f32_32x32x16_bf16 v[112:127], v[2:5], v[200:203], v[112:127]\n\t"
    "ds_read_b64_tr_b16 v[88:89], v13 offset:28672\n\t"
    "ds_read_b64_tr_b16 v[90:91], v13 offset:30464\n\t"
    "s_waitcnt lgkmcnt(6)\n\t"
    "v_mfma_f32_32x32x16_bf16 v[128:143], v[6:9], v[200:203], v[128:143]\n\t"
    "ds_read_b64_tr_b16 v[92:93], v13 offset:28928\n\t"
    "ds_read_b64_tr_b16 v[94:95], v13 offset:30720\n\t"
    "buffer_load_dword v156, v17, %[sws], s77 offen offset:3072 sc1\n\t"
    "buffer_load_dword v157, v17, %[sws], s77 offen offset:3328 sc1\n\t"
    "buffer_load_dword v158, v17, %[sws], s77 offen offset:3584 sc1\n\t"
    "buffer_load_dword v159, v17, %[sws], s77 offen offset:3840 sc1\n\t"
    "s_waitcnt vmcnt(24)\n\t"
    "s_waitcnt lgkmcnt(6)\n\t"
    "v_mfma_f32_32x32x16_bf16 v[112:127], v[96:99], v[204:207], v[112:127]\n\t"
    "ds_read_b64_tr_b16 v[2:3], v13 offset:32256\n\t"
    "ds_read_b64_tr_b16 v[4:5], v13 offset:34048\n\t"
    "s_waitcnt lgkmcnt(6)\n\t"
    "v_mfma_f32_32x32x16_bf16 v[128:143], v[100:103], v[204:207], v[128:143]\n\t"
    "ds_read_b64_tr_b16 v[6:7], v13 offset:32512\n\t"
    "ds_read_b64_tr_b16 v[8:9], v13 offset:34304\n\t"
    "s_waitcnt vmcnt(23)\n\t"
    "s_waitcnt lgkmcnt(6)\n\t"
    "v_mfma_f32_32x32x16_bf16 v[112:127], v[88:91], v[208:211], v[112:127]\n\t"
    "ds_read_b64_tr_b16 v[96:97], v13 offset:35840\n\t"
    "ds_read_b64_tr_b16 v[98:99], v13 offset:37632\n\t"
    "s_waitcnt lgkmcnt(6)\n\t"
    "v_mfma_f32_32x32x16_bf16 v[128:143], v[92:95], v[208:211], v[128:143]\n\t"
    "ds_read_b64_tr_b16 v[100:101], v13 offset:36096\n\t"
    "ds_read_b64_tr_b16 v[102:103], v13 offset:37888\n\t"
    "s_waitcnt vmcnt(22)\n\t"
    "s_waitcnt lgkmcnt(6)\n\t"
    "v_mfma_f32_32x32x16_bf16 v[112:127], v[2:5], v[212:215], v[112:127]\n\t"
    "ds_read_b64_tr_b16 v[88:89], v13 offset:39424\n\t"
    "ds_read_b64_tr_b16 v[90:91], v13 offset:41216\n\t"
    "s_waitcnt lgkmcnt(6)\n\t"
    "v_mfma_f32_32x32x16_bf16 v[128:143], v[6:9], v[212:215], v[128:143]\n\t"
    "ds_read_b64_tr_b16 v[92:93], v13 offset:39680\n\t"
    "ds_read_b64_tr_b16 v[94:95], v13 offset:41472\n\t"
    "s_waitcnt vmcnt(21)\n\t"
    "s_waitcnt lgkmcnt(6)\n\t"
    "v_mfma_f32_32x32x16_bf16 v[112:127], v[96:99], v[216:219], v[112:127]\n\t"
    "ds_read_b64_tr_b16 v[2:3], v13 offset:43008\n\t"
    "ds_read_b64_tr_b16 v[4:5], v13 offset:44800\n\t"
    "s_waitcnt lgkmcnt(6)\n\t"
    "v_mfma_f32_32x32x16_bf16 v[128:143], v[100:103], v[216:219], v[128:143]\n\t"
    "ds_read_b64_tr_b16 v[6:7], v13 offset:43264\n\t"
    "ds_read_b64_tr_b16 v[8:9], v13 offset:45056\n\t"
    "s_waitcnt vmcnt(20)\n\t"
    "s_waitcnt lgkmcnt(6)\n\t"
    "v_mfma_f32_32x32x16_bf16 v[112:127], v[88:91], v[220:223], v[112:127]\n\t"
    "ds_read_b64_tr_b16 v[96:97], v13 offset:46592\n\t"
    "ds_read_b64_tr_b16 v[98:99], v13 offset:48384\n\t"
    "s_waitcnt lgkmcnt(6)\n\t"
    "v_mfma_f32_32x32x16_bf16 v[128:143], v[92:95], v[220:223], v[128:143]\n\t"
    "ds_read_b64_tr_b16 v[100:101], v13 offset:46848\n\t"
    "ds_read_b64_tr_b16 v[102:103], v13 offset:48640\n\t"
    "s_waitcnt vmcnt(19)\n\t"
    "s_waitcnt lgkmcnt(6)\n\t"
    "v_mfma_f32_32x32x16_bf16 v[112:127], v[2:5], v[224:227], v[112:127]\n\t"
    "ds_read_b64_tr_b16 v[88:89], v13 offset:50176\n\t"
    "ds_read_b64_tr_b16 v[90:91], v13 offset:51968\n\t"
    "s_waitcnt lgkmcnt(6)\n\t"
    "v_mfma_f32_32x32x16_bf16 v[128:143], v[6:9], v[224:227], v[128:143]\n\t"
    "ds_read_b64_tr_b16 v[92:93], v13 offset:50432\n\t"
    "ds_read_b64_tr_b16 v[94:95], v13 offset:52224\n\t"
    "s_waitcnt vmcnt(18)\n\t"
    "s_waitcnt lgkmcnt(6)\n\t"
    "v_mfma_f32_32x32x16_bf16 v[112:127], v[96:99], v[228:231], v[112:127]\n\t"
    "ds_read_b64_tr_b16 v[2:3], v13 offset:53760\n\t"
    "ds_read_b64_tr_b16 v[4:5], v13 offset:55552\n\t"
    "s_waitcnt lgkmcnt(6)\n\t"
    "v_mfma_f32_32x32x16_bf16 v[128:143], v[100:103], v[228:231], v[128:143]\n\t"
    "ds_read_b64_tr_b16 v[6:7], v13 offset:54016\n\t"
    "ds_read_b64_tr_b16 v[8:9], v13 offset:55808\n\t"
    "s_waitcnt vmcnt(17)\n\t"
    "s_waitcnt vmcnt(0)\n\t"
    "v_pk_add_f32 v[144:145], v[144:145], v[152:153]\n\t"
    "v_pk_add_f32 v[146:147], v[146:147], v[154:155]\n\t"
    "v_pk_add_f32 v[148:149], v[148:149], v[156:157]\n\t"
    "v_pk_add_f32 v[150:151], v[150:151], v[158:159]\n\t"
    "v_pk_add_f32 v[144:145], v[144:145], v[146:147]\n\t"
    "v_pk_add_f32 v[148:149], v[148:149], v[150:151]\n\t"
    "v_pk_add_f32 v[144:145], v[144:145], v[148:149]\n\t"
    "v_add_f32 v144, v144, v145\n\t"
    "v_mul_f32 v106, 0x3b000000, v144\n\t"
    "v_mov_b32 v104, v106\n\t"
    "s_nop 1\n\t"
    "v_permlane32_swap_b32 v106, v104\n\t"
    "s_add_u32 s61, s78, 0x1000\n\t"
    "v_fma_f32 v104, -v106, v106, v104\n\t"
    "v_max_f32 v104, 0, v104\n\t"
    "v_add_f32 v104, 0x3727c5ac, v104\n\t"
    "v_rsq_f32 v104, v104\n\t"
    "v_xor_b32 v106, 0x80000000, v106\n\t"
    "s_waitcnt lgkmcnt(6)\n\t"
    "v_mfma_f32_32x32x16_bf16 v[112:127], v[88:91], v[232:235], v[112:127]\n\t"
    "ds_read_b64_tr_b16 v[96:97], v12 offset:512\n\t"
    "ds_read_b64_tr_b16 v[98:99], v12 offset:2304\n\t"
    "ds_read_b128 v[144:147], v22 offset:12288\n\t"
    "ds_read_b128 v[148:151], v22 offset:12320\n\t"
    "ds_read_b128 v[152:155], v22 offset:12352\n\t"
    "ds_read_b128 v[156:159], v22 offset:12384\n\t"
    "s_waitcnt lgkmcnt(10)\n\t"
    "v_mfma_f32_32x32x16_bf16 v[128:143], v[92:95], v[232:235], v[128:143]\n\t"
    "ds_read_b64_tr_b16 v[100:101], v14 offset:0\n\t"
    "ds_read_b64_tr_b16 v[102:103], v14 offset:1792\n\t"
    "s_waitcnt lgkmcnt(10)\n\t"
    "v_mfma_f32_32x32x16_bf16 v[112:127], v[2:5], v[236:239], v[112:127]\n\t"
    "ds_read_b64_tr_b16 v[88:89], v12 offset:4096\n\t"
    "ds_read_b64_tr_b16 v[90:91], v12 offset:5888\n\t"
    "s_waitcnt lgkmcnt(10)\n\t"
    "v_mfma_f32_32x32x16_bf16 v[128:143], v[6:9], v[236:239], v[128:143]\n\t"
    "ds_read_b64_tr_b16 v[92:93], v14 offset:3584\n\t"
    "ds_read_b64_tr_b16 v[94:95], v14 offset:5376\n\t"
    ASTAMP(27)
    "s_nop 7\n\t"
    "v_pk_fma_f32 v[112:113], v[106:107], v[160:161], v[112:113] op_sel_hi:[0,1,1]\n\t"
    "v_pk_fma_f32 v[114:115], v[106:107], v[162:163], v[114:115] op_sel_hi:[0,1,1]\n\t"
    "v_pk_fma_f32 v[116:117], v[106:107], v[164:165], v[116:117] op_sel_hi:[0,1,1]\n\t"
    "v_pk_fma_f32 v[118:119], v[106:107], v[166:167], v[118:119] op_sel_hi:[0,1,1]\n\t"
    "v_pk_fma_f32 v[120:121], v[106:107], v[168:169], v[120:121] op_sel_hi:[0,1,1]\n\t"
    "v_pk_fma_f32 v[122:123], v[106:107], v[170:171], v[122:123] op_sel_hi:[0,1,1]\n\t"
    "v_pk_fma_f32 v[124:125], v[106:107], v[172:173], v[124:125] op_sel_hi:[0,1,1]\n\t"
    "v_pk_fma_f32 v[126:127], v[106:107], v[174:175], v[126:127] op_sel_hi:[0,1,1]\n\t"
    "v_pk_mul_f32 v[112:113], v[104:105], v[112:113] op_sel_hi:[0,1]\n\t"
    "v_pk_mul_f32 v[114:115], v[104:105], v[114:115] op_sel_hi:[0,1]\n\t"
    "v_pk_mul_f32 v[116:117], v[104:105], v[116:117] op_sel_hi:[0,1]\n\t"
    "v_pk_mul_f32 v[118:119], v[104:105], v[118:119] op_sel_hi:[0,1]\n\t"
    "v_pk_mul_f32 v[120:121], v[104:105], v[120:121] op_sel_hi:[0,1]\n\t"
    "v_pk_mul_f32 v[122:123], v[104:105], v[122:123] op_sel_hi:[0,1]\n\t"
    "v_pk_mul_f32 v[124:125], v[104:105], v[124:125] op_sel_hi:[0,1]\n\t"
    "v_pk_mul_f32 v[126:127], v[104:105], v[126:127] op_sel_hi:[0,1]\n\t"
    "v_cvt_pk_bf16_f32 v112, v112, v113\n\t"
    "v_cvt_pk_bf16_f32 v113, v114, v115\n\t"
    "v_cvt_pk_bf16_f32 v114, v116, v117\n\t"
    "v_cvt_pk_bf16_f32 v115, v118, v119\n\t"
    "v_cvt_pk_bf16_f32 v116, v120, v121\n\t"
    "v_cvt_pk_bf16_f32 v117, v122, v123\n\t"
    "v_cvt_pk_bf16_f32 v118, v124, v125\n\t"
    "v_cvt_pk_bf16_f32 v119, v126, v127\n\t"
    "v_pk_max_i16 v112, v112, 0\n\t"
    "v_pk_max_i16 v113, v113, 0\n\t"
    "v_pk_max_i16 v114, v114, 0\n\t"
    "v_pk_max_i16 v115, v115, 0\n\t"
    "v_pk_max_i16 v116, v116, 0\n\t"
    "v_pk_max_i16 v117, v117, 0\n\t"
    "v_pk_max_i16 v118, v118, 0\n\t"
    "v_pk_max_i16 v119, v119, 0\n\t"
    "buffer_store_dwordx4 v[112:115], v16, %[sws], s78 offen offset:0 sc1\n\t"
    "buffer_store_dwordx4 v[116:119], v16, %[sws], s78 offen offset:1024 sc1\n\t"
    "s_waitcnt lgkmcnt(6)\n\t"
    "v_pk_fma_f32 v[128:129], v[106:107], v[144:145], v[128:129] op_sel_hi:[0,1,1]\n\t"
    "v_pk_fma_f32 v[130:131], v[106:107], v[146:147], v[130:131] op_sel_hi:[0,1,1]\n\t"
    "v_pk_fma_f32 v[132:133], v[106:107], v[148:149], v[132:133] op_sel_hi:[0,1,1]\n\t"
    "v_pk_fma_f32 v[134:135], v[106:107], v[150:151], v[134:135] op_sel_hi:[0,1,1]\n\t"
    "v_pk_fma_f32 v[136:137], v[106:107], v[152:153], v[136:137] op_sel_hi:[0,1,1]\n\t"
    "v_pk_fma_f32 v[138:139], v[106:107], v[154:155], v[138:139] op_sel_hi:[0,1,1]\n\t"
    "v_pk_fma_f32 v[140:141], v[106:107], v[156:157], v[140:141] op_sel_hi:[0,1,1]\n\t"
    "v_pk_fma_f32 v[142:143], v[106:107], v[158:159], v[142:143] op_sel_hi:[0,1,1]\n\t"
    "v_pk_mul_f32 v[128:129], v[104:105], v[128:129] op_sel_hi:[0,1]\n\t"
    "v_pk_mul_f32 v[130:131], v[104:105], v[130:131] op_sel_hi:[0,1]\n\t"
    "v_pk_mul_f32 v[132:133], v[104:105], v[132:133] op_sel_hi:[0,1]\n\t"
    "v_pk_mul_f32 v[134:135], v[104:105], v[134:135] op_sel_hi:[0,1]\n\t"
    "v_pk_mul_f32 v[136:137], v[104:105], v[136:137] op_sel_hi:[0,1]\n\t"
    "v_pk_mul_f32 v[138:139], v[104:105], v[138:139] op_sel_hi:[0,1]\n\t"
    "v_pk_mul_f32 v[140:141], v[104:105], v[140:141] op_sel_hi:[0,1]\n\t"
    "v_pk_mul_f32 v[142:143], v[104:105], v[142:143] op_sel_hi:[0,1]\n\t"
    "v_cvt_pk_bf16_f32 v128, v128, v129\n\t"
    "v_cvt_pk_bf16_f32 v129, v130, v131\n\t"
    "v_cvt_pk_bf16_f32 v130, v132, v133\n\t"
    "v_cvt_pk_bf16_f32 v131, v134, v135\n\t"
    "v_cvt_pk_bf16_f32 v132, v136, v137\n\t"
    "v_cvt_pk_bf16_f32 v133, v138, v139\n\t"
    "v_cvt_pk_bf16_f32 v134, v140, v141\n\t"
    "v_cvt_pk_bf16_f32 v135, v142, v143\n\t"
    "v_pk_max_i16 v128, v128, 0\n\t"
    "v_pk_max_i16 v129, v129, 0\n\t"
    "v_pk_max_i16 v130, v130, 0\n\t"
    "v_pk_max_i16 v131, v131, 0\n\t"
    "v_pk_max_i16 v132, v132, 0\n\t"
    "v_pk_max_i16 v133, v133, 0\n\t"
    "v_pk_max_i16 v134, v134, 0\n\t"
    "v_pk_max_i16 v135, v135, 0\n\t"
    "buffer_store_dwordx4 v[128:131], v16, %[sws], s78 offen offset:2048 sc1\n\t"
    "buffer_store_dwordx4 v[132:135], v16, %[sws], s78 offen offset:3072 sc1\n\t"
    "v_mfma_f32_32x32x16_bf16 v[144:159], v[96:99], v[24:27], 0\n\t"
    "ds_read_b64_tr_b16 v[2:3], v12 offset:7680\n\t"
    "ds_read_b64_tr_b16 v[4:5], v12 offset:9472\n\t"
    "s_waitcnt lgkmcnt(6)\n\t"
    "v_mfma_f32_32x32x16_bf16 v[160:175], v[100:103], v[24:27], 0\n\t"
    "ds_read_b64_tr_b16 v[6:7], v14 offset:7168\n\t"
    "ds_read_b64_tr_b16 v[8:9], v14 offset:8960\n\t"
    "s_waitcnt lgkmcnt(6)\n\t"
    "v_mfma_f32_32x32x16_bf16 v[144:159], v[88:91], v[28:31], v[144:159]\n\t"
    "ds_read_b64_tr_b16 v[96:97], v12 offset:11264\n\t"
    "ds_read_b64_tr_b16 v[98:99], v12 offset:13056\n\t"
    "s_waitcnt lgkmcnt(6)\n\t"
    "v_mfma_f32_32x32x16_bf16 v[160:175], v[92:95], v[28:31], v[160:175]\n\t"
    "ds_read_b64_tr_b16 v[100:101], v14 offset:10752\n\t"
    "ds_read_b64_tr_b16 v[102:103], v14 offset:12544\n\t"
    "s_waitcnt lgkmcnt(6)\n\t"
    "v_mfma_f32_32x32x16_bf16 v[144:159], v[2:5], v[32:35], v[144:159]\n\t"
    "ds_read_b64_tr_b16 v[88:89], v12 offset:14848\n\t"
    "ds_read_b64_tr_b16 v[90:91], v12 offset:16640\n\t"
    "s_waitcnt lgkmcnt(6)\n\t"
    "v_mfma_f32_32x32x16_bf16 v[160:175], v[6:9], v[32:35], v[160:175]\n\t"
    "ds_read_b64_tr_b16 v[92:93], v14 offset:14336\n\t"
    "ds_read_b64_tr_b16 v[94:95], v14 offset:16128\n\t"
    "s_waitcnt lgkmcnt(6)\n\t"
    "v_mfma_f32_32x32x16_bf16 v[144:159], v[96:99], v[36:39], v[144:159]\n\t"
    "ds_read_b64_tr_b16 v[2:3], v12 offset:18432\n\t"
    "ds_read_b64_tr_b16 v[4:5], v12 offset:20224\n\t"
    "s_waitcnt lgkmcnt(6)\n\t"
    "v_mfma_f32_32x32x16_bf16 v[160:175], v[100:103], v[36:39], v[160:175]\n\t"
    "ds_read_b64_tr_b16 v[6:7], v14 offset:17920\n\t"
    "ds_read_b64_tr_b16 v[8:9], v14 offset:19712\n\t"
    "s_waitcnt lgkmcnt(6)\n\t"
    "v_mfma_f32_32x32x16_bf16 v[144:159], v[88:91], v[40:43], v[144:159]\n\t"
    "ds_read_b64_tr_b16 v[96:97], v12 offset:22016\n\t"
    "ds_read_b64_tr_b16 v[98:99], v12 offset:23808\n\t"
    "s_waitcnt lgkmcnt(6)\n\t"
    "v_mfma_f32_32x32x16_bf16 v[160:175], v[92:95], v[40:43], v[160:175]\n\t"
    "ds_read_b64_tr_b16 v[100:101], v14 offset:21504\n\t"
    "ds_read_b64_tr_b16 v[102:103], v14 offset:23296\n\t"
    "s_waitcnt lgkmcnt(6)\n\t"
    "v_mfma_f32_32x32x16_bf16 v[144:159], v[2:5], v[44:47], v[144:159]\n\t"
    "ds_read_b64_tr_b16 v[88:89], v12 offset:25600\n\t"
    "ds_read_b64_tr_b16 v[90:91], v12 offset:27392\n\t"
    "s_waitcnt lgkmcnt(6)\n\t"
    "v_mfma_f32_32x32x16_bf16 v[160:175], v[6:9], v[44:47], v[160:175]\n\t"
    "ds_read_b64_tr_b16 v[92:93], v14 offset:25088\n\t"
    "ds_read_b64_tr_b16 v[94:95], v14 offset:26880\n\t"
    "s_waitcnt lgkmcnt(6)\n\t"
    "v_mfma_f32_32x32x16_bf16 v[144:159], v[96:99], v[48:51], v[144:159]\n\t"
    "ds_read_b64_tr_b16 v[2:3], v12 offset:29184\n\t"
    "ds_read_b64_tr_b16 v[4:5], v12 offset:30976\n\t"
    "s_waitcnt lgkmcnt(6)\n\t"
    "v_mfma_f32_32x32x16_bf16 v[160:175], v[100:103], v[48:51], v[160:175]\n\t"
    "ds_read_b64_tr_b16 v[6:7], v14 offset:28672\n\t"
    "ds_read_b64_tr_b16 v[8:9], v14 offset:30464\n\t"
    "s_waitcnt lgkmcnt(6)\n\t"
    "v_mfma_f32_32x32x16_bf16 v[144:159], v[88:91], v[52:55], v[144:159]\n\t"
    "ds_read_b64_tr_b16 v[96:97], v12 offset:32768\n\t"
    "ds_read_b64_tr_b16 v[98:99], v12 offset:34560\n\t"
    "s_waitcnt lgkmcnt(6)\n\t"
    "v_mfma_f32_32x32x16_bf16 v[160:175], v[92:95], v[52:55], v[160:175]\n\t"
    "ds_read_b64_tr_b16 v[100:101], v14 offset:32256\n\t"
    "ds_read_b64_tr_b16 v[102:103], v14 offset:34048\n\t"
    "s_waitcnt lgkmcnt(6)\n\t"
    "v_mfma_f32_32x32x16_bf16 v[144:159], v[2:5], v[56:59], v[144:159]\n\t"
    "ds_read_b64_tr_b16 v[88:89], v12 offset:36352\n\t"
    "ds_read_b64_tr_b16 v[90:91], v12 offset:38144\n\t"
    "s_waitcnt lgkmcnt(6)\n\t"
    "v_mfma_f32_32x32x16_bf16 v[160:175], v[6:9], v[56:59], v[160:175]\n\t"
    "ds_read_b64_tr_b16 v[92:93], v14 offset:35840\n\t"
    "ds_read_b64_tr_b16 v[94:95], v14 offset:37632\n\t"
    "s_waitcnt lgkmcnt(6)\n\t"
    "v_mfma_f32_32x32x16_bf16 v[144:159], v[96:99], v[60:63], v[144:159]\n\t"
    "ds_read_b64_tr_b16 v[2:3], v12 offset:39936\n\t"
    "ds_read_b64_tr_b16 v[4:5], v12 offset:41728\n\t"
    "s_waitcnt lgkmcnt(6)\n\t"
    "v_mfma_f32_32x32x16_bf16 v[160:175], v[100:103], v[60:63], v[160:175]\n\t"
    "ds_read_b64_tr_b16 v[6:7], v14 offset:39424\n\t"
    "ds_read_b64_tr_b16 v[8:9], v14 offset:41216\n\t"
    "s_waitcnt lgkmcnt(6)\n\t"
    "v_mfma_f32_32x32x16_bf16 v[144:159], v[88:91], v[64:67], v[144:159]\n\t"
    "ds_read_b64_tr_b16 v[96:97], v12 offset:43520\n\t"
    "ds_read_b64_tr_b16 v[98:99], v12 offset:45312\n\t"
    "s_waitcnt lgkmcnt(6)\n\t"
    "v_mfma_f32_32x32x16_bf16 v[160:175], v[92:95], v[64:67], v[160:175]\n\t"
    "ds_read_b64_tr_b16 v[100:101], v14 offset:43008\n\t"
    "ds_read_b64_tr_b16 v[102:103], v14 offset:44800\n\t"
    "s_waitcnt lgkmcnt(6)\n\t"
    "v_mfma_f32_32x32x16_bf16 v[144:159], v[2:5], v[68:71], v[144:159]\n\t"
    "ds_read_b64_tr_b16 v[88:89], v12 offset:47104\n\t"
    "ds_read_b64_tr_b16 v[90:91], v12 offset:48896\n\t"
    "s_waitcnt vmcnt(0)\n\t"
    "s_mov_b64 exec, 1\n\t"
    "buffer_store_dwordx2 v[10:11], off, %[sws], s79 sc1\n\t"
    "s_mov_b64 exec, -1\n\t"
    ASTAMP(20)
    "s_waitcnt lgkmcnt(6)\n\t"
    "v_mfma_f32_32x32x16_bf16 v[160:175], v[6:9], v[68:71], v[160:175]\n\t"
    "ds_read_b64_tr_b16 v[92:93], v14 offset:46592\n\t"
    "ds_read_b64_tr_b16 v[94:95], v14 offset:48384\n\t"
    "s_waitcnt lgkmcnt(6)\n\t"
    "v_mfma_f32_32x32x16_bf16 v[144:159], v[96:99], v[72:75], v[144:159]\n\t"
    "ds_read_b64_tr_b16 v[2:3], v12 offset:50688\n\t"
    "ds_read_b64_tr_b16 v[4:5], v12 offset:52480\n\t"
    "v_lshlrev_b32 v112, 16, v240\n\t"
    "s_waitcnt lgkmcnt(6)\n\t"
    "v_mfma_f32_32x32x16_bf16 v[160:175], v[100:103], v[72:75], v[160:175]\n\t"
    "ds_read_b64_tr_b16 v[6:7], v14 offset:50176\n\t"
    "ds_read_b64_tr_b16 v[8:9], v14 offset:51968\n\t"
    "v_and_b32 v113, 0xffff0000, v240\n\t"
    "s_waitcnt lgkmcnt(6)\n\t"
    "v_mfma_f32_32x32x16_bf16 v[144:159], v[88:91], v[76:79], v[144:159]\n\t"
    "ds_read_b64_tr_b16 v[96:97], v12 offset:54272\n\t"
    "ds_read_b64_tr_b16 v[98:99], v12 offset:56064\n\t"
    "v_lshlrev_b32 v114, 16, v241\n\t"
    "s_waitcnt lgkmcnt(6)\n\t"
    "v_mfma_f32_32x32x16_bf16 v[160:175], v[92:95], v[76:79], v[160:175]\n\t"
    "ds_read_b64_tr_b16 v[100:101], v14 offset:53760\n\t"
    "ds_read_b64_tr_b16 v[102:103], v14 offset:55552\n\t"
    "v_and_b32 v115, 0xffff0000, v241\n\t"
    "s_waitcnt lgkmcnt(6)\n\t"
    "v_mfma_f32_32x32x16_bf16 v[144:159], v[2:5], v[80:83], v[144:159]\n\t"
    "ds_read_b64_tr_b16 v[88:89], v13 offset:512\n\t"
    "ds_read_b64_tr_b16 v[90:91], v13 offset:2304\n\t"
    "v_lshlrev_b32 v116, 16, v242\n\t"
    "s_waitcnt lgkmcnt(6)\n\t"
    "v_mfma_f32_32x32x16_bf16 v[160:175], v[6:9], v[80:83], v[160:175]\n\t"
    "ds_read_b64_tr_b16 v[92:93], v15 offset:0\n\t"
    "ds_read_b64_tr_b16 v[94:95], v15 offset:1792\n\t"
    "v_and_b32 v117, 0xffff0000, v242\n\t"
    "s_waitcnt lgkmcnt(6)\n\t"
    "v_mfma_f32_32x32x16_bf16 v[144:159], v[96:99], v[84:87], v[144:159]\n\t"
    "ds_read_b64_tr_b16 v[2:3], v13 offset:4096\n\t"
    "ds_read_b64_tr_b16 v[4:5], v13 offset:5888\n\t"
    "v_lshlrev_b32 v118, 16, v243\n\t"
    "s_waitcnt lgkmcnt(6)\n\t"
    "v_mfma_f32_32x32x16_bf16 v[160:175], v[100:103], v[84:87], v[160:175]\n\t"
    "ds_read_b64_tr_b16 v[6:7], v15 offset:3584\n\t"
    "ds_read_b64_tr_b16 v[8:9], v15 offset:5376\n\t"
    "v_and_b32 v119, 0xffff0000, v243\n\t"
    "s_waitcnt lgkmcnt(6)\n\t"
    "v_mfma_f32_32x32x16_bf16 v[144:159], v[88:91], v[176:179], v[144:159]\n\t"
    "ds_read_b64_tr_b16 v[96:97], v13 offset:7680\n\t"
    "ds_read_b64_tr_b16 v[98:99], v13 offset:9472\n\t"
    "v_lshlrev_b32 v120, 16, v244\n\t"
    "s_waitcnt lgkmcnt(6)\n\t"
    "v_mfma_f32_32x32x16_bf16 v[160:175], v[92:95], v[176:179], v[160:175]\n\t"
    "ds_read_b64_tr_b16 v[100:101], v15 offset:7168\n\t"
    "ds_read_b64_tr_b16 v[102:103], v15 offset:8960\n\t"
    "v_and_b32 v121, 0xffff0000, v244\n\t"
    "s_waitcnt lgkmcnt(6)\n\t"
    "v_mfma_f32_32x32x16_bf16 v[144:159], v[2:5], v[180:183], v[144:159]\n\t"
    "ds_read_b64_tr_b16 v[88:89], v13 offset:11264\n\t"
    "ds_read_b64_tr_b16 v[90:91], v13 offset:13056\n\t"
    "v_lshlrev_b32 v122, 16, v245\n\t"
    "s_waitcnt lgkmcnt(6)\n\t"
    "v_mfma_f32_32x32x16_bf16 v[160:175], v[6:9], v[180:183], v[160:175]\n\t"
    "ds_read_b64_tr_b16 v[92:93], v15 offset:10752\n\t"
    "ds_read_b64_tr_b16 v[94:95], v15 offset:12544\n\t"
    "v_and_b32 v123, 0xffff0000, v245\n\t"
    "s_waitcnt lgkmcnt(6)\n\t"
    "v_mfma_f32_32x32x16_bf16 v[144:159], v[96:99], v[184:187], v[144:159]\n\t"
    "ds_read_b64_tr_b16 v[2:3], v13 offset:14848\n\t"
    "ds_read_b64_tr_b16 v[4:5], v13 offset:16640\n\t"
    "v_lshlrev_b32 v124, 16, v246\n\t"
    "s_waitcnt lgkmcnt(6)\n\t"
    "v_mfma_f32_32x32x16_bf16 v[160:175], v[100:103], v[184:187], v[160:175]\n\t"
    "ds_read_b64_tr_b16 v[6:7], v15 offset:14336\n\t"
    "ds_read_b64_tr_b16 v[8:9], v15 offset:16128\n\t"
    "v_and_b32 v125, 0xffff0000, v246\n\t"
    "s_waitcnt lgkmcnt(6)\n\t"
    "v_mfma_f32_32x32x16_bf16 v[144:159], v[88:91], v[188:191], v[144:159]\n\t"
    "ds_read_b64_tr_b16 v[96:97], v13 offset:18432\n\t"
    "ds_read_b64_tr_b16 v[98:99], v13 offset:20224\n\t"
    "v_lshlrev_b32 v126, 16, v247\n\t"
    "s_waitcnt lgkmcnt(6)\n\t"
    "v_mfma_f32_32x32x16_bf16 v[160:175], v[92:95], v[188:191], v[160:175]\n\t"
    "ds_read_b64_tr_b16 v[100:101], v15 offset:17920\n\t"
    "ds_read_b64_tr_b16 v[102:103], v15 offset:19712\n\t"
    "v_and_b32 v127, 0xffff0000, v247\n\t"
    "s_waitcnt lgkmcnt(6)\n\t"
    "v_mfma_f32_32x32x16_bf16 v[144:159], v[2:5], v[192:195], v[144:159]\n\t"
    "ds_read_b64_tr_b16 v[88:89], v13 offset:22016\n\t"
    "ds_read_b64_tr_b16 v[90:91], v13 offset:23808\n\t"
    "v_add_u32 v105, s84, v16\n\t"
    "s_waitcnt lgkmcnt(6)\n\t"
    "v_mfma_f32_32x32x16_bf16 v[160:175], v[6:9], v[192:195], v[160:175]\n\t"
    "ds_read_b64_tr_b16 v[92:93], v15 offset:21504\n\t"
    "ds_read_b64_tr_b16 v[94:95], v15 offset:23296\n\t"
    "v_add_u32 v107, s85, v16\n\t"
    "s_waitcnt lgkmcnt(6)\n\t"
    "v_mfma_f32_32x32x16_bf16 v[144:159], v[96:99], v[196:199], v[144:159]\n\t"
    "ds_read_b64_tr_b16 v[2:3], v13 offset:25600\n\t"
    "ds_read_b64_tr_b16 v[4:5], v13 offset:27392\n\t"
    "v_add_u32 v109, s86, v16\n\t"
    "s_waitcnt lgkmcnt(6)\n\t"
    "v_mfma_f32_32x32x16_bf16 v[160:175], v[100:103], v[196:199], v[160:175]\n\t"
    "ds_read_b64_tr_b16 v[6:7], v15 offset:25088\n\t"
    "ds_read_b64_tr_b16 v[8:9], v15 offset:26880\n\t"
    "v_bfe_u32 v250, v16, 4, 2\n\t"
    "s_waitcnt lgkmcnt(6)\n\t"
    "v_mfma_f32_32x32x16_bf16 v[144:159], v[88:91], v[200:203], v[144:159]\n\t"
    "ds_read_b64_tr_b16 v[96:97], v13 offset:29184\n\t"
    "ds_read_b64_tr_b16 v[98:99], v13 offset:30976\n\t"
    "v_bfe_u32 v108, v16, 6, 1\n\t"
    "s_waitcnt lgkmcnt(6)\n\t"
    "v_mfma_f32_32x32x16_bf16 v[160:175], v[92:95], v[200:203], v[160:175]\n\t"
    "ds_read_b64_tr_b16 v[100:101], v15 offset:28672\n\t"
    "ds_read_b64_tr_b16 v[102:103], v15 offset:30464\n\t"
    "v_lshlrev_b32 v108, 11, v108\n\t"
    "s_waitcnt lgkmcnt(6)\n\t"
    "v_mfma_f32_32x32x16_bf16 v[144:159], v[2:5], v[204:207], v[144:159]\n\t"
    "ds_read_b64_tr_b16 v[88:89], v13 offset:32768\n\t"
    "ds_read_b64_tr_b16 v[90:91], v13 offset:34560\n\t"
    "v_lshl_add_u32 v108, v250, 7, v108\n\t"
    "s_waitcnt lgkmcnt(6)\n\t"
    "v_mfma_f32_32x32x16_bf16 v[160:175], v[6:9], v[204:207], v[160:175]\n\t"
    "ds_read_b64_tr_b16 v[92:93], v15 offset:32256\n\t"
    "ds_read_b64_tr_b16 v[94:95], v15 offset:34048\n\t"
    "s_waitcnt lgkmcnt(6)\n\t"
    "v_mfma_f32_32x32x16_bf16 v[144:159], v[96:99], v[208:211], v[144:159]\n\t"
    "ds_read_b64_tr_b16 v[2:3], v13 offset:36352\n\t"
    "ds_read_b64_tr_b16 v[4:5], v13 offset:38144\n\t"
    "s_waitcnt lgkmcnt(6)\n\t"
    "v_mfma_f32_32x32x16_bf16 v[160:175], v[100:103], v[208:211], v[160:175]\n\t"
    "ds_read_b64_tr_b16 v[6:7], v15 offset:35840\n\t"
    "ds_read_b64_tr_b16 v[8:9], v15 offset:37632\n\t"
    "s_waitcnt lgkmcnt(6)\n\t"
    "v_mfma_f32_32x32x16_bf16 v[144:159], v[88:91], v[212:215], v[144:159]\n\t"
    "ds_read_b64_tr_b16 v[96:97], v13 offset:39936\n\t"
    "ds_read_b64_tr_b16 v[98:99], v13 offset:41728\n\t"
    "s_waitcnt lgkmcnt(6)\n\t"
    "v_mfma_f32_32x32x16_bf16 v[160:175], v[92:95], v[212:215], v[160:175]\n\t"
    "ds_read_b64_tr_b16 v[100:101], v15 offset:39424\n\t"
    "ds_read_b64_tr_b16 v[102:103], v15 offset:41216\n\t"
    "s_waitcnt lgkmcnt(6)\n\t"
    "v_mfma_f32_32x32x16_bf16 v[144:159], v[2:5], v[216:219], v[144:159]\n\t"
    "ds_read_b64_tr_b16 v[88:89], v13 offset:43520\n\t"
    "ds_read_b64_tr_b16 v[90:91], v13 offset:45312\n\t"
    "s_waitcnt lgkmcnt(6)\n\t"
    "v_mfma_f32_32x32x16_bf16 v[160:175], v[6:9], v[216:219], v[160:175]\n\t"
    "ds_read_b64_tr_b16 v[92:93], v15 offset:43008\n\t"
    "ds_read_b64_tr_b16 v[94:95], v15 offset:44800\n\t"
    "s_waitcnt lgkmcnt(6)\n\t"
    "v_mfma_f32_32x32x16_bf16 v[144:159], v[96:99], v[220:223], v[144:159]\n\t"
    "ds_read_b64_tr_b16 v[2:3], v13 offset:47104\n\t"
    "ds_read_b64_tr_b16 v[4:5], v13 offset:48896\n\t"
    "s_waitcnt lgkmcnt(6)\n\t"
    "v_mfma_f32_32x32x16_bf16 v[160:175], v[100:103], v[220:223], v[160:175]\n\t"
    "ds_read_b64_tr_b16 v[6:7], v15 offset:46592\n\t"
    "ds_read_b64_tr_b16 v[8:9], v15 offset:48384\n\t"
    "s_waitcnt lgkmcnt(6)\n\t"
    "v_mfma_f32_32x32x16_bf16 v[144:159], v[88:91], v[224:227], v[144:159]\n\t"
    "ds_read_b64_tr_b16 v[96:97], v13 offset:50688\n\t"
    "ds_read_b64_tr_b16 v[98:99], v13 offset:52480\n\t"
    "s_waitcnt lgkmcnt(6)\n\t"
    "v_mfma_f32_32x32x16_bf16 v[160:175], v[92:95], v[224:227], v[160:175]\n\t"
    "ds_read_b64_tr_b16 v[100:101], v15 offset:50176\n\t"
    "ds_read_b64_tr_b16 v[102:103], v15 offset:51968\n\t"
    "s_waitcnt lgkmcnt(6)\n\t"
    "v_mfma_f32_32x32x16_bf16 v[144:159], v[2:5], v[228:231], v[144:159]\n\t"
    "ds_read_b64_tr_b16 v[88:89], v13 offset:54272\n\t"
    "ds_read_b64_tr_b16 v[90:91], v13 offset:56064\n\t"
    "s_waitcnt lgkmcnt(6)\n\t"
    "v_mfma_f32_32x32x16_bf16 v[160:175], v[6:9], v[228:231], v[160:175]\n\t"
    "ds_read_b64_tr_b16 v[92:93], v15 offset:53760\n\t"
    "ds_read_b64_tr_b16 v[94:95], v15 offset:55552\n\t"
    "ds_read_b128 v[24:27], v22 offset:24576\n\t"
    "ds_read_b128 v[28:31], v22 offset:24608\n\t"
    "ds_read_b128 v[32:35], v22 offset:24640\n\t"
    "ds_read_b128 v[36:39], v22 offset:24672\n\t"
    "s_waitcnt lgkmcnt(10)\n\t"
    "v_mfma_f32_32x32x16_bf16 v[144:159], v[96:99], v[232:235], v[144:159]\n\t"
    "s_waitcnt lgkmcnt(8)\n\t"
    "v_mfma_f32_32x32x16_bf16 v[160:175], v[100:103], v[232:235], v[160:175]\n\t"
    "ds_read_b128 v[40:43], v22 offset:36864\n\t"
    "ds_read_b128 v[44:47], v22 offset:36896\n\t"
    "s_waitcnt lgkmcnt(8)\n\t"
    "v_mfma_f32_32x32x16_bf16 v[144:159], v[88:91], v[236:239], v[144:159]\n\t"
    "s_waitcnt lgkmcnt(6)\n\t"
    "v_mfma_f32_32x32x16_bf16 v[160:175], v[92:95], v[236:239], v[160:175]\n\t"
    "s_add_u32 s62, s80, 0x20000\n\t"
    "buffer_load_dwordx2 v[248:249], v108, %[sws], s80 offen sc1\n\t"
    "s_waitcnt lgkmcnt(0)\n\t"
    "s_nop 7\n\t"
    "v_pk_fma_f32 v[144:145], v[106:107], v[24:25], v[144:145] op_sel_hi:[0,1,1]\n\t"
    "v_pk_fma_f32 v[146:147], v[106:107], v[26:27], v[146:147] op_sel_hi:[0,1,1]\n\t"
    "v_pk_fma_f32 v[148:149], v[106:107], v[28:29], v[148:149] op_sel_hi:[0,1,1]\n\t"
    "v_pk_fma_f32 v[150:151], v[106:107], v[30:31], v[150:151] op_sel_hi:[0,1,1]\n\t"
    "v_pk_fma_f32 v[152:153], v[106:107], v[32:33], v[152:153] op_sel_hi:[0,1,1]\n\t"
    "v_pk_fma_f32 v[154:155], v[106:107], v[34:35], v[154:155] op_sel_hi:[0,1,1]\n\t"
    "v_pk_fma_f32 v[156:157], v[106:107], v[36:37], v[156:157] op_sel_hi:[0,1,1]\n\t"
    "v_pk_fma_f32 v[158:159], v[106:107], v[38:39], v[158:159] op_sel_hi:[0,1,1]\n\t"
    "v_pk_mul_f32 v[144:145], v[104:105], v[144:145] op_sel_hi:[0,1]\n\t"
    "v_pk_mul_f32 v[146:147], v[104:105], v[146:147] op_sel_hi:[0,1]\n\t"
    "v_pk_mul_f32 v[148:149], v[104:105], v[148:149] op_sel_hi:[0,1]\n\t"
    "v_pk_mul_f32 v[150:151], v[104:105], v[150:151] op_sel_hi:[0,1]\n\t"
    "v_pk_mul_f32 v[152:153], v[104:105], v[152:153] op_sel_hi:[0,1]\n\t"
    "v_pk_mul_f32 v[154:155], v[104:105], v[154:155] op_sel_hi:[0,1]\n\t"
    "v_pk_mul_f32 v[156:157], v[104:105], v[156:157] op_sel_hi:[0,1]\n\t"
    "v_pk_mul_f32 v[158:159], v[104:105], v[158:159] op_sel_hi:[0,1]\n\t"
    "v_cvt_pk_bf16_f32 v144, v144, v145\n\t"
    "v_cvt_pk_bf16_f32 v145, v146, v147\n\t"
    "v_cvt_pk_bf16_f32 v146, v148, v149\n\t"
    "v_cvt_pk_bf16_f32 v147, v150, v151\n\t"
    "v_cvt_pk_bf16_f32 v148, v152, v153\n\t"
    "v_cvt_pk_bf16_f32 v149, v154, v155\n\t"
    "v_cvt_pk_bf16_f32 v150, v156, v157\n\t"
    "v_cvt_pk_bf16_f32 v151, v158, v159\n\t"
    "v_pk_max_i16 v144, v144, 0\n\t"
    "v_pk_max_i16 v145, v145, 0\n\t"
    "v_pk_max_i16 v146, v146, 0\n\t"
    "v_pk_max_i16 v147, v147, 0\n\t"
    "v_pk_max_i16 v148, v148, 0\n\t"
    "v_pk_max_i16 v149, v149, 0\n\t"
    "v_pk_max_i16 v150, v150, 0\n\t"
    "v_pk_max_i16 v151, v151, 0\n\t"
    "v_pk_fma_f32 v[160:161], v[106:107], v[40:41], v[160:161] op_sel_hi:[0,1,1]\n\t"
    "v_pk_fma_f32 v[162:163], v[106:107], v[42:43], v[162:163] op_sel_hi:[0,1,1]\n\t"
    "v_pk_fma_f32 v[164:165], v[106:107], v[44:45], v[164:165] op_sel_hi:[0,1,1]\n\t"
    "v_pk_fma_f32 v[166:167], v[106:107], v[46:47], v[166:167] op_sel_hi:[0,1,1]\n\t"
    "v_pk_mul_f32 v[160:161], v[104:105], v[160:161] op_sel_hi:[0,1]\n\t"
    "v_pk_mul_f32 v[162:163], v[104:105], v[162:163] op_sel_hi:[0,1]\n\t"
    "v_pk_mul_f32 v[164:165], v[104:105], v[164:165] op_sel_hi:[0,1]\n\t"
    "v_pk_mul_f32 v[166:167], v[104:105], v[166:167] op_sel_hi:[0,1]\n\t"
    "v_cvt_pk_bf16_f32 v160, v160, v161\n\t"
    "v_cvt_pk_bf16_f32 v161, v162, v163\n\t"
    "v_cvt_pk_bf16_f32 v162, v164, v165\n\t"
    "v_cvt_pk_bf16_f32 v163, v166, v167\n\t"
    "v_pk_max_i16 v160, v160, 0\n\t"
    "v_pk_max_i16 v161, v161, 0\n\t"
    "v_pk_max_i16 v162, v162, 0\n\t"
    "v_pk_max_i16 v163, v163, 0\n\t"
    ASTAMP(21)
    "s_waitcnt vmcnt(0)\n\t"
    "v_cmp_ne_u64 vcc, %[tag], v[248:249]\n\t"
    "s_cbranch_vccz ffn2a_go\n\t"
    "s_mov_b32 s63, 0x40000\n\t"
    "ffn2a_poll:\n\t"
    "buffer_load_dwordx2 v[248:249], v108, %[sws], s80 offen sc1\n\t"
    "s_waitcnt vmcnt(0)\n\t"
    "v_cmp_ne_u64 vcc, %[tag], v[248:249]\n\t"
    "s_cbranch_vccz ffn2a_go\n\t"
    "s_sub_u32 s63, s63, 1\n\t"
    "s_cbranch_scc0 ffn2a_poll\n\t"
    "ffn2a_go:\n\t"
    ASTAMP(23)
    "s_add_u32 s60, s82, 0x0\n\t"
    "buffer_load_dwordx4 v[24:27], v16, %[sws], s60 offen sc1\n\t"
    "s_nop 11\n\t"
    "s_add_u32 s60, s82, 0x400\n\t"
    "buffer_load_dwordx4 v[28:31], v16, %[sws], s60 offen sc1\n\t"
    "s_nop 11\n\t"
    "buffer_store_dwordx4 v[144:147], v16, %[sws], s61 offen offset:0 sc1\n\t"
    "buffer_store_dwordx4 v[148:151], v16, %[sws], s61 offen offset:1024 sc1\n\t"
    "buffer_store_dwordx4 v[160:163], v16, %[sws], s61 offen offset:2048 sc1\n\t"
    "s_add_u32 s61, s79, 0x20000\n\t"
    "s_add_u32 s60, s82, 0x800\n\t"
    "buffer_load_dwordx4 v[32:35], v16, %[sws], s60 offen sc1\n\t"
    "s_nop 11\n\t"
    "s_add_u32 s60, s82, 0xc00\n\t"
    "buffer_load_dwordx4 v[36:39], v16, %[sws], s60 offen sc1\n\t"
    "s_nop 11\n\t"
    "s_add_u32 s60, s82, 0x1c00\n\t"
    "buffer_load_dwordx4 v[40:43], v16, %[sws], s60 offen sc1\n\t"
    "s_nop 11\n\t"
    "s_add_u32 s60, s82, 0x2000\n\t"
    "buffer_load_dwordx4 v[44:47], v16, %[sws], s60 offen sc1\n\t"
    "s_nop 11\n\t"
    "s_add_u32 s60, s82, 0x2400\n\t"
    "buffer_load_dwordx4 v[48:51], v16, %[sws], s60 offen sc1\n\t"
    "s_nop 11\n\t"
    "s_add_u32 s60, s82, 0x2800\n\t"
    "buffer_load_dwordx4 v[52:55], v16, %[sws], s60 offen sc1\n\t"
    "s_nop 11\n\t"
    "s_waitcnt vmcnt(10)\n\t"
    "v_mfma_f32_32x32x16_bf16 v[160:175], a[4:7], v[24:27], 0\n\t"
    "s_add_u32 s60, s82, 0x3800\n\t"
    "buffer_load_dwordx4 v[56:59], v16, %[sws], s60 offen sc1\n\t"
    "v_mfma_f32_32x32x16_bf16 v[144:159], a[0:3], v[24:27], 0\n\t"
    "s_waitcnt vmcnt(10)\n\t"
    "v_mfma_f32_32x32x16_bf16 v[160:175], a[12:15], v[28:31], v[160:175]\n\t"
    "s_add_u32 s60, s82, 0x3c00\n\t"
    "buffer_load_dwordx4 v[60:63], v16, %[sws], s60 offen sc1\n\t"
    "v_mfma_f32_32x32x16_bf16 v[144:159], a[8:11], v[28:31], v[144:159]\n\t"
    "s_waitcnt vmcnt(7)\n\t"
    "v_mfma_f32_32x32x16_bf16 v[160:175], a[20:23], v[32:35], v[160:175]\n\t"
    "s_mov_b64 exec, 1\n\t"
    "buffer_store_dwordx2 v[10:11], off, %[sws], s61 sc1\n\t"
    "s_mov_b64 exec, -1\n\t"
    "s_add_u32 s60, s82, 0x4000\n\t"
    "buffer_load_dwordx4 v[64:67], v16, %[sws], s60 offen sc1\n\t"
    "v_mfma_f32_32x32x16_bf16 v[144:159], a[16:19], v[32:35], v[144:159]\n\t"
    "s_waitcnt vmcnt(8)\n\t"
    "v_mfma_f32_32x32x16_bf16 v[160:175], a[28:31], v[36:39], v[160:175]\n\t"
    "s_add_u32 s60, s82, 0x4400\n\t"
    "buffer_load_dwordx4 v[68:71], v16, %[sws], s60 offen sc1\n\t"
    "v_mfma_f32_32x32x16_bf16 v[144:159], a[24:27], v[36:39], v[144:159]\n\t"
    "s_waitcnt vmcnt(8)\n\t"
    "v_mfma_f32_32x32x16_bf16 v[160:175], a[60:63], v[40:43], v[160:175]\n\t"
    "s_add_u32 s60, s82, 0x5400\n\t"
    "buffer_load_dwordx4 v[72:75], v16, %[sws], s60 offen sc1\n\t"
    "v_mfma_f32_32x32x16_bf16 v[144:159], a[56:59], v[40:43], v[144:159]\n\t"
    "s_waitcnt vmcnt(8)\n\t"
    "v_mfma_f32_32x32x16_bf16 v[160:175], a[68:71], v[44:47], v[160:175]\n\t"
    "s_add_u32 s60, s82, 0x5800\n\t"
    "buffer_load_dwordx4 v[76:79], v16, %[sws], s60 offen sc1\n\t"
    "v_mfma_f32_32x32x16_bf16 v[144:159], a[64:67], v[44:47], v[144:159]\n\t"
    "s_waitcnt vmcnt(8)\n\t"
    "v_mfma_f32_32x32x16_bf16 v[160:175], a[76:79], v[48:51], v[160:175]\n\t"
    "s_add_u32 s60, s82, 0x5c00\n\t"
    "buffer_load_dwordx4 v[80:83], v16, %[sws], s60 offen sc1\n\t"
    "v_mfma_f32_32x32x16_bf16 v[144:159], a[72:75], v[48:51], v[144:159]\n\t"
    "s_waitcnt vmcnt(8)\n\t"
    "v_mfma_f32_32x32x16_bf16 v[160:175], a[84:87], v[52:55], v[160:175]\n\t"
    "s_add_u32 s60, s82, 0x6000\n\t"
    "buffer_load_dwordx4 v[84:87], v16, %[sws], s60 offen sc1\n\t"
    "v_mfma_f32_32x32x16_bf16 v[144:159], a[80:83], v[52:55], v[144:159]\n\t"
    "s_waitcnt vmcnt(8)\n\t"
    "v_mfma_f32_32x32x16_bf16 v[160:175], a[116:119], v[56:59], v[160:175]\n\t"
    "s_add_u32 s60, s81, 0x0\n\t"
    "buffer_load_dwordx4 v[176:179], v16, %[sws], s60 offen sc1\n\t"
    "v_mfma_f32_32x32x16_bf16 v[144:159], a[112:115], v[56:59], v[144:159]\n\t"
    "s_waitcnt vmcnt(8)\n\t"
    "v_mfma_f32_32x32x16_bf16 v[160:175], a[124:127], v[60:63], v[160:175]\n\t"
    "s_add_u32 s60, s81, 0x400\n\t"
    "buffer_load_dwordx4 v[180:183], v16, %[sws], s60 offen sc1\n\t"
    "v_mfma_f32_32x32x16_bf16 v[144:159], a[120:123], v[60:63], v[144:159]\n\t"
    "s_waitcnt vmcnt(7)\n\t"
    "v_mfma_f32_32x32x16_bf16 v[160:175], a[132:135], v[64:67], v[160:175]\n\t"
    "s_add_u32 s60, s81, 0x800\n\t"
    "buffer_load_dwordx4 v[184:187], v16, %[sws], s60 offen sc1\n\t"
    "v_mfma_f32_32x32x16_bf16 v[144:159], a[128:131], v[64:67], v[144:159]\n\t"
    "s_waitcnt vmcnt(7)\n\t"
    "v_mfma_f32_32x32x16_bf16 v[160:175], a[140:143], v[68:71], v[160:175]\n\t"
    "s_add_u32 s60, s81, 0xc00\n\t"
    "buffer_load_dwordx4 v[188:191], v16, %[sws], s60 offen sc1\n\t"
    "v_mfma_f32_32x32x16_bf16 v[144:159], a[136:139], v[68:71], v[144:159]\n\t"
    "s_waitcnt vmcnt(7)\n\t"
    "v_mfma_f32_32x32x16_bf16 v[160:175], a[172:175], v[72:75], v[160:175]\n\t"
    "s_add_u32 s60, s81, 0x1c00\n\t"
    "buffer_load_dwordx4 v[192:195], v16, %[sws], s60 offen sc1\n\t"
    "v_mfma_f32_32x32x16_bf16 v[144:159], a[168:171], v[72:75], v[144:159]\n\t"
    "s_waitcnt vmcnt(7)\n\t"
    "v_mfma_f32_32x32x16_bf16 v[160:175], a[180:183], v[76:79], v[160:175]\n\t"
    "s_add_u32 s60, s81, 0x2000\n\t"
    "buffer_load_dwordx4 v[196:199], v16, %[sws], s60 offen sc1\n\t"
    "v_mfma_f32_32x32x16_bf16 v[144:159], a[176:179], v[76:79], v[144:159]\n\t"
    "s_waitcnt vmcnt(7)\n\t"
    "v_mfma_f32_32x32x16_bf16 v[160:175], a[188:191], v[80:83], v[160:175]\n\t"
    "s_add_u32 s60, s81, 0x2400\n\t"
    "buffer_load_dwordx4 v[200:203], v16, %[sws], s60 offen sc1\n\t"
    "v_mfma_f32_32x32x16_bf16 v[144:159], a[184:187], v[80:83], v[144:159]\n\t"
    "s_waitcnt vmcnt(7)\n\t"
    "v_mfma_f32_32x32x16_bf16 v[160:175], a[196:199], v[84:87], v[160:175]\n\t"
    "s_add_u32 s60, s81, 0x2800\n\t"
    "buffer_load_dwordx4 v[204:207], v16, %[sws], s60 offen sc1\n\t"
    "v_mfma_f32_32x32x16_bf16 v[144:159], a[192:195], v[84:87], v[144:159]\n\t"
    "s_waitcnt vmcnt(7)\n\t"
    "v_mfma_f32_32x32x16_bf16 v[128:143], a[4:7], v[176:179], 0\n\t"
    "s_add_u32 s60, s81, 0x3800\n\t"
    "buffer_load_dwordx4 v[24:27], v16, %[sws], s60 offen sc1\n\t"
    "buffer_load_dwordx2 v[248:249], v108, %[sws], s62 offen sc1\n\t"
    "v_mfma_f32_32x32x16_bf16 v[112:127], a[0:3], v[176:179], v[112:127]\n\t"
    "s_waitcnt vmcnt(8)\n\t"
    "v_mfma_f32_32x32x16_bf16 v[128:143], a[12:15], v[180:183], v[128:143]\n\t"
    "s_add_u32 s60, s81, 0x3c00\n\t"
    "buffer_load_dwordx4 v[28:31], v16, %[sws], s60 offen sc1\n\t"
    "v_mfma_f32_32x32x16_bf16 v[112:127], a[8:11], v[180:183], v[112:127]\n\t"
    "s_waitcnt vmcnt(8)\n\t"
    "v_mfma_f32_32x32x16_bf16 v[128:143], a[20:23], v[184:187], v[128:143]\n\t"
    "s_add_u32 s60, s81, 0x4000\n\t"
    "buffer_load_dwordx4 v[32:35], v16, %[sws], s60 offen sc1\n\t"
    "v_mfma_f32_32x32x16_bf16 v[112:127], a[16:19], v[184:187], v[112:127]\n\t"
    "s_waitcnt vmcnt(8)\n\t"
    "v_mfma_f32_32x32x16_bf16 v[128:143], a[28:31], v[188:191], v[128:143]\n\t"
    "s_add_u32 s60, s81, 0x4400\n\t"
    "buffer_load_dwordx4 v[36:39], v16, %[sws], s60 offen sc1\n\t"
    "v_mfma_f32_32x32x16_bf16 v[112:127], a[24:27], v[188:191], v[112:127]\n\t"
    "s_waitcnt vmcnt(8)\n\t"
    "v_mfma_f32_32x32x16_bf16 v[128:143], a[60:63], v[192:195], v[128:143]\n\t"
    "s_add_u32 s60, s81, 0x5400\n\t"
    "buffer_load_dwordx4 v[40:43], v16, %[sws], s60 offen sc1\n\t"
    "v_mfma_f32_32x32x16_bf16 v[112:127], a[56:59], v[192:195], v[112:127]\n\t"
    ASTAMP(28)
    "s_waitcnt vmcnt(8)\n\t"
    "v_mfma_f32_32x32x16_bf16 v[128:143], a[68:71], v[196:199], v[128:143]\n\t"
    "s_add_u32 s60, s81, 0x5800\n\t"
    "buffer_load_dwordx4 v[44:47], v16, %[sws], s60 offen sc1\n\t"
    "v_mfma_f32_32x32x16_bf16 v[112:127], a[64:67], v[196:199], v[112:127]\n\t"
    "s_waitcnt vmcnt(8)\n\t"
    "v_mfma_f32_32x32x16_bf16 v[128:143], a[76:79], v[200:203], v[128:143]\n\t"
    "s_add_u32 s60, s81, 0x5c00\n\t"
    "buffer_load_dwordx4 v[48:51], v16, %[sws], s60 offen sc1\n\t"
    "v_mfma_f32_32x32x16_bf16 v[112:127], a[72:75], v[200:203], v[112:127]\n\t"
    "s_waitcnt vmcnt(8)\n\t"
    "v_mfma_f32_32x32x16_bf16 v[128:143], a[84:87], v[204:207], v[128:143]\n\t"
    "s_add_u32 s60, s81, 0x6000\n\t"
    "buffer_load_dwordx4 v[52:55], v16, %[sws], s60 offen sc1\n\t"
    "v_mfma_f32_32x32x16_bf16 v[112:127], a[80:83], v[204:207], v[112:127]\n\t"
    "s_waitcnt vmcnt(8)\n\t"
    "v_mfma_f32_32x32x16_bf16 v[128:143], a[116:119], v[24:27], v[128:143]\n\t"
    "s_waitcnt vmcnt(7)\n\t"
    "v_cmp_ne_u64 vcc, %[tag], v[248:249]\n\t"
    "s_cbranch_vccz ffn2b_go\n\t"
    "s_mov_b32 s63, 0x40000\n\t"
    "ffn2b_poll:\n\t"
    "buffer_load_dwordx2 v[248:249], v108, %[sws], s62 offen sc1\n\t"
    "s_waitcnt vmcnt(0)\n\t"
    "v_cmp_ne_u64 vcc, %[tag], v[248:249]\n\t"
    "s_cbranch_vccz ffn2b_go\n\t"
    "s_sub_u32 s63, s63, 1\n\t"
    "s_cbranch_scc0 ffn2b_poll\n\t"
    "ffn2b_go:\n\t"
    "s_add_u32 s60, s82, 0x1000\n\t"
    "buffer_load_dwordx4 v[56:59], v16, %[sws], s60 offen sc1\n\t"
    "v_mfma_f32_32x32x16_bf16 v[112:127], a[112:115], v[24:27], v[112:127]\n\t"
    "s_waitcnt vmcnt(7)\n\t"
    "v_mfma_f32_32x32x16_bf16 v[128:143], a[124:127], v[28:31], v[128:143]\n\t"
    "s_add_u32 s60, s82, 0x1400\n\t"
    "buffer_load_dwordx4 v[60:63], v16, %[sws], s60 offen sc1\n\t"
    "v_mfma_f32_32x32x16_bf16 v[112:127], a[120:123], v[28:31], v[112:127]\n\t"
    "s_waitcnt vmcnt(7)\n\t"
    "v_mfma_f32_32x32x16_bf16 v[128:143], a[132:135], v[32:35], v[128:143]\n\t"
    "s_add_u32 s60, s82, 0x1800\n\t"
    "buffer_load_dwordx4 v[64:67], v16, %[sws], s60 offen sc1\n\t"
    "v_mfma_f32_32x32x16_bf16 v[112:127], a[128:131], v[32:35], v[112:127]\n\t"
    "s_waitcnt vmcnt(7)\n\t"
    "v_mfma_f32_32x32x16_bf16 v[128:143], a[140:143], v[36:39], v[128:143]\n\t"
    "s_add_u32 s60, s82, 0x2c00\n\t"
    "buffer_load_dwordx4 v[68:71], v16, %[sws], s60 offen sc1\n\t"
    "v_mfma_f32_32x32x16_bf16 v[112:127], a[136:139], v[36:39], v[112:127]\n\t"
    "s_waitcnt vmcnt(7)\n\t"
    "v_mfma_f32_32x32x16_bf16 v[128:143], a[172:175], v[40:43], v[128:143]\n\t"
    "s_add_u32 s60, s82, 0x3000\n\t"
    "buffer_load_dwordx4 v[72:75], v16, %[sws], s60 offen sc1\n\t"
    "v_mfma_f32_32x32x16_bf16 v[112:127], a[168:171], v[40:43], v[112:127]\n\t"
    "s_waitcnt vmcnt(7)\n\t"
    "v_mfma_f32_32x32x16_bf16 v[128:143], a[180:183], v[44:47], v[128:143]\n\t"
    "s_add_u32 s60, s82, 0x3400\n\t"
    "buffer_load_dwordx4 v[76:79], v16, %[sws], s60 offen sc1\n\t"
    "v_mfma_f32_32x32x16_bf16 v[112:127], a[176:179], v[44:47], v[112:127]\n\t"
    "s_waitcnt vmcnt(7)\n\t"
    "v_mfma_f32_32x32x16_bf16 v[128:143], a[188:191], v[48:51], v[128:143]\n\t"
    "s_add_u32 s60, s82, 0x4800\n\t"
    "buffer_load_dwordx4 v[80:83], v16, %[sws], s60 offen sc1\n\t"
    "v_mfma_f32_32x32x16_bf16 v[112:127], a[184:187], v[48:51], v[112:127]\n\t"
    "s_waitcnt vmcnt(7)\n\t"
    "v_mfma_f32_32x32x16_bf16 v[128:143], a[196:199], v[52:55], v[128:143]\n\t"
    "s_add_u32 s60, s82, 0x4c00\n\t"
    "buffer_load_dwordx4 v[84:87], v16, %[sws], s60 offen sc1\n\t"
    "v_mfma_f32_32x32x16_bf16 v[112:127], a[192:195], v[52:55], v[112:127]\n\t"
    ASTAMP(29)
    "s_waitcnt vmcnt(7)\n\t"
    "v_mfma_f32_32x32x16_bf16 v[160:175], a[36:39], v[56:59], v[160:175]\n\t"
    "s_add_u32 s60, s82, 0x5000\n\t"
    "buffer_load_dwordx4 v[176:179], v16, %[sws], s60 offen sc1\n\t"
    "v_mfma_f32_32x32x16_bf16 v[144:159], a[32:35], v[56:59], v[144:159]\n\t"
    "s_waitcnt vmcnt(7)\n\t"
    "v_mfma_f32_32x32x16_bf16 v[160:175], a[44:47], v[60:63], v[160:175]\n\t"
    "s_add_u32 s60, s82, 0x6400\n\t"
    "buffer_load_dwordx4 v[180:183], v16, %[sws], s60 offen sc1\n\t"
    "v_mfma_f32_32x32x16_bf16 v[144:159], a[40:43], v[60:63], v[144:159]\n\t"
    "s_waitcnt vmcnt(7)\n\t"
    "v_mfma_f32_32x32x16_bf16 v[160:175], a[52:55], v[64:67], v[160:175]\n\t"
    "s_add_u32 s60, s82, 0x6800\n\t"
    "buffer_load_dwordx4 v[184:187], v16, %[sws], s60 offen sc1\n\t"
    "v_mfma_f32_32x32x16_bf16 v[144:159], a[48:51], v[64:67], v[144:159]\n\t"
    "s_waitcnt vmcnt(7)\n\t"
    "v_mfma_f32_32x32x16_bf16 v[160:175], a[92:95], v[68:71], v[160:175]\n\t"
    "s_add_u32 s60, s82, 0x6c00\n\t"
    "buffer_load_dwordx4 v[188:191], v16, %[sws], s60 offen sc1\n\t"
    "v_mfma_f32_32x32x16_bf16 v[144:159], a[88:91], v[68:71], v[144:159]\n\t"
    "s_waitcnt vmcnt(7)\n\t"
    "v_mfma_f32_32x32x16_bf16 v[160:175], a[100:103], v[72:75], v[160:175]\n\t"
    "s_add_u32 s60, s81, 0x1000\n\t"
    "buffer_load_dwordx4 v[192:195], v16, %[sws], s60 offen sc1\n\t"
    "v_mfma_f32_32x32x16_bf16 v[144:159], a[96:99], v[72:75], v[144:159]\n\t"
    "s_waitcnt vmcnt(7)\n\t"
    "v_mfma_f32_32x32x16_bf16 v[160:175], a[108:111], v[76:79], v[160:175]\n\t"
    "s_add_u32 s60, s81, 0x1400\n\t"
    "buffer_load_dwordx4 v[196:199], v16, %[sws], s60 offen sc1\n\t"
    "v_mfma_f32_32x32x16_bf16 v[144:159], a[104:107], v[76:79], v[144:159]\n\t"
    "s_waitcnt vmcnt(7)\n\t"
    "v_mfma_f32_32x32x16_bf16 v[160:175], a[148:151], v[80:83], v[160:175]\n\t"
    "s_add_u32 s60, s81, 0x1800\n\t"
    "buffer_load_dwordx4 v[200:203], v16, %[sws], s60 offen sc1\n\t"
    "v_mfma_f32_32x32x16_bf16 v[144:159], a[144:147], v[80:83], v[144:159]\n\t"
    "s_waitcnt vmcnt(7)\n\t"
    "v_mfma_f32_32x32x16_bf16 v[160:175], a[156:159], v[84:87], v[160:175]\n\t"
    "s_add_u32 s60, s81, 0x2c00\n\t"
    "buffer_load_dwordx4 v[204:207], v16, %[sws], s60 offen sc1\n\t"
    "v_mfma_f32_32x32x16_bf16 v[144:159], a[152:155], v[84:87], v[144:159]\n\t"
    "s_waitcnt vmcnt(7)\n\t"
    "v_mfma_f32_32x32x16_bf16 v[160:175], a[164:167], v[176:179], v[160:175]\n\t"
    "s_add_u32 s60, s81, 0x3000\n\t"
    "buffer_load_dwordx4 v[24:27], v16, %[sws], s60 offen sc1\n\t"
    "v_mfma_f32_32x32x16_bf16 v[144:159], a[160:163], v[176:179], v[144:159]\n\t"
    "s_waitcnt vmcnt(7)\n\t"
    "v_mfma_f32_32x32x16_bf16 v[160:175], a[204:207], v[180:183], v[160:175]\n\t"
    "s_add_u32 s60, s81, 0x3400\n\t"
    "buffer_load_dwordx4 v[28:31], v16, %[sws], s60 offen sc1\n\t"
    "v_mfma_f32_32x32x16_bf16 v[144:159], a[200:203], v[180:183], v[144:159]\n\t"
    "s_waitcnt vmcnt(7)\n\t"
    "v_mfma_f32_32x32x16_bf16 v[160:175], a[212:215], v[184:187], v[160:175]\n\t"
    "s_add_u32 s60, s81, 0x4800\n\t"
    "buffer_load_dwordx4 v[32:35], v16, %[sws], s60 offen sc1\n\t"
    "v_mfma_f32_32x32x16_bf16 v[144:159], a[208:211], v[184:187], v[144:159]\n\t"
    "s_waitcnt vmcnt(7)\n\t"
    "v_mfma_f32_32x32x16_bf16 v[160:175], a[220:223], v[188:191], v[160:175]\n\t"
    "s_add_u32 s60, s81, 0x4c00\n\t"
    "buffer_load_dwordx4 v[36:39], v16, %[sws], s60 offen sc1\n\t"
    "v_mfma_f32_32x32x16_bf16 v[144:159], a[216:219], v[188:191], v[144:159]\n\t"
    "s_waitcnt vmcnt(7)\n\t"
    "v_mfma_f32_32x32x16_bf16 v[128:143], a[36:39], v[192:195], v[128:143]\n\t"
    "s_add_u32 s60, s81, 0x5000\n\t"
    "buffer_load_dwordx4 v[40:43], v16, %[sws], s60 offen sc1\n\t"
    "v_mfma_f32_32x32x16_bf16 v[112:127], a[32:35], v[192:195], v[112:127]\n\t"
    ASTAMP(30)
    "s_barrier\n\t"
    "s_waitcnt vmcnt(7)\n\t"
    "v_mfma_f32_32x32x16_bf16 v[128:143], a[44:47], v[196:199], v[128:143]\n\t"
    "s_add_u32 s60, s81, 0x6400\n\t"
    "buffer_load_dwordx4 v[44:47], v16, %[sws], s60 offen sc1\n\t"
    "v_mfma_f32_32x32x16_bf16 v[112:127], a[40:43], v[196:199], v[112:127]\n\t"
    "s_waitcnt vmcnt(7)\n\t"
    "v_mfma_f32_32x32x16_bf16 v[128:143], a[52:55], v[200:203], v[128:143]\n\t"
    "s_add_u32 s60, s81, 0x6800\n\t"
    "buffer_load_dwordx4 v[48:51], v16, %[sws], s60 offen sc1\n\t"
    "v_mfma_f32_32x32x16_bf16 v[112:127], a[48:51], v[200:203], v[112:127]\n\t"
    "s_waitcnt vmcnt(7)\n\t"
    "v_mfma_f32_32x32x16_bf16 v[128:143], a[92:95], v[204:207], v[128:143]\n\t"
    "s_add_u32 s60, s81, 0x6c00\n\t"
    "buffer_load_dwordx4 v[52:55], v16, %[sws], s60 offen sc1\n\t"
    "v_mfma_f32_32x32x16_bf16 v[112:127], a[88:91], v[204:207], v[112:127]\n\t"
    "ds_write_b128 v107, v[144:147] offset:4096\n\t"
    "s_waitcnt vmcnt(7)\n\t"
    "v_mfma_f32_32x32x16_bf16 v[128:143], a[100:103], v[24:27], v[128:143]\n\t"
    "v_mfma_f32_32x32x16_bf16 v[112:127], a[96:99], v[24:27], v[112:127]\n\t"
    "ds_write_b128 v107, v[148:151] offset:5120\n\t"
    "s_waitcnt vmcnt(6)\n\t"
    "v_mfma_f32_32x32x16_bf16 v[128:143], a[108:111], v[28:31], v[128:143]\n\t"
    "v_mfma_f32_32x32x16_bf16 v[112:127], a[104:107], v[28:31], v[112:127]\n\t"
    "ds_write_b128 v107, v[152:155] offset:6144\n\t"
    "s_waitcnt vmcnt(5)\n\t"
    "v_mfma_f32_32x32x16_bf16 v[128:143], a[148:151], v[32:35], v[128:143]\n\t"
    "ds_write_b128 v107, v[156:159] offset:7168\n\t"
    "s_waitcnt vmcnt(4)\n\t"
    "v_mfma_f32_32x32x16_bf16 v[128:143], a[156:159], v[36:39], v[128:143]\n\t"
    "ds_write_b128 v109, v[160:163] offset:8192\n\t"
    "s_waitcnt vmcnt(3)\n\t"
    "v_mfma_f32_32x32x16_bf16 v[128:143], a[164:167], v[40:43], v[128:143]\n\t"
    "ds_write_b128 v109, v[164:167] offset:9216\n\t"
    "s_waitcnt vmcnt(2)\n\t"
    "v_mfma_f32_32x32x16_bf16 v[128:143], a[204:207], v[44:47], v[128:143]\n\t"
    "ds_write_b128 v109, v[168:171] offset:10240\n\t"
    "s_waitcnt vmcnt(1)\n\t"
    "v_mfma_f32_32x32x16_bf16 v[128:143], a[212:215], v[48:51], v[128:143]\n\t"
    "ds_write_b128 v109, v[172:175] offset:11264\n\t"
    "s_waitcnt vmcnt(0)\n\t"
    "v_mfma_f32_32x32x16_bf16 v[128:143], a[220:223], v[52:55], v[128:143]\n\t"
    "v_mfma_f32_32x32x16_bf16 v[112:127], a[144:147], v[32:35], v[112:127]\n\t"
    "v_mfma_f32_32x32x16_bf16 v[112:127], a[152:155], v[36:39], v[112:127]\n\t"
    "s_nop 9\n\t"
    "ds_write_b128 v105, v[128:131] offset:0\n\t"
    "v_mfma_f32_32x32x16_bf16 v[112:127], a[160:163], v[40:43], v[112:127]\n\t"
    "ds_write_b128 v105, v[132:135] offset:1024\n\t"
    "v_mfma_f32_32x32x16_bf16 v[112:127], a[200:203], v[44:47], v[112:127]\n\t"
    "ds_write_b128 v105, v[136:139] offset:2048\n\t"
    "v_mfma_f32_32x32x16_bf16 v[112:127], a[208:211], v[48:51], v[112:127]\n\t"
    "ds_write_b128 v105, v[140:143] offset:3072\n\t"
    "v_mfma_f32_32x32x16_bf16 v[112:127], a[216:219], v[52:55], v[112:127]\n\t"
    ASTAMP(24)
    "s_waitcnt lgkmcnt(0)\n\t"
    "s_barrier\n\t"
    "ds_read_b128 v[24:27], v21 offset:0\n\t"
    "ds_read_b128 v[28:31], v21 offset:4096\n\t"
    "ds_read_b128 v[32:35], v21 offset:8192\n\t"
    "ds_read_b128 v[36:39], v21 offset:1024\n\t"
    "ds_read_b128 v[40:43], v21 offset:5120\n\t"
    "ds_read_b128 v[44:47], v21 offset:9216\n\t"
    "ds_read_b128 v[48:51], v21 offset:2048\n\t"
    "ds_read_b128 v[52:55], v21 offset:6144\n\t"
    "ds_read_b128 v[56:59], v21 offset:10240\n\t"
    "ds_read_b128 v[60:63], v21 offset:3072\n\t"
    "ds_read_b128 v[64:67], v21 offset:7168\n\t"
    "ds_read_b128 v[68:71], v21 offset:11264\n\t"
    "s_waitcnt lgkmcnt(9)\n\t"
    "v_pk_add_f32 v[24:25], v[24:25], v[28:29]\n\t"
    "v_pk_add_f32 v[112:113], v[112:113], v[32:33]\n\t"
    "v_pk_add_f32 v[26:27], v[26:27], v[30:31]\n\t"
    "v_pk_add_f32 v[114:115], v[114:115], v[34:35]\n\t"
    "v_pk_add_f32 v[112:113], v[112:113], v[24:25]\n\t"
    "v_pk_add_f32 v[114:115], v[114:115], v[26:27]\n\t"
    "v_cvt_pk_bf16_f32 v240, v112, v113\n\t"
    "v_cvt_pk_bf16_f32 v241, v114, v115\n\t"
    "s_waitcnt lgkmcnt(6)\n\t"
    "v_pk_add_f32 v[36:37], v[36:37], v[40:41]\n\t"
    "v_pk_add_f32 v[116:117], v[116:117], v[44:45]\n\t"
    "v_pk_add_f32 v[38:39], v[38:39], v[42:43]\n\t"
    "v_pk_add_f32 v[118:119], v[118:119], v[46:47]\n\t"
    "v_pk_add_f32 v[116:117], v[116:117], v[36:37]\n\t"
    "v_pk_add_f32 v[118:119], v[118:119], v[38:39]\n\t"
    "v_cvt_pk_bf16_f32 v242, v116, v117\n\t"
    "v_cvt_pk_bf16_f32 v243, v118, v119\n\t"
    "s_nop 1\n\t"
    "v_permlane32_swap_b32 v240, v242\n\t"
    "v_permlane32_swap_b32 v241, v243\n\t"
    "buffer_store_dwordx4 v[240:243], v18, %[sout], 0 offen offset:0\n\t"
    "s_waitcnt lgkmcnt(3)\n\t"
    "v_pk_add_f32 v[48:49], v[48:49], v[52:53]\n\t"
    "v_pk_add_f32 v[120:121], v[120:121], v[56:57]\n\t"
    "v_pk_add_f32 v[50:51], v[50:51], v[54:55]\n\t"
    "v_pk_add_f32 v[122:123], v[122:123], v[58:59]\n\t"
    "v_pk_add_f32 v[120:121], v[120:121], v[48:49]\n\t"
    "v_pk_add_f32 v[122:123], v[122:123], v[50:51]\n\t"
    "v_cvt_pk_bf16_f32 v244, v120, v121\n\t"
    "v_cvt_pk_bf16_f32 v245, v122, v123\n\t"
    "s_waitcnt lgkmcnt(0)\n\t"
    "v_pk_add_f32 v[60:61], v[60:61], v[64:65]\n\t"
    "v_pk_add_f32 v[124:125], v[124:125], v[68:69]\n\t"
    "v_pk_add_f32 v[62:63], v[62:63], v[66:67]\n\t"
    "v_pk_add_f32 v[126:127], v[126:127], v[70:71]\n\t"
    "v_pk_add_f32 v[124:125], v[124:125], v[60:61]\n\t"
    "v_pk_add_f32 v[126:127], v[126:127], v[62:63]\n\t"
    "v_cvt_pk_bf16_f32 v246, v124, v125\n\t"
    "v_cvt_pk_bf16_f32 v247, v126, v127\n\t"
    "s_nop 1\n\t"
    "v_permlane32_swap_b32 v244, v246\n\t"
    "v_permlane32_swap_b32 v245, v247\n\t"
    "buffer_store_dwordx4 v[244:247], v18, %[sout], 0 offen offset:32\n\t"
    "s_cmp_eq_u32 s87, 0\n\t"
    "s_cbranch_scc1 epoch_done\n\t"
    "s_mov_b64 exec, 1\n\t"
    "buffer_store_dwordx2 v[10:11], off, %[sws], s87 sc1\n\t"
    "s_mov_b64 exec, -1\n\t"
    "epoch_done:\n\t"
            : "+{v[176:207]}"(woA), "+{v[208:239]}"(woB), "+{v[72:79]}"(xres), "+{v[104:107]}"(la), "+{v[108:109]}"(ix) ASTAMP_OPS
            : "{v[12:15]}"(tr), "{v[16:23]}"(va), "{s[68:83]}"(sp), "{s[84:87]}"(sq), [tag] "s"(tag), [tagl] "s"((unsigned)tag),
              [tagh] "s"((unsigned)(tag >> 32)), [sws] "s"(sws), [sout] "s"(sout), [ct1k] "s"(ct * 1024), [ct42k] "s"(ct4 * 2048),
              [a5] "s"(WS_A + (rt64 * 224 + (ct >> 2) * 112 + wv * 28 + (ct & 3) * 7) * 1024)
            : "v2", "v3", "v4", "v5", "v6", "v7", "v8", "v9", "v10", "v11", "v24", "v25", "v26", "v27", "v28", "v29", "v30", "v31", "v32", "v33", "v34", "v35", "v36", "v37", "v38", "v39", "v40", "v41", "v42", "v43", "v44", "v45", "v46", "v47", "v48", "v49", "v50", "v51", "v52", "v53", "v54", "v55", "v56", "v57", "v58", "v59", "v60", "v61", "v62", "v63", "v64", "v65", "v66", "v67", "v68", "v69", "v70", "v71", "v80", "v81", "v82", "v83", "v84", "v85", "v86", "v87", "v88", "v89", "v90", "v91", "v92", "v93", "v94", "v95", "v96", "v97", "v98", "v99", "v100", "v101", "v102", "v103", "v112", "v113", "v114", "v115", "v116", "v117", "v118", "v119", "v120", "v121", "v122", "v123", "v124", "v125", "v126", "v127", "v128", "v129", "v130", "v131", "v132", "v133", "v134", "v135", "v136", "v137", "v138", "v139", "v140", "v141", "v142", "v143", "v144", "v145", "v146", "v147", "v148", "v149", "v150", "v151", "v152", "v153", "v154", "v155", "v156", "v157", "v158", "v159", "v160", "v161", "v162", "v163", "v164", "v165", "v166", "v167", "v168", "v169", "v170", "v171", "v172", "v173", "v174", "v175", "v240", "v241", "v242", "v243", "v244", "v245", "v246", "v247", "v248", "v249", "v250", "v251", "v252", "v253", "v254", "v255", "s60", "s61", "s62", "s63", "vcc", "scc", "memory" ASTAMP_CLOB);
        STAMP(40);
        STAMP_END();
        asm volatile("fused_block_end:");
    }
}

extern "C" void kernel_launch(void* const* d_in, const int* in_sizes, int n_in, void* d_out, int out_size, void* d_ws, size_t ws_size,
                              hipStream_t stream) {
    fused_block<<<256, 256, 0, stream>>>((const char*)d_in[0], (const char*)d_in[1], (const char*)d_in[2], (const char*)d_in[4],
                                         (const char*)d_in[5], (char*)d_ws, (char*)d_out);
}
```
